```python
import torch

def _softmax(x, axis=-1):
    return torch.softmax(x, dim=axis)
def _gelu(x):
    return torch.nn.functional.gelu(x, approximate="tanh")
def _silu(x):
    return torch.nn.functional.silu(x)
def _sigmoid(x):
    return torch.sigmoid(x)
def _layernorm(x, axis=-1, eps=1e-5):
    m = x.mean(dim=axis, keepdim=True)
    v = x.var(dim=axis, keepdim=True, unbiased=False)
    return (x - m) / torch.sqrt(v + eps)
def _scatter_add(tgt, idx, src, axis=0):
    return tgt.clone().index_add_(axis, idx, src)
def _segment_sum(src, seg, n):
    out = torch.zeros((n,) + src.shape[1:], dtype=src.dtype)
    return out.index_add_(0, seg, src)
def _pad(x, pw):
    flat = tuple(v for pair in reversed(pw) for v in pair)
    return torch.nn.functional.pad(x, flat)

kernel_name = 'standalone__mha_block_cached__near_ridge'

def setup_inputs():

    _w = (lambda a: (lambda t: t.to(torch.bfloat16) if t.is_floating_point() else t)(torch.as_tensor(a)))
    B, Sq, D = 16, 160, 448
    H, Sk, Dh = 16, 512, 256
    return {
        "x": _w((lambda *s: torch.randn(*s, dtype=torch.bfloat16) * 0.8)(B, Sq, D)),
        "k_cache": _w((lambda *s: torch.randn(*s, dtype=torch.bfloat16) * 0.8)(B, H, Sk, Dh)),
        "v_cache": _w((lambda *s: torch.randn(*s, dtype=torch.bfloat16) * 0.05)(B, H, Sk, Dh)),
        "wq": _w((lambda *s: torch.randn(*s, dtype=torch.bfloat16) * 0.8)(D, H * Dh) * (D ** -0.5)),
        "wo": _w((lambda *s: torch.randn(*s, dtype=torch.bfloat16) * 0.05)(H * Dh, D) * ((H * Dh) ** -0.5)),
    }

def reference(x, k_cache, v_cache, wq, wo):
    B, Sq, D = x.shape
    H, Dh = 16, 256
    q = (lambda x, *d: x.permute(*d))((x @ wq).reshape(B, Sq, H, Dh), 0, 2, 1, 3)
    scores = torch.einsum("bhqd,bhkd->bhqk", q, k_cache) * (Dh ** -0.5)
    p = _softmax(scores, axis=-1)
    o = torch.einsum("bhqk,bhkd->bhqd", p, v_cache)
    return (lambda x, *d: x.permute(*d))(o, 0, 2, 1, 3).reshape(B, Sq, H * Dh) @ wo
```

```cpp
#include <hip/hip_runtime.h>
#include <hip/hip_bf16.h>
typedef unsigned u32x4 __attribute__((ext_vector_type(4)));
typedef unsigned u32x2 __attribute__((ext_vector_type(2)));
typedef float f32x2 __attribute__((ext_vector_type(2)));
typedef __bf16 bf16x2 __attribute__((ext_vector_type(2)));

__global__ __launch_bounds__(256, 1) __attribute__((aligned(4096))) void mha_fused(const char* __restrict__ x, const char* __restrict__ k, const char* __restrict__ v,
                                                    const char* __restrict__ wq, const char* __restrict__ wo, char* __restrict__ ws) {
  __shared__ __attribute__((aligned(1024))) char lds[163840];
  const unsigned bid = blockIdx.x, xg = bid & 7u, r = bid >> 3;
  const unsigned h = 4u * (xg & 3u) + (r & 3u), b = 8u * (xg >> 2) + (r >> 2), bh = b * 16u + h;
  const unsigned w = __builtin_amdgcn_readfirstlane(threadIdx.x >> 6);
  const char* px = x + (size_t)b * 143360u;
  const char* pk = k + (size_t)bh * 262144u;
  const char* pv = v + (size_t)bh * 262144u;
  const char* pq = wq + h * 512u;
  const char* po = wo + (size_t)h * 229376u;
  char* pp = ws + (size_t)bh * 143360u;
  char* ps = ws + (size_t)256 * 143360u + bid * 512u;
  asm volatile(
"s_mov_b32 s44, %6\n"
"v_mbcnt_lo_u32_b32 v211, -1, 0\n"
"v_mbcnt_hi_u32_b32 v211, -1, v211\n"
"s_mov_b64 s[16:17], %0\n"
"s_mov_b32 s18, 0x23000\n"
"s_mov_b32 s19, 0x20000\n"
"s_mov_b64 s[20:21], %3\n"
"s_mov_b32 s22, 0x37e200\n"
"s_mov_b32 s23, 0x20000\n"
"v_lshrrev_b32 v244, 3, v211\n"
"v_lshrrev_b32 v245, 4, v211\n"
"v_and_b32 v246, 7, v211\n"
"v_and_b32 v247, 1, v245\n"
"v_lshlrev_b32 v247, 2, v247\n"
"v_xor_b32 v247, v247, v246\n"
"v_lshlrev_b32 v247, 4, v247\n"
"s_and_b32 s45, s44, 1\n"
"s_lshl_b32 s45, s45, 2\n"
"v_add_u32 v248, s45, v245\n"
"v_xor_b32 v248, v248, v246\n"
"v_lshlrev_b32 v248, 4, v248\n"
"s_lshl_b32 s45, s44, 3\n"
"v_add_u32 v249, s45, v244\n"
"v_mul_u32_u24 v249, 0x380, v249\n"
"v_add_u32 v208, v248, v249\n"
"s_lshl_b32 s41, s44, 10\n"
"s_mul_i32 s40, s44, 0x4c00\n"
"s_add_u32 s40, s40, 0x14000\n"
"s_lshl_b32 s46, s44, 7\n"
"v_lshrrev_b32 v248, 5, v211\n"
"v_and_b32 v249, 3, v244\n"
"v_lshl_add_u32 v248, v248, 4, v249\n"
"v_lshl_add_u32 v248, v248, 13, v247\n"
"v_add_u32 v209, s46, v248\n"
"v_add_u32 v210, 0xfbe0, v209\n"
"s_lshl_b32 s72, %10, 7\n"
"s_lshl_b32 s84, s72, 12\n"
"s_getpc_b64 s[94:95]\n"
".Lmha_pc%=:\n"
"s_lshl_b32 s45, %7, 11\n"
"v_and_b32 v248, 15, v211\n"
"v_lshl_add_u32 v248, v248, 7, s45\n"
"v_min_u32 v248, (.Lmha_end%=-.Lmha_pc%=-4), v248\n"
"global_load_dword v240, v248, s[94:95]\n"
"s_add_u32 m0, s41, 0x5000\n"
"s_add_u32 s42, s72, 0\n"
"buffer_load_dwordx4 v208, s[16:19], s42 offen lds\n"
"s_add_u32 m0, s41, 0x6000\n"
"s_add_u32 s42, s72, 0x7000\n"
"buffer_load_dwordx4 v208, s[16:19], s42 offen lds\n"
"s_add_u32 m0, s41, 0x7000\n"
"s_add_u32 s42, s72, 0xe000\n"
"buffer_load_dwordx4 v208, s[16:19], s42 offen lds\n"
"s_add_u32 m0, s41, 0x8000\n"
"s_add_u32 s42, s72, 0x15000\n"
"buffer_load_dwordx4 v208, s[16:19], s42 offen lds\n"
"s_add_u32 m0, s41, 0x9000\n"
"s_add_u32 s42, s72, 0x1c000\n"
"buffer_load_dwordx4 v208, s[16:19], s42 offen lds\n"
"s_add_u32 m0, s40, 0\n"
"s_add_u32 s42, s84, 0\n"
"buffer_load_dwordx4 v209, s[20:23], s42 offen lds\n"
"buffer_load_dwordx4 v210, s[20:23], s42 offen offset:1056 lds\n"
"s_add_u32 m0, s40, 0x840\n"
"s_add_u32 s42, s84, 0x8000\n"
"buffer_load_dwordx4 v209, s[20:23], s42 offen lds\n"
"buffer_load_dwordx4 v210, s[20:23], s42 offen offset:1056 lds\n"
"s_add_u32 m0, s40, 0x1080\n"
"s_add_u32 s42, s84, 0x40000\n"
"buffer_load_dwordx4 v209, s[20:23], s42 offen lds\n"
"buffer_load_dwordx4 v210, s[20:23], s42 offen offset:1056 lds\n"
"s_add_u32 s73, s72, 0x80\n"
"s_cmpk_eq_u32 s73, 896\n"
"s_cselect_b32 s73, 0, s73\n"
"s_lshl_b32 s85, s73, 12\n"
"s_add_u32 s74, s73, 0x80\n"
"s_cmpk_eq_u32 s74, 896\n"
"s_cselect_b32 s74, 0, s74\n"
"s_lshl_b32 s86, s74, 12\n"
"s_add_u32 s75, s74, 0x80\n"
"s_cmpk_eq_u32 s75, 896\n"
"s_cselect_b32 s75, 0, s75\n"
"s_lshl_b32 s87, s75, 12\n"
"s_add_u32 s76, s75, 0x80\n"
"s_cmpk_eq_u32 s76, 896\n"
"s_cselect_b32 s76, 0, s76\n"
"s_lshl_b32 s88, s76, 12\n"
"s_add_u32 s77, s76, 0x80\n"
"s_cmpk_eq_u32 s77, 896\n"
"s_cselect_b32 s77, 0, s77\n"
"s_lshl_b32 s89, s77, 12\n"
"s_add_u32 s78, s77, 0x80\n"
"s_cmpk_eq_u32 s78, 896\n"
"s_cselect_b32 s78, 0, s78\n"
"s_lshl_b32 s90, s78, 12\n"
"s_mov_b64 s[24:25], %1\n"
"s_mov_b32 s26, 0x40000\n"
"s_mov_b32 s27, 0x20000\n"
"v_and_b32 v248, 15, v211\n"
"v_xor_b32 v249, v248, v245\n"
"v_lshlrev_b32 v249, 4, v249\n"
"v_lshl_add_u32 v249, v245, 9, v249\n"
"s_lshl_b32 s45, s44, 14\n"
"v_xor_b32 v204, 0, v249\n"
"v_add_u32 v204, s45, v204\n"
"v_add_u32 v204, 0, v204\n"
"v_xor_b32 v205, 64, v249\n"
"v_add_u32 v205, s45, v205\n"
"v_add_u32 v205, 0x400, v205\n"
"v_xor_b32 v206, 0x80, v249\n"
"v_add_u32 v206, s45, v206\n"
"v_add_u32 v206, 0x800, v206\n"
"v_xor_b32 v207, 0xc0, v249\n"
"v_add_u32 v207, s45, v207\n"
"v_add_u32 v207, 0xc00, v207\n"
"v_xor_b32 v249, v248, v245\n"
"v_lshlrev_b32 v249, 4, v249\n"
"v_lshl_add_u32 v249, v248, 8, v249\n"
"v_xor_b32 v200, 0, v249\n"
"v_add_u32 v200, s40, v200\n"
"v_xor_b32 v201, 64, v249\n"
"v_add_u32 v201, s40, v201\n"
"v_xor_b32 v202, 0x80, v249\n"
"v_add_u32 v202, s40, v202\n"
"v_xor_b32 v203, 0xc0, v249\n"
"v_add_u32 v203, s40, v203\n"
"v_lshrrev_b32 v249, 1, v248\n"
"v_xor_b32 v249, v249, v245\n"
"v_lshlrev_b32 v249, 4, v249\n"
"v_lshl_add_u32 v249, v248, 7, v249\n"
"v_add_u32 v198, 0x5000, v249\n"
"v_xor_b32 v199, 64, v198\n"
"v_lshlrev_b32 v217, 4, v248\n"
"v_add_u32 v217, 0x27000, v217\n"
"v_lshrrev_b32 v249, 5, v211\n"
"v_lshl_add_u32 v249, v249, 8, v217\n"
"s_lshl_b32 s45, s44, 2\n"
"v_add_u32 v216, s45, v249\n"
"v_lshrrev_b32 v248, 5, v211\n"
"v_bfe_u32 v249, v211, 2, 2\n"
"v_lshl_add_u32 v248, v248, 2, v249\n"
"v_lshlrev_b32 v248, 7, v248\n"
"v_bfe_u32 v249, v211, 3, 1\n"
"v_lshl_add_u32 v248, v249, 6, v248\n"
"v_bfe_u32 v249, v211, 4, 1\n"
"v_mul_u32_u24 v246, 0x420, v249\n"
"v_add_u32 v246, s40, v246\n"
"v_and_b32 v249, 3, v211\n"
"v_add_u32 v192, v248, v246\n"
"v_lshl_add_u32 v196, v249, 4, v192\n"
"v_lshl_add_u32 v192, v249, 3, v192\n"
"v_xor_b32 v248, 64, v248\n"
"v_add_u32 v193, v248, v246\n"
"v_lshl_add_u32 v197, v249, 4, v193\n"
"v_lshl_add_u32 v193, v249, 3, v193\n"
"v_lshlrev_b32 v194, 4, v211\n"
"v_add_u32 v195, 0x10000, v194\n"
"s_lshl_b32 s45, s44, 11\n"
"v_add_u32 v212, s45, v194\n"
"v_add_u32 v213, s45, v195\n"
"v_add_u32 v214, s41, v194\n"
"v_add_u32 v215, s41, v195\n"
"v_lshrrev_b32 v248, 5, v211\n"
"v_and_b32 v249, 3, v244\n"
"v_lshl_add_u32 v248, v248, 3, v249\n"
"v_lshl_add_u32 v248, v248, 9, v247\n"
"v_add_u32 v238, s46, v248\n"
"v_add_u32 v239, 0x3e0, v238\n"
"s_mov_b64 s[28:29], %2\n"
"s_mov_b32 s30, 0x40000\n"
"s_mov_b32 s31, 0x20000\n"
"s_mov_b64 s[32:33], %4\n"
"s_mov_b32 s34, 0x38000\n"
"s_mov_b32 s35, 0x20000\n"
"s_mov_b64 s[36:37], %5\n"
"s_mov_b32 s38, 0x23000\n"
"s_mov_b32 s39, 0x20000\n"
"s_mov_b32 s43, 0x3db8aa3b\n"
"v_mov_b32 v228, 0\n"
"v_mov_b32 v229, 0\n"
"v_mov_b32 v230, 0\n"
"v_mov_b32 v231, 0\n"
"v_mov_b32 v232, 0\n"
"v_mov_b32 v233, 0\n"
"v_mov_b32 v234, 0\n"
"v_mov_b32 v235, 0\n"
"v_mov_b32 v236, 0\n"
"v_mov_b32 v237, 0\n"
"s_mov_b64 vcc, 0\n"
"s_add_u32 m0, s40, 0x18c0\n"
"s_add_u32 s42, s84, 0x48000\n"
"buffer_load_dwordx4 v209, s[20:23], s42 offen lds\n"
"buffer_load_dwordx4 v210, s[20:23], s42 offen offset:1056 lds\n"
"s_add_u32 m0, s40, 0x2100\n"
"s_add_u32 s42, s85, 0\n"
"buffer_load_dwordx4 v209, s[20:23], s42 offen lds\n"
"buffer_load_dwordx4 v210, s[20:23], s42 offen offset:1056 lds\n"
"s_add_u32 m0, s40, 0x2940\n"
"s_add_u32 s42, s85, 0x8000\n"
"buffer_load_dwordx4 v209, s[20:23], s42 offen lds\n"
"buffer_load_dwordx4 v210, s[20:23], s42 offen offset:1056 lds\n"
"s_add_u32 m0, s41, 0xa000\n"
"s_add_u32 s42, s73, 0\n"
"buffer_load_dwordx4 v208, s[16:19], s42 offen lds\n"
"s_add_u32 m0, s40, 0x3180\n"
"s_add_u32 s42, s85, 0x40000\n"
"buffer_load_dwordx4 v209, s[20:23], s42 offen lds\n"
"buffer_load_dwordx4 v210, s[20:23], s42 offen offset:1056 lds\n"
"s_add_u32 m0, s41, 0xb000\n"
"s_add_u32 s42, s73, 0x7000\n"
"buffer_load_dwordx4 v208, s[16:19], s42 offen lds\n"
"s_add_u32 m0, s41, 0xc000\n"
"s_add_u32 s42, s73, 0xe000\n"
"buffer_load_dwordx4 v208, s[16:19], s42 offen lds\n"
"s_add_u32 m0, s40, 0x39c0\n"
"s_add_u32 s42, s85, 0x48000\n"
"buffer_load_dwordx4 v209, s[20:23], s42 offen lds\n"
"buffer_load_dwordx4 v210, s[20:23], s42 offen offset:1056 lds\n"
"s_waitcnt vmcnt(19)\n"
"s_barrier\n"
"ds_read_b128 v[160:163], v198 offset:0\n"
"ds_read_b128 v[164:167], v199 offset:0\n"
"ds_read_b128 v[168:171], v198 offset:2048\n"
"ds_read_b128 v[172:175], v199 offset:2048\n"
"s_waitcnt vmcnt(17)\n"
"ds_read_b64_tr_b16 a[128:129], v196 offset:0\n"
"ds_read_b64_tr_b16 a[136:137], v196 offset:8\n"
"ds_read_b64_tr_b16 a[144:145], v197 offset:0\n"
"ds_read_b64_tr_b16 a[152:153], v197 offset:8\n"
"s_waitcnt vmcnt(15)\n"
"ds_read_b64_tr_b16 a[130:131], v196 offset:2112\n"
"ds_read_b64_tr_b16 a[138:139], v196 offset:2120\n"
"ds_read_b64_tr_b16 a[146:147], v197 offset:2112\n"
"ds_read_b64_tr_b16 a[154:155], v197 offset:2120\n"
"s_waitcnt vmcnt(13)\n"
"ds_read_b64_tr_b16 a[132:133], v196 offset:4224\n"
"ds_read_b64_tr_b16 a[140:141], v196 offset:4232\n"
"ds_read_b64_tr_b16 a[148:149], v197 offset:4224\n"
"ds_read_b64_tr_b16 a[156:157], v197 offset:4232\n"
"s_waitcnt vmcnt(11)\n"
"ds_read_b64_tr_b16 a[134:135], v196 offset:6336\n"
"ds_read_b64_tr_b16 a[142:143], v196 offset:6344\n"
"ds_read_b64_tr_b16 a[150:151], v197 offset:6336\n"
"ds_read_b64_tr_b16 a[158:159], v197 offset:6344\n"
"s_waitcnt lgkmcnt(11)\n"
"v_mfma_f32_16x16x32_bf16 v[0:3], a[128:131], v[160:163], 0\n"
"s_waitcnt lgkmcnt(3)\n"
"v_mfma_f32_16x16x32_bf16 v[0:3], a[132:135], v[164:167], v[0:3]\n"
"v_mfma_f32_16x16x32_bf16 v[4:7], a[136:139], v[160:163], 0\n"
"s_waitcnt lgkmcnt(2)\n"
"v_mfma_f32_16x16x32_bf16 v[4:7], a[140:143], v[164:167], v[4:7]\n"
"v_mfma_f32_16x16x32_bf16 v[8:11], a[144:147], v[160:163], 0\n"
"s_waitcnt vmcnt(9) lgkmcnt(1)\n"
"s_add_u32 m0, s41, 0xd000\n"
"ds_read_b64_tr_b16 a[160:161], v196 offset:8448\n"
"v_mfma_f32_16x16x32_bf16 v[8:11], a[148:151], v[164:167], v[8:11]\n"
"s_add_u32 s42, s73, 0x15000\n"
"buffer_load_dwordx4 v208, s[16:19], s42 offen lds\n"
"v_mfma_f32_16x16x32_bf16 v[12:15], a[152:155], v[160:163], 0\n"
"s_waitcnt lgkmcnt(1)\n"
"ds_read_b128 v[160:163], v198 offset:4096\n"
"ds_read_b64_tr_b16 a[168:169], v196 offset:8456\n"
"v_mfma_f32_16x16x32_bf16 v[12:15], a[156:159], v[164:167], v[12:15]\n"
"ds_read_b128 v[164:167], v199 offset:4096\n"
"ds_read_b64_tr_b16 a[176:177], v197 offset:8448\n"
"v_mfma_f32_16x16x32_bf16 v[16:19], a[128:131], v[168:171], 0\n"
"ds_read_b64_tr_b16 a[184:185], v197 offset:8456\n"
"v_mfma_f32_16x16x32_bf16 v[16:19], a[132:135], v[172:175], v[16:19]\n"
"v_mfma_f32_16x16x32_bf16 v[20:23], a[136:139], v[168:171], 0\n"
"v_mfma_f32_16x16x32_bf16 v[20:23], a[140:143], v[172:175], v[20:23]\n"
"v_mfma_f32_16x16x32_bf16 v[24:27], a[144:147], v[168:171], 0\n"
"s_add_u32 m0, s40, 0x4200\n"
"v_mfma_f32_16x16x32_bf16 v[24:27], a[148:151], v[172:175], v[24:27]\n"
"s_add_u32 s42, s86, 0\n"
"buffer_load_dwordx4 v209, s[20:23], s42 offen lds\n"
"v_mfma_f32_16x16x32_bf16 v[28:31], a[152:155], v[168:171], 0\n"
"ds_read_b128 v[168:171], v198 offset:6144\n"
"v_mfma_f32_16x16x32_bf16 v[28:31], a[156:159], v[172:175], v[28:31]\n"
"s_waitcnt lgkmcnt(5)\n"
"ds_read_b128 v[172:175], v199 offset:6144\n"
"v_mfma_f32_16x16x32_bf16 v[32:35], a[128:131], v[160:163], 0\n"
"s_waitcnt lgkmcnt(4)\n"
"v_mfma_f32_16x16x32_bf16 v[32:35], a[132:135], v[164:167], v[32:35]\n"
"buffer_load_dwordx4 v210, s[20:23], s42 offen offset:1056 lds\n"
"v_mfma_f32_16x16x32_bf16 v[36:39], a[136:139], v[160:163], 0\n"
"v_mfma_f32_16x16x32_bf16 v[36:39], a[140:143], v[164:167], v[36:39]\n"
"v_mfma_f32_16x16x32_bf16 v[40:43], a[144:147], v[160:163], 0\n"
"s_add_u32 m0, s40, 0\n"
"v_mfma_f32_16x16x32_bf16 v[40:43], a[148:151], v[164:167], v[40:43]\n"
"s_mov_b32 s42, 0\n"
"buffer_load_dwordx4 v204, s[24:27], s42 offen nt lds\n"
"v_mfma_f32_16x16x32_bf16 v[44:47], a[152:155], v[160:163], 0\n"
"ds_read_b128 v[160:163], v198 offset:8192\n"
"v_mfma_f32_16x16x32_bf16 v[44:47], a[156:159], v[164:167], v[44:47]\n"
"s_waitcnt lgkmcnt(2)\n"
"ds_read_b128 v[164:167], v199 offset:8192\n"
"v_mfma_f32_16x16x32_bf16 v[48:51], a[128:131], v[168:171], 0\n"
"s_waitcnt vmcnt(11) lgkmcnt(2)\n"
"ds_read_b64_tr_b16 a[162:163], v196 offset:10560\n"
"v_mfma_f32_16x16x32_bf16 v[48:51], a[132:135], v[172:175], v[48:51]\n"
"buffer_load_dwordx4 v205, s[24:27], s42 offen offset:1024 nt lds\n"
"v_mfma_f32_16x16x32_bf16 v[52:55], a[136:139], v[168:171], 0\n"
"ds_read_b64_tr_b16 a[170:171], v196 offset:10568\n"
"v_mfma_f32_16x16x32_bf16 v[52:55], a[140:143], v[172:175], v[52:55]\n"
"ds_read_b64_tr_b16 a[178:179], v197 offset:10560\n"
"v_mfma_f32_16x16x32_bf16 v[56:59], a[144:147], v[168:171], 0\n"
"ds_read_b64_tr_b16 a[186:187], v197 offset:10568\n"
"v_mfma_f32_16x16x32_bf16 v[56:59], a[148:151], v[172:175], v[56:59]\n"
"buffer_load_dwordx4 v206, s[24:27], s42 offen offset:2048 nt lds\n"
"v_mfma_f32_16x16x32_bf16 v[60:63], a[152:155], v[168:171], 0\n"
"ds_read_b128 v[168:171], v198 offset:10240\n"
"v_mfma_f32_16x16x32_bf16 v[60:63], a[156:159], v[172:175], v[60:63]\n"
"s_waitcnt lgkmcnt(6)\n"
"ds_read_b128 v[172:175], v199 offset:10240\n"
"v_mfma_f32_16x16x32_bf16 v[64:67], a[128:131], v[160:163], 0\n"
"s_waitcnt lgkmcnt(6)\n"
"v_mfma_f32_16x16x32_bf16 v[64:67], a[132:135], v[164:167], v[64:67]\n"
"buffer_load_dwordx4 v207, s[24:27], s42 offen offset:3072 nt lds\n"
"v_mfma_f32_16x16x32_bf16 v[68:71], a[136:139], v[160:163], 0\n"
"v_mfma_f32_16x16x32_bf16 v[68:71], a[140:143], v[164:167], v[68:71]\n"
"v_mfma_f32_16x16x32_bf16 v[72:75], a[144:147], v[160:163], 0\n"
"s_add_u32 m0, s41, 0xe000\n"
"v_mfma_f32_16x16x32_bf16 v[72:75], a[148:151], v[164:167], v[72:75]\n"
"s_add_u32 s42, s73, 0x1c000\n"
"buffer_load_dwordx4 v208, s[16:19], s42 offen lds\n"
"v_mfma_f32_16x16x32_bf16 v[76:79], a[152:155], v[160:163], 0\n"
"s_add_u32 m0, s40, 0x2940\n"
"ds_read_b128 v[160:163], v198 offset:12288\n"
"v_mfma_f32_16x16x32_bf16 v[76:79], a[156:159], v[164:167], v[76:79]\n"
"s_waitcnt lgkmcnt(2)\n"
"ds_read_b128 v[164:167], v199 offset:12288\n"
"v_mfma_f32_16x16x32_bf16 v[80:83], a[128:131], v[168:171], 0\n"
"s_waitcnt lgkmcnt(2)\n"
"v_mfma_f32_16x16x32_bf16 v[80:83], a[132:135], v[172:175], v[80:83]\n"
"s_add_u32 s42, s86, 0x8000\n"
"buffer_load_dwordx4 v209, s[20:23], s42 offen lds\n"
"s_mov_b64 vcc, -1\n"
"s_cmp_eq_u32 s44, 0\n"
"s_cbranch_scc1 .Lpad0_%=\n"
"s_cmp_eq_u32 s44, 1\n"
"s_cbranch_scc1 .Lpad1_%=\n"
"s_cmp_eq_u32 s44, 2\n"
"s_cbranch_scc1 .Lpad2_%=\n"
"s_branch .Lpad3_%=\n"
".Lpad12_%=:\n"
"s_mov_b64 vcc, 0\n"
"v_mfma_f32_16x16x32_bf16 v[84:87], a[136:139], v[168:171], 0\n"
"v_mfma_f32_16x16x32_bf16 v[84:87], a[140:143], v[172:175], v[84:87]\n"
"v_mfma_f32_16x16x32_bf16 v[88:91], a[144:147], v[168:171], 0\n"
"s_waitcnt vmcnt(13)\n"
"ds_read_b64_tr_b16 a[164:165], v196 offset:12672\n"
"v_mfma_f32_16x16x32_bf16 v[88:91], a[148:151], v[172:175], v[88:91]\n"
"buffer_load_dwordx4 v210, s[20:23], s42 offen offset:1056 lds\n"
"v_mfma_f32_16x16x32_bf16 v[92:95], a[152:155], v[168:171], 0\n"
"s_add_u32 m0, s41, 0xf000\n"
"ds_read_b128 v[168:171], v198 offset:14336\n"
"ds_read_b64_tr_b16 a[172:173], v196 offset:12680\n"
"v_mfma_f32_16x16x32_bf16 v[92:95], a[156:159], v[172:175], v[92:95]\n"
"s_waitcnt lgkmcnt(4)\n"
"ds_read_b128 v[172:175], v199 offset:14336\n"
"ds_read_b64_tr_b16 a[180:181], v197 offset:12672\n"
"v_mfma_f32_16x16x32_bf16 v[96:99], a[128:131], v[160:163], 0\n"
"s_waitcnt lgkmcnt(5)\n"
"ds_read_b64_tr_b16 a[188:189], v197 offset:12680\n"
"v_mfma_f32_16x16x32_bf16 v[96:99], a[132:135], v[164:167], v[96:99]\n"
"s_add_u32 s42, s74, 0\n"
"buffer_load_dwordx4 v208, s[16:19], s42 offen lds\n"
"v_mfma_f32_16x16x32_bf16 v[100:103], a[136:139], v[160:163], 0\n"
"v_mfma_f32_16x16x32_bf16 v[100:103], a[140:143], v[164:167], v[100:103]\n"
"v_mfma_f32_16x16x32_bf16 v[104:107], a[144:147], v[160:163], 0\n"
"s_add_u32 m0, s41, 0x10000\n"
"v_mfma_f32_16x16x32_bf16 v[104:107], a[148:151], v[164:167], v[104:107]\n"
"s_add_u32 s42, s74, 0x7000\n"
"buffer_load_dwordx4 v208, s[16:19], s42 offen lds\n"
"v_mfma_f32_16x16x32_bf16 v[108:111], a[152:155], v[160:163], 0\n"
"s_add_u32 m0, s40, 0x3180\n"
"ds_read_b128 v[160:163], v198 offset:16384\n"
"v_mfma_f32_16x16x32_bf16 v[108:111], a[156:159], v[164:167], v[108:111]\n"
"s_waitcnt lgkmcnt(5)\n"
"ds_read_b128 v[164:167], v199 offset:16384\n"
"v_mfma_f32_16x16x32_bf16 v[112:115], a[128:131], v[168:171], 0\n"
"s_waitcnt lgkmcnt(4)\n"
"v_mfma_f32_16x16x32_bf16 v[112:115], a[132:135], v[172:175], v[112:115]\n"
"s_waitcnt lgkmcnt(2)\n"
"s_add_u32 s42, s86, 0x40000\n"
"buffer_load_dwordx4 v209, s[20:23], s42 offen lds\n"
"v_mfma_f32_16x16x32_bf16 v[116:119], a[136:139], v[168:171], 0\n"
"v_mfma_f32_16x16x32_bf16 v[116:119], a[140:143], v[172:175], v[116:119]\n"
"v_mfma_f32_16x16x32_bf16 v[120:123], a[144:147], v[168:171], 0\n"
"v_mfma_f32_16x16x32_bf16 v[120:123], a[148:151], v[172:175], v[120:123]\n"
"buffer_load_dwordx4 v210, s[20:23], s42 offen offset:1056 lds\n"
"v_mfma_f32_16x16x32_bf16 v[124:127], a[152:155], v[168:171], 0\n"
"s_waitcnt vmcnt(14)\n"
"s_add_u32 m0, s40, 0x1080\n"
"ds_read_b128 v[168:171], v198 offset:18432\n"
"ds_read_b64_tr_b16 a[166:167], v196 offset:14784\n"
"v_mfma_f32_16x16x32_bf16 v[124:127], a[156:159], v[172:175], v[124:127]\n"
"s_waitcnt lgkmcnt(3)\n"
"ds_read_b128 v[172:175], v199 offset:18432\n"
"ds_read_b64_tr_b16 a[174:175], v196 offset:14792\n"
"v_mfma_f32_16x16x32_bf16 v[128:131], a[128:131], v[160:163], 0\n"
"s_waitcnt lgkmcnt(4)\n"
"ds_read_b64_tr_b16 a[182:183], v197 offset:14784\n"
"v_mfma_f32_16x16x32_bf16 v[128:131], a[132:135], v[164:167], v[128:131]\n"
"s_mov_b32 s42, 0x100\n"
"buffer_load_dwordx4 v204, s[24:27], s42 offen nt lds\n"
"v_mfma_f32_16x16x32_bf16 v[132:135], a[136:139], v[160:163], 0\n"
"ds_read_b64_tr_b16 a[190:191], v197 offset:14792\n"
"v_mfma_f32_16x16x32_bf16 v[132:135], a[140:143], v[164:167], v[132:135]\n"
"v_mfma_f32_16x16x32_bf16 v[136:139], a[144:147], v[160:163], 0\n"
"v_mfma_f32_16x16x32_bf16 v[136:139], a[148:151], v[164:167], v[136:139]\n"
"buffer_load_dwordx4 v205, s[24:27], s42 offen offset:1024 nt lds\n"
"v_mfma_f32_16x16x32_bf16 v[140:143], a[152:155], v[160:163], 0\n"
"s_waitcnt vmcnt(8)\n"
"s_barrier\n"
"ds_read_b128 v[160:163], v198 offset:20480\n"
"v_mfma_f32_16x16x32_bf16 v[140:143], a[156:159], v[164:167], v[140:143]\n"
"s_waitcnt lgkmcnt(6)\n"
"ds_read_b128 v[164:167], v199 offset:20480\n"
"v_mfma_f32_16x16x32_bf16 v[144:147], a[128:131], v[168:171], 0\n"
"s_waitcnt lgkmcnt(5)\n"
"v_mfma_f32_16x16x32_bf16 v[144:147], a[132:135], v[172:175], v[144:147]\n"
"buffer_load_dwordx4 v206, s[24:27], s42 offen offset:2048 nt lds\n"
"v_mfma_f32_16x16x32_bf16 v[148:151], a[136:139], v[168:171], 0\n"
"v_mfma_f32_16x16x32_bf16 v[148:151], a[140:143], v[172:175], v[148:151]\n"
"v_mfma_f32_16x16x32_bf16 v[152:155], a[144:147], v[168:171], 0\n"
"v_mfma_f32_16x16x32_bf16 v[152:155], a[148:151], v[172:175], v[152:155]\n"
"buffer_load_dwordx4 v207, s[24:27], s42 offen offset:3072 nt lds\n"
"v_mfma_f32_16x16x32_bf16 v[156:159], a[152:155], v[168:171], 0\n"
"s_add_u32 m0, s41, 0x11000\n"
"ds_read_b128 v[168:171], v198 offset:22528\n"
"ds_read_b64_tr_b16 a[192:193], v196 offset:16896\n"
"v_mfma_f32_16x16x32_bf16 v[156:159], a[156:159], v[172:175], v[156:159]\n"
"s_waitcnt lgkmcnt(3)\n"
"ds_read_b128 v[172:175], v199 offset:22528\n"
"ds_read_b64_tr_b16 a[200:201], v196 offset:16904\n"
"v_mfma_f32_16x16x32_bf16 v[0:3], a[160:163], v[160:163], v[0:3]\n"
"s_waitcnt lgkmcnt(4)\n"
"ds_read_b64_tr_b16 a[208:209], v197 offset:16896\n"
"v_mfma_f32_16x16x32_bf16 v[0:3], a[164:167], v[164:167], v[0:3]\n"
"s_add_u32 s42, s74, 0xe000\n"
"buffer_load_dwordx4 v208, s[16:19], s42 offen lds\n"
"v_mfma_f32_16x16x32_bf16 v[4:7], a[168:171], v[160:163], v[4:7]\n"
"ds_read_b64_tr_b16 a[216:217], v197 offset:16904\n"
"v_mfma_f32_16x16x32_bf16 v[4:7], a[172:175], v[164:167], v[4:7]\n"
"v_mfma_f32_16x16x32_bf16 v[8:11], a[176:179], v[160:163], v[8:11]\n"
"s_add_u32 m0, s40, 0x39c0\n"
"v_mfma_f32_16x16x32_bf16 v[8:11], a[180:183], v[164:167], v[8:11]\n"
"s_add_u32 s42, s86, 0x48000\n"
"buffer_load_dwordx4 v209, s[20:23], s42 offen lds\n"
"v_mfma_f32_16x16x32_bf16 v[12:15], a[184:187], v[160:163], v[12:15]\n"
"ds_read_b128 v[160:163], v198 offset:24576\n"
"v_mfma_f32_16x16x32_bf16 v[12:15], a[188:191], v[164:167], v[12:15]\n"
"s_waitcnt lgkmcnt(6)\n"
"ds_read_b128 v[164:167], v199 offset:24576\n"
"v_mfma_f32_16x16x32_bf16 v[16:19], a[160:163], v[168:171], v[16:19]\n"
"s_waitcnt lgkmcnt(5)\n"
"v_mfma_f32_16x16x32_bf16 v[16:19], a[164:167], v[172:175], v[16:19]\n"
"buffer_load_dwordx4 v210, s[20:23], s42 offen offset:1056 lds\n"
"v_mfma_f32_16x16x32_bf16 v[20:23], a[168:171], v[168:171], v[20:23]\n"
"ds_read_b128 a[0:3], v200 offset:0\n"
"v_mfma_f32_16x16x32_bf16 v[20:23], a[172:175], v[172:175], v[20:23]\n"
"ds_read_b128 a[4:7], v201 offset:0\n"
"v_mfma_f32_16x16x32_bf16 v[24:27], a[176:179], v[168:171], v[24:27]\n"
"s_add_u32 m0, s41, 0x12000\n"
"ds_read_b128 a[8:11], v202 offset:0\n"
"v_mfma_f32_16x16x32_bf16 v[24:27], a[180:183], v[172:175], v[24:27]\n"
"s_add_u32 s42, s74, 0x15000\n"
"buffer_load_dwordx4 v208, s[16:19], s42 offen lds\n"
"v_mfma_f32_16x16x32_bf16 v[28:31], a[184:187], v[168:171], v[28:31]\n"
"s_add_u32 m0, s41, 0x13000\n"
"ds_read_b128 v[168:171], v198 offset:26624\n"
"ds_read_b128 a[12:15], v203 offset:0\n"
"v_mfma_f32_16x16x32_bf16 v[28:31], a[188:191], v[172:175], v[28:31]\n"
"s_waitcnt lgkmcnt(6)\n"
"ds_read_b128 v[172:175], v199 offset:26624\n"
"v_mfma_f32_16x16x32_bf16 v[32:35], a[160:163], v[160:163], v[32:35]\n"
"s_waitcnt lgkmcnt(6)\n"
"v_mfma_f32_16x16x32_bf16 v[32:35], a[164:167], v[164:167], v[32:35]\n"
"s_add_u32 s42, s74, 0x1c000\n"
"buffer_load_dwordx4 v208, s[16:19], s42 offen lds\n"
"v_mfma_f32_16x16x32_bf16 v[36:39], a[168:171], v[160:163], v[36:39]\n"
"v_mfma_f32_16x16x32_bf16 v[36:39], a[172:175], v[164:167], v[36:39]\n"
"v_mfma_f32_16x16x32_bf16 v[40:43], a[176:179], v[160:163], v[40:43]\n"
"s_add_u32 m0, s40, 0x4200\n"
"v_mfma_f32_16x16x32_bf16 v[40:43], a[180:183], v[164:167], v[40:43]\n"
"s_add_u32 s42, s87, 0\n"
"buffer_load_dwordx4 v209, s[20:23], s42 offen lds\n"
"v_mfma_f32_16x16x32_bf16 v[44:47], a[184:187], v[160:163], v[44:47]\n"
"ds_read_b128 v[160:163], v198 offset:28672\n"
"v_mfma_f32_16x16x32_bf16 v[44:47], a[188:191], v[164:167], v[44:47]\n"
"s_waitcnt lgkmcnt(3)\n"
"ds_read_b128 v[164:167], v199 offset:28672\n"
"v_mfma_f32_16x16x32_bf16 v[48:51], a[160:163], v[168:171], v[48:51]\n"
"s_waitcnt lgkmcnt(2)\n"
"v_mfma_f32_16x16x32_bf16 v[48:51], a[164:167], v[172:175], v[48:51]\n"
"buffer_load_dwordx4 v210, s[20:23], s42 offen offset:1056 lds\n"
"v_mfma_f32_16x16x32_bf16 v[52:55], a[168:171], v[168:171], v[52:55]\n"
"s_waitcnt vmcnt(15)\n"
"ds_read_b64_tr_b16 a[194:195], v196 offset:10560\n"
"v_mfma_f32_16x16x32_bf16 v[52:55], a[172:175], v[172:175], v[52:55]\n"
"ds_read_b64_tr_b16 a[202:203], v196 offset:10568\n"
"v_mfma_f32_16x16x32_bf16 v[56:59], a[176:179], v[168:171], v[56:59]\n"
"s_add_u32 m0, s41, 0\n"
"ds_read_b64_tr_b16 a[210:211], v197 offset:10560\n"
"v_mfma_f32_16x16x32_bf16 v[56:59], a[180:183], v[172:175], v[56:59]\n"
"s_add_u32 s42, s75, 0\n"
"buffer_load_dwordx4 v208, s[16:19], s42 offen lds\n"
"v_mfma_f32_16x16x32_bf16 v[60:63], a[184:187], v[168:171], v[60:63]\n"
"s_add_u32 m0, s40, 0\n"
"ds_read_b128 v[168:171], v198 offset:30720\n"
"ds_read_b64_tr_b16 a[218:219], v197 offset:10568\n"
"v_mfma_f32_16x16x32_bf16 v[60:63], a[188:191], v[172:175], v[60:63]\n"
"s_waitcnt lgkmcnt(6)\n"
"ds_read_b128 v[172:175], v199 offset:30720\n"
"v_mfma_f32_16x16x32_bf16 v[64:67], a[160:163], v[160:163], v[64:67]\n"
"s_waitcnt lgkmcnt(6)\n"
"v_mfma_f32_16x16x32_bf16 v[64:67], a[164:167], v[164:167], v[64:67]\n"
"s_mov_b32 s42, 0x2000\n"
"buffer_load_dwordx4 v204, s[24:27], s42 offen nt lds\n"
"v_mfma_f32_16x16x32_bf16 v[68:71], a[168:171], v[160:163], v[68:71]\n"
"v_mfma_f32_16x16x32_bf16 v[68:71], a[172:175], v[164:167], v[68:71]\n"
"v_mfma_f32_16x16x32_bf16 v[72:75], a[176:179], v[160:163], v[72:75]\n"
"v_mfma_f32_16x16x32_bf16 v[72:75], a[180:183], v[164:167], v[72:75]\n"
"buffer_load_dwordx4 v205, s[24:27], s42 offen offset:1024 nt lds\n"
"v_mfma_f32_16x16x32_bf16 v[76:79], a[184:187], v[160:163], v[76:79]\n"
"ds_read_b128 v[160:163], v198 offset:32768\n"
"v_mfma_f32_16x16x32_bf16 v[76:79], a[188:191], v[164:167], v[76:79]\n"
"s_waitcnt lgkmcnt(3)\n"
"ds_read_b128 v[164:167], v199 offset:32768\n"
"v_mfma_f32_16x16x32_bf16 v[80:83], a[160:163], v[168:171], v[80:83]\n"
"s_waitcnt lgkmcnt(2)\n"
"v_mfma_f32_16x16x32_bf16 v[80:83], a[164:167], v[172:175], v[80:83]\n"
"buffer_load_dwordx4 v206, s[24:27], s42 offen offset:2048 nt lds\n"
"v_mfma_f32_16x16x32_bf16 v[84:87], a[168:171], v[168:171], v[84:87]\n"
"s_waitcnt vmcnt(15)\n"
"ds_read_b64_tr_b16 a[196:197], v196 offset:12672\n"
"v_mfma_f32_16x16x32_bf16 v[84:87], a[172:175], v[172:175], v[84:87]\n"
"ds_read_b64_tr_b16 a[204:205], v196 offset:12680\n"
"v_mfma_f32_16x16x32_bf16 v[88:91], a[176:179], v[168:171], v[88:91]\n"
"ds_read_b64_tr_b16 a[212:213], v197 offset:12672\n"
"v_mfma_f32_16x16x32_bf16 v[88:91], a[180:183], v[172:175], v[88:91]\n"
"buffer_load_dwordx4 v207, s[24:27], s42 offen offset:3072 nt lds\n"
"v_mfma_f32_16x16x32_bf16 v[92:95], a[184:187], v[168:171], v[92:95]\n"
"s_add_u32 m0, s40, 0x2940\n"
"ds_read_b128 v[168:171], v198 offset:34816\n"
"ds_read_b64_tr_b16 a[220:221], v197 offset:12680\n"
"v_mfma_f32_16x16x32_bf16 v[92:95], a[188:191], v[172:175], v[92:95]\n"
"s_waitcnt lgkmcnt(6)\n"
"ds_read_b128 v[172:175], v199 offset:34816\n"
"v_mfma_f32_16x16x32_bf16 v[96:99], a[160:163], v[160:163], v[96:99]\n"
"s_waitcnt lgkmcnt(6)\n"
"v_mfma_f32_16x16x32_bf16 v[96:99], a[164:167], v[164:167], v[96:99]\n"
"s_add_u32 s42, s87, 0x8000\n"
"buffer_load_dwordx4 v209, s[20:23], s42 offen lds\n"
"v_mfma_f32_16x16x32_bf16 v[100:103], a[168:171], v[160:163], v[100:103]\n"
"v_mfma_f32_16x16x32_bf16 v[100:103], a[172:175], v[164:167], v[100:103]\n"
"v_mfma_f32_16x16x32_bf16 v[104:107], a[176:179], v[160:163], v[104:107]\n"
"v_mfma_f32_16x16x32_bf16 v[104:107], a[180:183], v[164:167], v[104:107]\n"
"buffer_load_dwordx4 v210, s[20:23], s42 offen offset:1056 lds\n"
"v_mfma_f32_16x16x32_bf16 v[108:111], a[184:187], v[160:163], v[108:111]\n"
"s_waitcnt vmcnt(14)\n"
"s_add_u32 m0, s41, 0x1000\n"
"ds_read_b128 v[160:163], v198 offset:36864\n"
"ds_read_b128 a[16:19], v200 offset:4224\n"
"v_mfma_f32_16x16x32_bf16 v[108:111], a[188:191], v[164:167], v[108:111]\n"
"s_waitcnt lgkmcnt(4)\n"
"ds_read_b128 v[164:167], v199 offset:36864\n"
"ds_read_b128 a[20:23], v201 offset:4224\n"
"v_mfma_f32_16x16x32_bf16 v[112:115], a[160:163], v[168:171], v[112:115]\n"
"s_waitcnt lgkmcnt(4)\n"
"ds_read_b128 a[24:27], v202 offset:4224\n"
"v_mfma_f32_16x16x32_bf16 v[112:115], a[164:167], v[172:175], v[112:115]\n"
"s_add_u32 s42, s75, 0x7000\n"
"buffer_load_dwordx4 v208, s[16:19], s42 offen lds\n"
".Lpad0_%=:\n"
"s_cbranch_vccnz .Lpad4_%=\n"
"v_mfma_f32_16x16x32_bf16 v[116:119], a[168:171], v[168:171], v[116:119]\n"
"ds_read_b128 a[28:31], v203 offset:4224\n"
"v_mfma_f32_16x16x32_bf16 v[116:119], a[172:175], v[172:175], v[116:119]\n"
"v_mfma_f32_16x16x32_bf16 v[120:123], a[176:179], v[168:171], v[120:123]\n"
"s_add_u32 m0, s40, 0x3180\n"
"v_mfma_f32_16x16x32_bf16 v[120:123], a[180:183], v[172:175], v[120:123]\n"
"s_add_u32 s42, s87, 0x40000\n"
"buffer_load_dwordx4 v209, s[20:23], s42 offen lds\n"
"v_mfma_f32_16x16x32_bf16 v[124:127], a[184:187], v[168:171], v[124:127]\n"
"s_waitcnt vmcnt(13)\n"
"ds_read_b128 v[168:171], v198 offset:38912\n"
"ds_read_b64_tr_b16 a[198:199], v196 offset:14784\n"
"v_mfma_f32_16x16x32_bf16 v[124:127], a[188:191], v[172:175], v[124:127]\n"
"s_waitcnt lgkmcnt(7)\n"
"ds_read_b128 v[172:175], v199 offset:38912\n"
"ds_read_b64_tr_b16 a[206:207], v196 offset:14792\n"
"v_mfma_f32_16x16x32_bf16 v[128:131], a[160:163], v[160:163], v[128:131]\n"
"s_waitcnt lgkmcnt(7)\n"
"ds_read_b64_tr_b16 a[214:215], v197 offset:14784\n"
"v_mfma_f32_16x16x32_bf16 v[128:131], a[164:167], v[164:167], v[128:131]\n"
"buffer_load_dwordx4 v210, s[20:23], s42 offen offset:1056 lds\n"
"v_mfma_f32_16x16x32_bf16 v[132:135], a[168:171], v[160:163], v[132:135]\n"
"ds_read_b64_tr_b16 a[222:223], v197 offset:14792\n"
"v_mfma_f32_16x16x32_bf16 v[132:135], a[172:175], v[164:167], v[132:135]\n"
"v_mfma_f32_16x16x32_bf16 v[136:139], a[176:179], v[160:163], v[136:139]\n"
"s_add_u32 m0, s41, 0x2000\n"
"v_mfma_f32_16x16x32_bf16 v[136:139], a[180:183], v[164:167], v[136:139]\n"
"s_add_u32 s42, s75, 0xe000\n"
"buffer_load_dwordx4 v208, s[16:19], s42 offen lds\n"
"v_mfma_f32_16x16x32_bf16 v[140:143], a[184:187], v[160:163], v[140:143]\n"
"s_waitcnt vmcnt(13)\n"
"s_add_u32 m0, s41, 0x3000\n"
"s_barrier\n"
"ds_read_b128 v[160:163], v198 offset:40960\n"
"v_mfma_f32_16x16x32_bf16 v[140:143], a[188:191], v[164:167], v[140:143]\n"
"s_waitcnt lgkmcnt(6)\n"
"ds_read_b128 v[164:167], v199 offset:40960\n"
"v_mfma_f32_16x16x32_bf16 v[144:147], a[160:163], v[168:171], v[144:147]\n"
"s_waitcnt lgkmcnt(5)\n"
"v_mfma_f32_16x16x32_bf16 v[144:147], a[164:167], v[172:175], v[144:147]\n"
"s_add_u32 s42, s75, 0x15000\n"
"buffer_load_dwordx4 v208, s[16:19], s42 offen lds\n"
"v_mfma_f32_16x16x32_bf16 v[148:151], a[168:171], v[168:171], v[148:151]\n"
"v_mfma_f32_16x16x32_bf16 v[148:151], a[172:175], v[172:175], v[148:151]\n"
"v_mfma_f32_16x16x32_bf16 v[152:155], a[176:179], v[168:171], v[152:155]\n"
"s_add_u32 m0, s40, 0x39c0\n"
"v_mfma_f32_16x16x32_bf16 v[152:155], a[180:183], v[172:175], v[152:155]\n"
"s_waitcnt lgkmcnt(2)\n"
"s_add_u32 s42, s87, 0x48000\n"
"buffer_load_dwordx4 v209, s[20:23], s42 offen lds\n"
"v_mfma_f32_16x16x32_bf16 v[156:159], a[184:187], v[168:171], v[156:159]\n"
"ds_read_b128 v[168:171], v198 offset:43008\n"
"v_mfma_f32_16x16x32_bf16 v[156:159], a[188:191], v[172:175], v[156:159]\n"
"s_waitcnt lgkmcnt(2)\n"
"ds_read_b128 v[172:175], v199 offset:43008\n"
"v_mfma_f32_16x16x32_bf16 v[0:3], a[192:195], v[160:163], v[0:3]\n"
"s_waitcnt lgkmcnt(2)\n"
"v_mfma_f32_16x16x32_bf16 v[0:3], a[196:199], v[164:167], v[0:3]\n"
"buffer_load_dwordx4 v210, s[20:23], s42 offen offset:1056 lds\n"
"v_mfma_f32_16x16x32_bf16 v[4:7], a[200:203], v[160:163], v[4:7]\n"
"v_mfma_f32_16x16x32_bf16 v[4:7], a[204:207], v[164:167], v[4:7]\n"
"v_mfma_f32_16x16x32_bf16 v[8:11], a[208:211], v[160:163], v[8:11]\n"
"s_add_u32 m0, s40, 0x1080\n"
"v_mfma_f32_16x16x32_bf16 v[8:11], a[212:215], v[164:167], v[8:11]\n"
"s_mov_b32 s42, 0x2100\n"
"buffer_load_dwordx4 v204, s[24:27], s42 offen nt lds\n"
"v_mfma_f32_16x16x32_bf16 v[12:15], a[216:219], v[160:163], v[12:15]\n"
"s_waitcnt vmcnt(15)\n"
"ds_read_b128 v[160:163], v198 offset:45056\n"
"ds_read_b64_tr_b16 a[224:225], v196 offset:16896\n"
"v_mfma_f32_16x16x32_bf16 v[12:15], a[220:223], v[164:167], v[12:15]\n"
"s_waitcnt lgkmcnt(3)\n"
"ds_read_b128 v[164:167], v199 offset:45056\n"
"ds_read_b64_tr_b16 a[232:233], v196 offset:16904\n"
"v_mfma_f32_16x16x32_bf16 v[16:19], a[192:195], v[168:171], v[16:19]\n"
"s_waitcnt lgkmcnt(4)\n"
"ds_read_b64_tr_b16 a[240:241], v197 offset:16896\n"
"v_mfma_f32_16x16x32_bf16 v[16:19], a[196:199], v[172:175], v[16:19]\n"
"buffer_load_dwordx4 v205, s[24:27], s42 offen offset:1024 nt lds\n"
"v_mfma_f32_16x16x32_bf16 v[20:23], a[200:203], v[168:171], v[20:23]\n"
"ds_read_b64_tr_b16 a[248:249], v197 offset:16904\n"
"v_mfma_f32_16x16x32_bf16 v[20:23], a[204:207], v[172:175], v[20:23]\n"
"v_mfma_f32_16x16x32_bf16 v[24:27], a[208:211], v[168:171], v[24:27]\n"
"v_mfma_f32_16x16x32_bf16 v[24:27], a[212:215], v[172:175], v[24:27]\n"
"buffer_load_dwordx4 v206, s[24:27], s42 offen offset:2048 nt lds\n"
"v_mfma_f32_16x16x32_bf16 v[28:31], a[216:219], v[168:171], v[28:31]\n"
"ds_read_b128 v[168:171], v198 offset:47104\n"
"v_mfma_f32_16x16x32_bf16 v[28:31], a[220:223], v[172:175], v[28:31]\n"
"s_waitcnt lgkmcnt(6)\n"
"ds_read_b128 v[172:175], v199 offset:47104\n"
"v_mfma_f32_16x16x32_bf16 v[32:35], a[192:195], v[160:163], v[32:35]\n"
"s_waitcnt lgkmcnt(5)\n"
"v_mfma_f32_16x16x32_bf16 v[32:35], a[196:199], v[164:167], v[32:35]\n"
"buffer_load_dwordx4 v207, s[24:27], s42 offen offset:3072 nt lds\n"
"v_mfma_f32_16x16x32_bf16 v[36:39], a[200:203], v[160:163], v[36:39]\n"
"v_mfma_f32_16x16x32_bf16 v[36:39], a[204:207], v[164:167], v[36:39]\n"
"v_mfma_f32_16x16x32_bf16 v[40:43], a[208:211], v[160:163], v[40:43]\n"
"s_add_u32 m0, s41, 0x4000\n"
"v_mfma_f32_16x16x32_bf16 v[40:43], a[212:215], v[164:167], v[40:43]\n"
"s_add_u32 s42, s75, 0x1c000\n"
"buffer_load_dwordx4 v208, s[16:19], s42 offen lds\n"
"v_mfma_f32_16x16x32_bf16 v[44:47], a[216:219], v[160:163], v[44:47]\n"
"s_waitcnt vmcnt(14)\n"
"s_add_u32 m0, s40, 0x4200\n"
"ds_read_b128 v[160:163], v198 offset:49152\n"
"ds_read_b128 a[32:35], v200 offset:0\n"
"v_mfma_f32_16x16x32_bf16 v[44:47], a[220:223], v[164:167], v[44:47]\n"
"s_waitcnt lgkmcnt(3)\n"
"ds_read_b128 v[164:167], v199 offset:49152\n"
"ds_read_b128 a[36:39], v201 offset:0\n"
"v_mfma_f32_16x16x32_bf16 v[48:51], a[192:195], v[168:171], v[48:51]\n"
"s_waitcnt lgkmcnt(4)\n"
"ds_read_b128 a[40:43], v202 offset:0\n"
"v_mfma_f32_16x16x32_bf16 v[48:51], a[196:199], v[172:175], v[48:51]\n"
"s_add_u32 s42, s88, 0\n"
"buffer_load_dwordx4 v209, s[20:23], s42 offen lds\n"
"v_mfma_f32_16x16x32_bf16 v[52:55], a[200:203], v[168:171], v[52:55]\n"
"ds_read_b128 a[44:47], v203 offset:0\n"
"v_mfma_f32_16x16x32_bf16 v[52:55], a[204:207], v[172:175], v[52:55]\n"
"v_mfma_f32_16x16x32_bf16 v[56:59], a[208:211], v[168:171], v[56:59]\n"
"v_mfma_f32_16x16x32_bf16 v[56:59], a[212:215], v[172:175], v[56:59]\n"
"buffer_load_dwordx4 v210, s[20:23], s42 offen offset:1056 lds\n"
"v_mfma_f32_16x16x32_bf16 v[60:63], a[216:219], v[168:171], v[60:63]\n"
"s_add_u32 m0, s41, 0x5000\n"
"ds_read_b128 v[168:171], v198 offset:51200\n"
"v_mfma_f32_16x16x32_bf16 v[60:63], a[220:223], v[172:175], v[60:63]\n"
"s_waitcnt lgkmcnt(6)\n"
"ds_read_b128 v[172:175], v199 offset:51200\n"
"v_mfma_f32_16x16x32_bf16 v[64:67], a[192:195], v[160:163], v[64:67]\n"
"s_waitcnt lgkmcnt(5)\n"
"v_mfma_f32_16x16x32_bf16 v[64:67], a[196:199], v[164:167], v[64:67]\n"
"s_add_u32 s42, s76, 0\n"
"buffer_load_dwordx4 v208, s[16:19], s42 offen lds\n"
"v_mfma_f32_16x16x32_bf16 v[68:71], a[200:203], v[160:163], v[68:71]\n"
"s_waitcnt vmcnt(15)\n"
"ds_read_b64_tr_b16 a[226:227], v196 offset:10560\n"
"v_mfma_f32_16x16x32_bf16 v[68:71], a[204:207], v[164:167], v[68:71]\n"
"ds_read_b64_tr_b16 a[234:235], v196 offset:10568\n"
"v_mfma_f32_16x16x32_bf16 v[72:75], a[208:211], v[160:163], v[72:75]\n"
"s_add_u32 m0, s40, 0x2100\n"
"ds_read_b64_tr_b16 a[242:243], v197 offset:10560\n"
"v_mfma_f32_16x16x32_bf16 v[72:75], a[212:215], v[164:167], v[72:75]\n"
"s_add_u32 s42, s88, 0x8000\n"
"buffer_load_dwordx4 v209, s[20:23], s42 offen lds\n"
"v_mfma_f32_16x16x32_bf16 v[76:79], a[216:219], v[160:163], v[76:79]\n"
"ds_read_b128 v[160:163], v198 offset:53248\n"
"ds_read_b64_tr_b16 a[250:251], v197 offset:10568\n"
"v_mfma_f32_16x16x32_bf16 v[76:79], a[220:223], v[164:167], v[76:79]\n"
"s_waitcnt lgkmcnt(6)\n"
"ds_read_b128 v[164:167], v199 offset:53248\n"
"v_mfma_f32_16x16x32_bf16 v[80:83], a[192:195], v[168:171], v[80:83]\n"
"s_waitcnt lgkmcnt(6)\n"
"v_mfma_f32_16x16x32_bf16 v[80:83], a[196:199], v[172:175], v[80:83]\n"
"buffer_load_dwordx4 v210, s[20:23], s42 offen offset:1056 lds\n"
"v_mfma_f32_16x16x32_bf16 v[84:87], a[200:203], v[168:171], v[84:87]\n"
"v_mfma_f32_16x16x32_bf16 v[84:87], a[204:207], v[172:175], v[84:87]\n"
"v_mfma_f32_16x16x32_bf16 v[88:91], a[208:211], v[168:171], v[88:91]\n"
"s_add_u32 m0, s41, 0x6000\n"
"v_mfma_f32_16x16x32_bf16 v[88:91], a[212:215], v[172:175], v[88:91]\n"
"s_add_u32 s42, s76, 0x7000\n"
"buffer_load_dwordx4 v208, s[16:19], s42 offen lds\n"
"v_mfma_f32_16x16x32_bf16 v[92:95], a[216:219], v[168:171], v[92:95]\n"
"s_waitcnt vmcnt(15)\n"
"s_add_u32 m0, s40, 0\n"
"ds_read_b128 v[168:171], v198 offset:55296\n"
"ds_read_b64_tr_b16 a[228:229], v196 offset:12672\n"
"v_mfma_f32_16x16x32_bf16 v[92:95], a[220:223], v[172:175], v[92:95]\n"
"s_waitcnt lgkmcnt(4)\n"
"ds_read_b128 v[172:175], v199 offset:55296\n"
"ds_read_b64_tr_b16 a[236:237], v196 offset:12680\n"
"v_mfma_f32_16x16x32_bf16 v[96:99], a[192:195], v[160:163], v[96:99]\n"
"s_waitcnt lgkmcnt(4)\n"
"ds_read_b64_tr_b16 a[244:245], v197 offset:12672\n"
"v_mfma_f32_16x16x32_bf16 v[96:99], a[196:199], v[164:167], v[96:99]\n"
"s_mov_b32 s42, 0x10000\n"
"buffer_load_dwordx4 v204, s[24:27], s42 offen nt lds\n"
"v_mfma_f32_16x16x32_bf16 v[100:103], a[200:203], v[160:163], v[100:103]\n"
"ds_read_b64_tr_b16 a[252:253], v197 offset:12680\n"
"v_mfma_f32_16x16x32_bf16 v[100:103], a[204:207], v[164:167], v[100:103]\n"
"v_mfma_f32_16x16x32_bf16 v[104:107], a[208:211], v[160:163], v[104:107]\n"
"v_mfma_f32_16x16x32_bf16 v[104:107], a[212:215], v[164:167], v[104:107]\n"
"buffer_load_dwordx4 v205, s[24:27], s42 offen offset:1024 nt lds\n"
"v_mfma_f32_16x16x32_bf16 v[108:111], a[216:219], v[160:163], v[108:111]\n"
"ds_read_b128 v[160:163], v198 offset:57344\n"
"v_mfma_f32_16x16x32_bf16 v[108:111], a[220:223], v[164:167], v[108:111]\n"
"s_waitcnt lgkmcnt(6)\n"
"ds_read_b128 v[164:167], v199 offset:57344\n"
"v_mfma_f32_16x16x32_bf16 v[112:115], a[192:195], v[168:171], v[112:115]\n"
"s_waitcnt lgkmcnt(5)\n"
"v_mfma_f32_16x16x32_bf16 v[112:115], a[196:199], v[172:175], v[112:115]\n"
"buffer_load_dwordx4 v206, s[24:27], s42 offen offset:2048 nt lds\n"
"v_mfma_f32_16x16x32_bf16 v[116:119], a[200:203], v[168:171], v[116:119]\n"
"v_mfma_f32_16x16x32_bf16 v[116:119], a[204:207], v[172:175], v[116:119]\n"
"v_mfma_f32_16x16x32_bf16 v[120:123], a[208:211], v[168:171], v[120:123]\n"
"v_mfma_f32_16x16x32_bf16 v[120:123], a[212:215], v[172:175], v[120:123]\n"
"buffer_load_dwordx4 v207, s[24:27], s42 offen offset:3072 nt lds\n"
"v_mfma_f32_16x16x32_bf16 v[124:127], a[216:219], v[168:171], v[124:127]\n"
"s_waitcnt vmcnt(15)\n"
"s_add_u32 m0, s41, 0x7000\n"
"ds_read_b128 v[168:171], v198 offset:59392\n"
"ds_read_b64_tr_b16 a[230:231], v196 offset:14784\n"
"v_mfma_f32_16x16x32_bf16 v[124:127], a[220:223], v[172:175], v[124:127]\n"
"s_waitcnt lgkmcnt(3)\n"
"ds_read_b128 v[172:175], v199 offset:59392\n"
"ds_read_b64_tr_b16 a[238:239], v196 offset:14792\n"
"v_mfma_f32_16x16x32_bf16 v[128:131], a[192:195], v[160:163], v[128:131]\n"
"s_waitcnt lgkmcnt(4)\n"
"ds_read_b64_tr_b16 a[246:247], v197 offset:14784\n"
"v_mfma_f32_16x16x32_bf16 v[128:131], a[196:199], v[164:167], v[128:131]\n"
"s_add_u32 s42, s76, 0xe000\n"
"buffer_load_dwordx4 v208, s[16:19], s42 offen lds\n"
"v_mfma_f32_16x16x32_bf16 v[132:135], a[200:203], v[160:163], v[132:135]\n"
"ds_read_b64_tr_b16 a[254:255], v197 offset:14792\n"
"v_mfma_f32_16x16x32_bf16 v[132:135], a[204:207], v[164:167], v[132:135]\n"
"v_add_u32 v198, 0xffffb000, v198\n"
"v_mfma_f32_16x16x32_bf16 v[136:139], a[208:211], v[160:163], v[136:139]\n"
"s_add_u32 m0, s40, 0x3180\n"
"v_add_u32 v199, 0xffffb000, v199\n"
"v_mfma_f32_16x16x32_bf16 v[136:139], a[212:215], v[164:167], v[136:139]\n"
"s_add_u32 s42, s88, 0x40000\n"
"buffer_load_dwordx4 v209, s[20:23], s42 offen lds\n"
"v_mfma_f32_16x16x32_bf16 v[140:143], a[216:219], v[160:163], v[140:143]\n"
"s_waitcnt vmcnt(12)\n"
"s_barrier\n"
"ds_read_b128 v[160:163], v198 offset:0\n"
"v_mfma_f32_16x16x32_bf16 v[140:143], a[220:223], v[164:167], v[140:143]\n"
"s_waitcnt lgkmcnt(6)\n"
"ds_read_b128 v[164:167], v199 offset:0\n"
"v_mfma_f32_16x16x32_bf16 v[144:147], a[192:195], v[168:171], v[144:147]\n"
"s_waitcnt lgkmcnt(5)\n"
"v_mfma_f32_16x16x32_bf16 v[144:147], a[196:199], v[172:175], v[144:147]\n"
"buffer_load_dwordx4 v210, s[20:23], s42 offen offset:1056 lds\n"
"v_mfma_f32_16x16x32_bf16 v[148:151], a[200:203], v[168:171], v[148:151]\n"
"ds_read_b128 a[48:51], v200 offset:4224\n"
"v_mfma_f32_16x16x32_bf16 v[148:151], a[204:207], v[172:175], v[148:151]\n"
"ds_read_b128 a[52:55], v201 offset:4224\n"
"v_mfma_f32_16x16x32_bf16 v[152:155], a[208:211], v[168:171], v[152:155]\n"
"s_add_u32 m0, s41, 0x8000\n"
"ds_read_b128 a[56:59], v202 offset:4224\n"
"v_mfma_f32_16x16x32_bf16 v[152:155], a[212:215], v[172:175], v[152:155]\n"
"s_add_u32 s42, s76, 0x15000\n"
"buffer_load_dwordx4 v208, s[16:19], s42 offen lds\n"
"v_mfma_f32_16x16x32_bf16 v[156:159], a[216:219], v[168:171], v[156:159]\n"
"s_add_u32 m0, s40, 0x39c0\n"
"ds_read_b128 v[168:171], v198 offset:2048\n"
"ds_read_b128 a[60:63], v203 offset:4224\n"
"v_mfma_f32_16x16x32_bf16 v[156:159], a[220:223], v[172:175], v[156:159]\n"
"s_waitcnt lgkmcnt(6)\n"
"ds_read_b128 v[172:175], v199 offset:2048\n"
"v_mfma_f32_16x16x32_bf16 v[0:3], a[224:227], v[160:163], v[0:3]\n"
"s_waitcnt lgkmcnt(6)\n"
"v_mfma_f32_16x16x32_bf16 v[0:3], a[228:231], v[164:167], v[0:3]\n"
"s_add_u32 s42, s88, 0x48000\n"
"buffer_load_dwordx4 v209, s[20:23], s42 offen lds\n"
"v_mfma_f32_16x16x32_bf16 v[4:7], a[232:235], v[160:163], v[4:7]\n"
"v_mfma_f32_16x16x32_bf16 v[4:7], a[236:239], v[164:167], v[4:7]\n"
"v_mfma_f32_16x16x32_bf16 v[8:11], a[240:243], v[160:163], v[8:11]\n"
"v_mfma_f32_16x16x32_bf16 v[8:11], a[244:247], v[164:167], v[8:11]\n"
"buffer_load_dwordx4 v210, s[20:23], s42 offen offset:1056 lds\n"
"v_mfma_f32_16x16x32_bf16 v[12:15], a[248:251], v[160:163], v[12:15]\n"
"s_add_u32 m0, s41, 0x9000\n"
"ds_read_b128 v[160:163], v198 offset:4096\n"
"v_mfma_f32_16x16x32_bf16 v[12:15], a[252:255], v[164:167], v[12:15]\n"
"s_waitcnt lgkmcnt(3)\n"
"ds_read_b128 v[164:167], v199 offset:4096\n"
"v_mfma_f32_16x16x32_bf16 v[16:19], a[224:227], v[168:171], v[16:19]\n"
"s_waitcnt lgkmcnt(2)\n"
"v_mfma_f32_16x16x32_bf16 v[16:19], a[228:231], v[172:175], v[16:19]\n"
"s_add_u32 s42, s76, 0x1c000\n"
"buffer_load_dwordx4 v208, s[16:19], s42 offen lds\n"
"v_mfma_f32_16x16x32_bf16 v[20:23], a[232:235], v[168:171], v[20:23]\n"
"s_waitcnt vmcnt(15)\n"
"ds_read_b64_tr_b16 a[128:129], v196 offset:16896\n"
"v_mfma_f32_16x16x32_bf16 v[20:23], a[236:239], v[172:175], v[20:23]\n"
"ds_read_b64_tr_b16 a[136:137], v196 offset:16904\n"
"v_mfma_f32_16x16x32_bf16 v[24:27], a[240:243], v[168:171], v[24:27]\n"
"s_add_u32 m0, s40, 0x1080\n"
"ds_read_b64_tr_b16 a[144:145], v197 offset:16896\n"
"v_mfma_f32_16x16x32_bf16 v[24:27], a[244:247], v[172:175], v[24:27]\n"
"s_mov_b32 s42, 0x10100\n"
"buffer_load_dwordx4 v204, s[24:27], s42 offen nt lds\n"
"v_mfma_f32_16x16x32_bf16 v[28:31], a[248:251], v[168:171], v[28:31]\n"
"ds_read_b128 v[168:171], v198 offset:6144\n"
"ds_read_b64_tr_b16 a[152:153], v197 offset:16904\n"
"v_mfma_f32_16x16x32_bf16 v[28:31], a[252:255], v[172:175], v[28:31]\n"
"s_waitcnt lgkmcnt(6)\n"
"ds_read_b128 v[172:175], v199 offset:6144\n"
"v_mfma_f32_16x16x32_bf16 v[32:35], a[224:227], v[160:163], v[32:35]\n"
"s_waitcnt lgkmcnt(6)\n"
"v_mfma_f32_16x16x32_bf16 v[32:35], a[228:231], v[164:167], v[32:35]\n"
"buffer_load_dwordx4 v205, s[24:27], s42 offen offset:1024 nt lds\n"
"v_mfma_f32_16x16x32_bf16 v[36:39], a[232:235], v[160:163], v[36:39]\n"
"v_mfma_f32_16x16x32_bf16 v[36:39], a[236:239], v[164:167], v[36:39]\n"
"v_mfma_f32_16x16x32_bf16 v[40:43], a[240:243], v[160:163], v[40:43]\n"
"v_mfma_f32_16x16x32_bf16 v[40:43], a[244:247], v[164:167], v[40:43]\n"
"buffer_load_dwordx4 v206, s[24:27], s42 offen offset:2048 nt lds\n"
"v_mfma_f32_16x16x32_bf16 v[44:47], a[248:251], v[160:163], v[44:47]\n"
"s_waitcnt vmcnt(15)\n"
"ds_read_b128 v[160:163], v198 offset:8192\n"
"ds_read_b64_tr_b16 a[130:131], v196 offset:8448\n"
"v_mfma_f32_16x16x32_bf16 v[44:47], a[252:255], v[164:167], v[44:47]\n"
"s_waitcnt lgkmcnt(4)\n"
"ds_read_b128 v[164:167], v199 offset:8192\n"
"ds_read_b64_tr_b16 a[138:139], v196 offset:8456\n"
"v_mfma_f32_16x16x32_bf16 v[48:51], a[224:227], v[168:171], v[48:51]\n"
"s_waitcnt lgkmcnt(4)\n"
"ds_read_b64_tr_b16 a[146:147], v197 offset:8448\n"
"v_mfma_f32_16x16x32_bf16 v[48:51], a[228:231], v[172:175], v[48:51]\n"
"buffer_load_dwordx4 v207, s[24:27], s42 offen offset:3072 nt lds\n"
"v_mfma_f32_16x16x32_bf16 v[52:55], a[232:235], v[168:171], v[52:55]\n"
"ds_read_b64_tr_b16 a[154:155], v197 offset:8456\n"
"v_mfma_f32_16x16x32_bf16 v[52:55], a[236:239], v[172:175], v[52:55]\n"
"v_mfma_f32_16x16x32_bf16 v[56:59], a[240:243], v[168:171], v[56:59]\n"
"s_add_u32 m0, s41, 0xa000\n"
"v_mfma_f32_16x16x32_bf16 v[56:59], a[244:247], v[172:175], v[56:59]\n"
"s_add_u32 s42, s77, 0\n"
"buffer_load_dwordx4 v208, s[16:19], s42 offen lds\n"
"v_mfma_f32_16x16x32_bf16 v[60:63], a[248:251], v[168:171], v[60:63]\n"
"s_add_u32 m0, s40, 0x4200\n"
"ds_read_b128 v[168:171], v198 offset:10240\n"
"v_mfma_f32_16x16x32_bf16 v[60:63], a[252:255], v[172:175], v[60:63]\n"
"s_waitcnt lgkmcnt(6)\n"
"ds_read_b128 v[172:175], v199 offset:10240\n"
"v_mfma_f32_16x16x32_bf16 v[64:67], a[224:227], v[160:163], v[64:67]\n"
"s_waitcnt lgkmcnt(5)\n"
"v_mfma_f32_16x16x32_bf16 v[64:67], a[228:231], v[164:167], v[64:67]\n"
"s_add_u32 s42, s89, 0\n"
"buffer_load_dwordx4 v209, s[20:23], s42 offen lds\n"
"v_mfma_f32_16x16x32_bf16 v[68:71], a[232:235], v[160:163], v[68:71]\n"
"v_mfma_f32_16x16x32_bf16 v[68:71], a[236:239], v[164:167], v[68:71]\n"
"v_mfma_f32_16x16x32_bf16 v[72:75], a[240:243], v[160:163], v[72:75]\n"
"v_mfma_f32_16x16x32_bf16 v[72:75], a[244:247], v[164:167], v[72:75]\n"
"buffer_load_dwordx4 v210, s[20:23], s42 offen offset:1056 lds\n"
"v_mfma_f32_16x16x32_bf16 v[76:79], a[248:251], v[160:163], v[76:79]\n"
"s_waitcnt vmcnt(14)\n"
"s_add_u32 m0, s41, 0xb000\n"
"ds_read_b128 v[160:163], v198 offset:12288\n"
"ds_read_b128 a[64:67], v200 offset:0\n"
"v_mfma_f32_16x16x32_bf16 v[76:79], a[252:255], v[164:167], v[76:79]\n"
"s_waitcnt lgkmcnt(3)\n"
"ds_read_b128 v[164:167], v199 offset:12288\n"
"ds_read_b128 a[68:71], v201 offset:0\n"
"v_mfma_f32_16x16x32_bf16 v[80:83], a[224:227], v[168:171], v[80:83]\n"
"s_waitcnt lgkmcnt(4)\n"
"ds_read_b128 a[72:75], v202 offset:0\n"
"v_mfma_f32_16x16x32_bf16 v[80:83], a[228:231], v[172:175], v[80:83]\n"
"s_add_u32 s42, s77, 0x7000\n"
"buffer_load_dwordx4 v208, s[16:19], s42 offen lds\n"
"v_mfma_f32_16x16x32_bf16 v[84:87], a[232:235], v[168:171], v[84:87]\n"
"ds_read_b128 a[76:79], v203 offset:0\n"
"v_mfma_f32_16x16x32_bf16 v[84:87], a[236:239], v[172:175], v[84:87]\n"
"v_mfma_f32_16x16x32_bf16 v[88:91], a[240:243], v[168:171], v[88:91]\n"
"s_add_u32 m0, s40, 0x2100\n"
"v_mfma_f32_16x16x32_bf16 v[88:91], a[244:247], v[172:175], v[88:91]\n"
"s_add_u32 s42, s89, 0x8000\n"
"buffer_load_dwordx4 v209, s[20:23], s42 offen lds\n"
"v_mfma_f32_16x16x32_bf16 v[92:95], a[248:251], v[168:171], v[92:95]\n"
"ds_read_b128 v[168:171], v198 offset:14336\n"
"v_mfma_f32_16x16x32_bf16 v[92:95], a[252:255], v[172:175], v[92:95]\n"
"s_waitcnt lgkmcnt(6)\n"
"ds_read_b128 v[172:175], v199 offset:14336\n"
"v_mfma_f32_16x16x32_bf16 v[96:99], a[224:227], v[160:163], v[96:99]\n"
"s_waitcnt lgkmcnt(5)\n"
"v_mfma_f32_16x16x32_bf16 v[96:99], a[228:231], v[164:167], v[96:99]\n"
"buffer_load_dwordx4 v210, s[20:23], s42 offen offset:1056 lds\n"
"v_mfma_f32_16x16x32_bf16 v[100:103], a[232:235], v[160:163], v[100:103]\n"
"v_mfma_f32_16x16x32_bf16 v[100:103], a[236:239], v[164:167], v[100:103]\n"
"v_mfma_f32_16x16x32_bf16 v[104:107], a[240:243], v[160:163], v[104:107]\n"
"s_add_u32 m0, s41, 0xc000\n"
"v_mfma_f32_16x16x32_bf16 v[104:107], a[244:247], v[164:167], v[104:107]\n"
"s_add_u32 s42, s77, 0xe000\n"
"buffer_load_dwordx4 v208, s[16:19], s42 offen lds\n"
"v_mfma_f32_16x16x32_bf16 v[108:111], a[248:251], v[160:163], v[108:111]\n"
"s_waitcnt vmcnt(15)\n"
"s_add_u32 m0, s40, 0x2940\n"
"ds_read_b128 v[160:163], v198 offset:16384\n"
"ds_read_b64_tr_b16 a[132:133], v196 offset:12672\n"
"v_mfma_f32_16x16x32_bf16 v[108:111], a[252:255], v[164:167], v[108:111]\n"
"s_waitcnt lgkmcnt(3)\n"
"ds_read_b128 v[164:167], v199 offset:16384\n"
"ds_read_b64_tr_b16 a[140:141], v196 offset:12680\n"
"v_mfma_f32_16x16x32_bf16 v[112:115], a[224:227], v[168:171], v[112:115]\n"
"s_waitcnt lgkmcnt(4)\n"
"ds_read_b64_tr_b16 a[148:149], v197 offset:12672\n"
"v_mfma_f32_16x16x32_bf16 v[112:115], a[228:231], v[172:175], v[112:115]\n"
"s_add_u32 s42, s89, 0x40000\n"
"buffer_load_dwordx4 v209, s[20:23], s42 offen lds\n"
"v_mfma_f32_16x16x32_bf16 v[116:119], a[232:235], v[168:171], v[116:119]\n"
"ds_read_b64_tr_b16 a[156:157], v197 offset:12680\n"
"v_mfma_f32_16x16x32_bf16 v[116:119], a[236:239], v[172:175], v[116:119]\n"
"v_mfma_f32_16x16x32_bf16 v[120:123], a[240:243], v[168:171], v[120:123]\n"
"v_mfma_f32_16x16x32_bf16 v[120:123], a[244:247], v[172:175], v[120:123]\n"
"buffer_load_dwordx4 v210, s[20:23], s42 offen offset:1056 lds\n"
"v_mfma_f32_16x16x32_bf16 v[124:127], a[248:251], v[168:171], v[124:127]\n"
"s_waitcnt vmcnt(14)\n"
"s_add_u32 m0, s41, 0xd000\n"
"ds_read_b128 v[168:171], v198 offset:18432\n"
"ds_read_b64_tr_b16 a[134:135], v196 offset:14784\n"
"v_mfma_f32_16x16x32_bf16 v[124:127], a[252:255], v[172:175], v[124:127]\n"
"s_waitcnt lgkmcnt(7)\n"
"ds_read_b128 v[172:175], v199 offset:18432\n"
"ds_read_b64_tr_b16 a[142:143], v196 offset:14792\n"
"v_mfma_f32_16x16x32_bf16 v[128:131], a[224:227], v[160:163], v[128:131]\n"
"s_waitcnt lgkmcnt(7)\n"
"ds_read_b64_tr_b16 a[150:151], v197 offset:14784\n"
"v_mfma_f32_16x16x32_bf16 v[128:131], a[228:231], v[164:167], v[128:131]\n"
"s_add_u32 s42, s77, 0x15000\n"
"buffer_load_dwordx4 v208, s[16:19], s42 offen lds\n"
"v_mfma_f32_16x16x32_bf16 v[132:135], a[232:235], v[160:163], v[132:135]\n"
"ds_read_b64_tr_b16 a[158:159], v197 offset:14792\n"
"v_mfma_f32_16x16x32_bf16 v[132:135], a[236:239], v[164:167], v[132:135]\n"
"v_add_u32 v198, 0x5000, v198\n"
"v_mfma_f32_16x16x32_bf16 v[136:139], a[240:243], v[160:163], v[136:139]\n"
"s_add_u32 m0, s40, 0\n"
"v_add_u32 v199, 0x5000, v199\n"
"v_mfma_f32_16x16x32_bf16 v[136:139], a[244:247], v[164:167], v[136:139]\n"
"s_mov_b32 s42, 0x12000\n"
"buffer_load_dwordx4 v204, s[24:27], s42 offen nt lds\n"
"v_mfma_f32_16x16x32_bf16 v[140:143], a[248:251], v[160:163], v[140:143]\n"
"s_waitcnt vmcnt(15)\n"
"s_barrier\n"
"ds_read_b128 v[160:163], v198 offset:0\n"
"v_mfma_f32_16x16x32_bf16 v[140:143], a[252:255], v[164:167], v[140:143]\n"
"s_waitcnt lgkmcnt(6)\n"
"ds_read_b128 v[164:167], v199 offset:0\n"
"v_mfma_f32_16x16x32_bf16 v[144:147], a[224:227], v[168:171], v[144:147]\n"
"s_waitcnt lgkmcnt(5)\n"
"v_mfma_f32_16x16x32_bf16 v[144:147], a[228:231], v[172:175], v[144:147]\n"
"buffer_load_dwordx4 v205, s[24:27], s42 offen offset:1024 nt lds\n"
"v_mfma_f32_16x16x32_bf16 v[148:151], a[232:235], v[168:171], v[148:151]\n"
"v_mfma_f32_16x16x32_bf16 v[148:151], a[236:239], v[172:175], v[148:151]\n"
"v_mfma_f32_16x16x32_bf16 v[152:155], a[240:243], v[168:171], v[152:155]\n"
"v_mfma_f32_16x16x32_bf16 v[152:155], a[244:247], v[172:175], v[152:155]\n"
"buffer_load_dwordx4 v206, s[24:27], s42 offen offset:2048 nt lds\n"
"v_mfma_f32_16x16x32_bf16 v[156:159], a[248:251], v[168:171], v[156:159]\n"
"ds_read_b128 v[168:171], v198 offset:2048\n"
"v_mfma_f32_16x16x32_bf16 v[156:159], a[252:255], v[172:175], v[156:159]\n"
"s_waitcnt lgkmcnt(2)\n"
"ds_read_b128 v[172:175], v199 offset:2048\n"
"v_mfma_f32_16x16x32_bf16 v[0:3], a[128:131], v[160:163], v[0:3]\n"
"s_waitcnt lgkmcnt(2)\n"
"v_mfma_f32_16x16x32_bf16 v[0:3], a[132:135], v[164:167], v[0:3]\n"
"buffer_load_dwordx4 v207, s[24:27], s42 offen offset:3072 nt lds\n"
"v_mfma_f32_16x16x32_bf16 v[4:7], a[136:139], v[160:163], v[4:7]\n"
"s_waitcnt vmcnt(14)\n"
"ds_read_b128 a[80:83], v200 offset:4224\n"
"v_mfma_f32_16x16x32_bf16 v[4:7], a[140:143], v[164:167], v[4:7]\n"
"ds_read_b128 a[84:87], v201 offset:4224\n"
"v_mfma_f32_16x16x32_bf16 v[8:11], a[144:147], v[160:163], v[8:11]\n"
"s_add_u32 m0, s41, 0xe000\n"
"ds_read_b128 a[88:91], v202 offset:4224\n"
"v_mfma_f32_16x16x32_bf16 v[8:11], a[148:151], v[164:167], v[8:11]\n"
"s_add_u32 s42, s77, 0x1c000\n"
"buffer_load_dwordx4 v208, s[16:19], s42 offen lds\n"
"v_mfma_f32_16x16x32_bf16 v[12:15], a[152:155], v[160:163], v[12:15]\n"
"s_add_u32 m0, s40, 0x39c0\n"
"ds_read_b128 v[160:163], v198 offset:4096\n"
"ds_read_b128 a[92:95], v203 offset:4224\n"
"v_mfma_f32_16x16x32_bf16 v[12:15], a[156:159], v[164:167], v[12:15]\n"
"s_waitcnt lgkmcnt(6)\n"
"ds_read_b128 v[164:167], v199 offset:4096\n"
"v_mfma_f32_16x16x32_bf16 v[16:19], a[128:131], v[168:171], v[16:19]\n"
"s_waitcnt lgkmcnt(6)\n"
"v_mfma_f32_16x16x32_bf16 v[16:19], a[132:135], v[172:175], v[16:19]\n"
"s_add_u32 s42, s89, 0x48000\n"
"buffer_load_dwordx4 v209, s[20:23], s42 offen lds\n"
"v_mfma_f32_16x16x32_bf16 v[20:23], a[136:139], v[168:171], v[20:23]\n"
"v_mfma_f32_16x16x32_bf16 v[20:23], a[140:143], v[172:175], v[20:23]\n"
"v_mfma_f32_16x16x32_bf16 v[24:27], a[144:147], v[168:171], v[24:27]\n"
"v_mfma_f32_16x16x32_bf16 v[24:27], a[148:151], v[172:175], v[24:27]\n"
"buffer_load_dwordx4 v210, s[20:23], s42 offen offset:1056 lds\n"
"v_mfma_f32_16x16x32_bf16 v[28:31], a[152:155], v[168:171], v[28:31]\n"
"s_add_u32 m0, s41, 0xf000\n"
"ds_read_b128 v[168:171], v198 offset:6144\n"
"v_mfma_f32_16x16x32_bf16 v[28:31], a[156:159], v[172:175], v[28:31]\n"
"s_waitcnt lgkmcnt(3)\n"
"ds_read_b128 v[172:175], v199 offset:6144\n"
"v_mfma_f32_16x16x32_bf16 v[32:35], a[128:131], v[160:163], v[32:35]\n"
"s_waitcnt lgkmcnt(2)\n"
"v_mfma_f32_16x16x32_bf16 v[32:35], a[132:135], v[164:167], v[32:35]\n"
"s_add_u32 s42, s78, 0\n"
"buffer_load_dwordx4 v208, s[16:19], s42 offen lds\n"
"v_mfma_f32_16x16x32_bf16 v[36:39], a[136:139], v[160:163], v[36:39]\n"
"s_waitcnt vmcnt(15)\n"
"ds_read_b64_tr_b16 a[160:161], v196 offset:16896\n"
"v_mfma_f32_16x16x32_bf16 v[36:39], a[140:143], v[164:167], v[36:39]\n"
"ds_read_b64_tr_b16 a[168:169], v196 offset:16904\n"
"v_mfma_f32_16x16x32_bf16 v[40:43], a[144:147], v[160:163], v[40:43]\n"
"s_add_u32 m0, s40, 0x3180\n"
"ds_read_b64_tr_b16 a[176:177], v197 offset:16896\n"
"v_mfma_f32_16x16x32_bf16 v[40:43], a[148:151], v[164:167], v[40:43]\n"
"s_add_u32 s42, s90, 0\n"
"buffer_load_dwordx4 v209, s[20:23], s42 offen lds\n"
"v_mfma_f32_16x16x32_bf16 v[44:47], a[152:155], v[160:163], v[44:47]\n"
"ds_read_b128 v[160:163], v198 offset:8192\n"
"ds_read_b64_tr_b16 a[184:185], v197 offset:16904\n"
"v_mfma_f32_16x16x32_bf16 v[44:47], a[156:159], v[164:167], v[44:47]\n"
"s_waitcnt lgkmcnt(6)\n"
"ds_read_b128 v[164:167], v199 offset:8192\n"
"v_mfma_f32_16x16x32_bf16 v[48:51], a[128:131], v[168:171], v[48:51]\n"
"s_waitcnt lgkmcnt(6)\n"
"v_mfma_f32_16x16x32_bf16 v[48:51], a[132:135], v[172:175], v[48:51]\n"
"buffer_load_dwordx4 v210, s[20:23], s42 offen offset:1056 lds\n"
"v_mfma_f32_16x16x32_bf16 v[52:55], a[136:139], v[168:171], v[52:55]\n"
"v_mfma_f32_16x16x32_bf16 v[52:55], a[140:143], v[172:175], v[52:55]\n"
"v_mfma_f32_16x16x32_bf16 v[56:59], a[144:147], v[168:171], v[56:59]\n"
"s_add_u32 m0, s41, 0x10000\n"
"v_mfma_f32_16x16x32_bf16 v[56:59], a[148:151], v[172:175], v[56:59]\n"
"s_add_u32 s42, s78, 0x7000\n"
"buffer_load_dwordx4 v208, s[16:19], s42 offen lds\n"
"v_mfma_f32_16x16x32_bf16 v[60:63], a[152:155], v[168:171], v[60:63]\n"
"s_waitcnt vmcnt(15)\n"
"s_add_u32 m0, s40, 0x1080\n"
"ds_read_b128 v[168:171], v198 offset:10240\n"
"ds_read_b64_tr_b16 a[162:163], v196 offset:8448\n"
"v_mfma_f32_16x16x32_bf16 v[60:63], a[156:159], v[172:175], v[60:63]\n"
"s_waitcnt lgkmcnt(4)\n"
"ds_read_b128 v[172:175], v199 offset:10240\n"
"ds_read_b64_tr_b16 a[170:171], v196 offset:8456\n"
"v_mfma_f32_16x16x32_bf16 v[64:67], a[128:131], v[160:163], v[64:67]\n"
"s_waitcnt lgkmcnt(4)\n"
"ds_read_b64_tr_b16 a[178:179], v197 offset:8448\n"
"v_mfma_f32_16x16x32_bf16 v[64:67], a[132:135], v[164:167], v[64:67]\n"
"s_mov_b32 s42, 0x12100\n"
"buffer_load_dwordx4 v204, s[24:27], s42 offen nt lds\n"
"v_mfma_f32_16x16x32_bf16 v[68:71], a[136:139], v[160:163], v[68:71]\n"
"ds_read_b64_tr_b16 a[186:187], v197 offset:8456\n"
"v_mfma_f32_16x16x32_bf16 v[68:71], a[140:143], v[164:167], v[68:71]\n"
"v_mfma_f32_16x16x32_bf16 v[72:75], a[144:147], v[160:163], v[72:75]\n"
"v_mfma_f32_16x16x32_bf16 v[72:75], a[148:151], v[164:167], v[72:75]\n"
"buffer_load_dwordx4 v205, s[24:27], s42 offen offset:1024 nt lds\n"
"v_mfma_f32_16x16x32_bf16 v[76:79], a[152:155], v[160:163], v[76:79]\n"
"ds_read_b128 v[160:163], v198 offset:12288\n"
"v_mfma_f32_16x16x32_bf16 v[76:79], a[156:159], v[164:167], v[76:79]\n"
"s_waitcnt lgkmcnt(6)\n"
"ds_read_b128 v[164:167], v199 offset:12288\n"
"v_mfma_f32_16x16x32_bf16 v[80:83], a[128:131], v[168:171], v[80:83]\n"
"s_waitcnt lgkmcnt(5)\n"
"v_mfma_f32_16x16x32_bf16 v[80:83], a[132:135], v[172:175], v[80:83]\n"
"buffer_load_dwordx4 v206, s[24:27], s42 offen offset:2048 nt lds\n"
"v_mfma_f32_16x16x32_bf16 v[84:87], a[136:139], v[168:171], v[84:87]\n"
"s_waitcnt vmcnt(15)\n"
"ds_read_b64_tr_b16 a[164:165], v196 offset:10560\n"
"v_mfma_f32_16x16x32_bf16 v[84:87], a[140:143], v[172:175], v[84:87]\n"
"ds_read_b64_tr_b16 a[172:173], v196 offset:10568\n"
"v_mfma_f32_16x16x32_bf16 v[88:91], a[144:147], v[168:171], v[88:91]\n"
"ds_read_b64_tr_b16 a[180:181], v197 offset:10560\n"
"v_mfma_f32_16x16x32_bf16 v[88:91], a[148:151], v[172:175], v[88:91]\n"
"buffer_load_dwordx4 v207, s[24:27], s42 offen offset:3072 nt lds\n"
"v_mfma_f32_16x16x32_bf16 v[92:95], a[152:155], v[168:171], v[92:95]\n"
"s_add_u32 m0, s40, 0x2100\n"
"ds_read_b128 v[168:171], v198 offset:14336\n"
"ds_read_b64_tr_b16 a[188:189], v197 offset:10568\n"
"v_mfma_f32_16x16x32_bf16 v[92:95], a[156:159], v[172:175], v[92:95]\n"
".Lpad1_%=:\n"
"s_cbranch_vccnz .Lpad5_%=\n"
"s_waitcnt lgkmcnt(6)\n"
"ds_read_b128 v[172:175], v199 offset:14336\n"
"v_mfma_f32_16x16x32_bf16 v[96:99], a[128:131], v[160:163], v[96:99]\n"
"s_waitcnt lgkmcnt(6)\n"
"v_mfma_f32_16x16x32_bf16 v[96:99], a[132:135], v[164:167], v[96:99]\n"
"s_add_u32 s42, s90, 0x8000\n"
"buffer_load_dwordx4 v209, s[20:23], s42 offen lds\n"
"v_mfma_f32_16x16x32_bf16 v[100:103], a[136:139], v[160:163], v[100:103]\n"
"v_mfma_f32_16x16x32_bf16 v[100:103], a[140:143], v[164:167], v[100:103]\n"
"v_mfma_f32_16x16x32_bf16 v[104:107], a[144:147], v[160:163], v[104:107]\n"
"v_mfma_f32_16x16x32_bf16 v[104:107], a[148:151], v[164:167], v[104:107]\n"
"buffer_load_dwordx4 v210, s[20:23], s42 offen offset:1056 lds\n"
"v_mfma_f32_16x16x32_bf16 v[108:111], a[152:155], v[160:163], v[108:111]\n"
"s_add_u32 m0, s41, 0x11000\n"
"ds_read_b128 v[160:163], v198 offset:16384\n"
"v_mfma_f32_16x16x32_bf16 v[108:111], a[156:159], v[164:167], v[108:111]\n"
"s_waitcnt lgkmcnt(3)\n"
"ds_read_b128 v[164:167], v199 offset:16384\n"
"v_mfma_f32_16x16x32_bf16 v[112:115], a[128:131], v[168:171], v[112:115]\n"
"s_waitcnt lgkmcnt(2)\n"
"v_mfma_f32_16x16x32_bf16 v[112:115], a[132:135], v[172:175], v[112:115]\n"
"s_add_u32 s42, s78, 0xe000\n"
"buffer_load_dwordx4 v208, s[16:19], s42 offen lds\n"
"v_mfma_f32_16x16x32_bf16 v[116:119], a[136:139], v[168:171], v[116:119]\n"
"s_waitcnt vmcnt(14)\n"
"ds_read_b128 a[96:99], v200 offset:0\n"
"v_mfma_f32_16x16x32_bf16 v[116:119], a[140:143], v[172:175], v[116:119]\n"
"ds_read_b128 a[100:103], v201 offset:0\n"
"v_mfma_f32_16x16x32_bf16 v[120:123], a[144:147], v[168:171], v[120:123]\n"
"s_add_u32 m0, s41, 0x12000\n"
"ds_read_b128 a[104:107], v202 offset:0\n"
"v_mfma_f32_16x16x32_bf16 v[120:123], a[148:151], v[172:175], v[120:123]\n"
"s_add_u32 s42, s78, 0x15000\n"
"buffer_load_dwordx4 v208, s[16:19], s42 offen lds\n"
"v_mfma_f32_16x16x32_bf16 v[124:127], a[152:155], v[168:171], v[124:127]\n"
"s_add_u32 m0, s40, 0x2940\n"
"ds_read_b128 v[168:171], v198 offset:18432\n"
"ds_read_b128 a[108:111], v203 offset:0\n"
"v_mfma_f32_16x16x32_bf16 v[124:127], a[156:159], v[172:175], v[124:127]\n"
"s_waitcnt vmcnt(12) lgkmcnt(6)\n"
"ds_read_b128 v[172:175], v199 offset:18432\n"
"ds_read_b64_tr_b16 a[166:167], v196 offset:14784\n"
"v_mfma_f32_16x16x32_bf16 v[128:131], a[128:131], v[160:163], v[128:131]\n"
"s_waitcnt lgkmcnt(7)\n"
"ds_read_b64_tr_b16 a[174:175], v196 offset:14792\n"
"v_mfma_f32_16x16x32_bf16 v[128:131], a[132:135], v[164:167], v[128:131]\n"
"s_add_u32 s42, s90, 0x40000\n"
"buffer_load_dwordx4 v209, s[20:23], s42 offen lds\n"
"v_mfma_f32_16x16x32_bf16 v[132:135], a[136:139], v[160:163], v[132:135]\n"
"ds_read_b64_tr_b16 a[182:183], v197 offset:14784\n"
"v_mfma_f32_16x16x32_bf16 v[132:135], a[140:143], v[164:167], v[132:135]\n"
"ds_read_b64_tr_b16 a[190:191], v197 offset:14792\n"
"v_mfma_f32_16x16x32_bf16 v[136:139], a[144:147], v[160:163], v[136:139]\n"
"v_mfma_f32_16x16x32_bf16 v[136:139], a[148:151], v[164:167], v[136:139]\n"
"buffer_load_dwordx4 v210, s[20:23], s42 offen offset:1056 lds\n"
"v_mfma_f32_16x16x32_bf16 v[140:143], a[152:155], v[160:163], v[140:143]\n"
"s_add_u32 m0, s41, 0x13000\n"
"s_barrier\n"
"ds_read_b128 v[160:163], v198 offset:20480\n"
"v_mfma_f32_16x16x32_bf16 v[140:143], a[156:159], v[164:167], v[140:143]\n"
"s_waitcnt lgkmcnt(7)\n"
"ds_read_b128 v[164:167], v199 offset:20480\n"
"v_mfma_f32_16x16x32_bf16 v[144:147], a[128:131], v[168:171], v[144:147]\n"
"s_waitcnt lgkmcnt(6)\n"
"v_mfma_f32_16x16x32_bf16 v[144:147], a[132:135], v[172:175], v[144:147]\n"
"s_add_u32 s42, s78, 0x1c000\n"
"buffer_load_dwordx4 v208, s[16:19], s42 offen lds\n"
"v_mfma_f32_16x16x32_bf16 v[148:151], a[136:139], v[168:171], v[148:151]\n"
"v_mfma_f32_16x16x32_bf16 v[148:151], a[140:143], v[172:175], v[148:151]\n"
"v_mfma_f32_16x16x32_bf16 v[152:155], a[144:147], v[168:171], v[152:155]\n"
"s_add_u32 m0, s40, 0x39c0\n"
"v_mfma_f32_16x16x32_bf16 v[152:155], a[148:151], v[172:175], v[152:155]\n"
"s_waitcnt lgkmcnt(2)\n"
"s_add_u32 s42, s90, 0x48000\n"
"buffer_load_dwordx4 v209, s[20:23], s42 offen lds\n"
"v_mfma_f32_16x16x32_bf16 v[156:159], a[152:155], v[168:171], v[156:159]\n"
"ds_read_b128 v[168:171], v198 offset:22528\n"
"v_mfma_f32_16x16x32_bf16 v[156:159], a[156:159], v[172:175], v[156:159]\n"
"s_waitcnt lgkmcnt(2)\n"
"ds_read_b128 v[172:175], v199 offset:22528\n"
"v_mfma_f32_16x16x32_bf16 v[0:3], a[160:163], v[160:163], v[0:3]\n"
"s_waitcnt lgkmcnt(2)\n"
"v_mfma_f32_16x16x32_bf16 v[0:3], a[164:167], v[164:167], v[0:3]\n"
"buffer_load_dwordx4 v210, s[20:23], s42 offen offset:1056 lds\n"
"v_mfma_f32_16x16x32_bf16 v[4:7], a[168:171], v[160:163], v[4:7]\n"
"v_mfma_f32_16x16x32_bf16 v[4:7], a[172:175], v[164:167], v[4:7]\n"
"v_mfma_f32_16x16x32_bf16 v[8:11], a[176:179], v[160:163], v[8:11]\n"
"s_add_u32 m0, s40, 0\n"
"v_mfma_f32_16x16x32_bf16 v[8:11], a[180:183], v[164:167], v[8:11]\n"
"s_mov_b32 s42, 0x32000\n"
"buffer_load_dwordx4 v204, s[24:27], s42 offen nt lds\n"
"v_mfma_f32_16x16x32_bf16 v[12:15], a[184:187], v[160:163], v[12:15]\n"
"s_waitcnt vmcnt(15)\n"
"ds_read_b128 v[160:163], v198 offset:24576\n"
"ds_read_b64_tr_b16 a[192:193], v196 offset:12672\n"
"v_mfma_f32_16x16x32_bf16 v[12:15], a[188:191], v[164:167], v[12:15]\n"
"s_waitcnt lgkmcnt(3)\n"
"ds_read_b128 v[164:167], v199 offset:24576\n"
"ds_read_b64_tr_b16 a[200:201], v196 offset:12680\n"
"v_mfma_f32_16x16x32_bf16 v[16:19], a[160:163], v[168:171], v[16:19]\n"
"s_waitcnt lgkmcnt(4)\n"
"ds_read_b64_tr_b16 a[208:209], v197 offset:12672\n"
"v_mfma_f32_16x16x32_bf16 v[16:19], a[164:167], v[172:175], v[16:19]\n"
"buffer_load_dwordx4 v205, s[24:27], s42 offen offset:1024 nt lds\n"
"v_mfma_f32_16x16x32_bf16 v[20:23], a[168:171], v[168:171], v[20:23]\n"
"ds_read_b64_tr_b16 a[216:217], v197 offset:12680\n"
"v_mfma_f32_16x16x32_bf16 v[20:23], a[172:175], v[172:175], v[20:23]\n"
"v_mfma_f32_16x16x32_bf16 v[24:27], a[176:179], v[168:171], v[24:27]\n"
"v_mfma_f32_16x16x32_bf16 v[24:27], a[180:183], v[172:175], v[24:27]\n"
"buffer_load_dwordx4 v206, s[24:27], s42 offen offset:2048 nt lds\n"
"v_mfma_f32_16x16x32_bf16 v[28:31], a[184:187], v[168:171], v[28:31]\n"
"ds_read_b128 v[168:171], v198 offset:26624\n"
"v_mfma_f32_16x16x32_bf16 v[28:31], a[188:191], v[172:175], v[28:31]\n"
"s_waitcnt lgkmcnt(6)\n"
"ds_read_b128 v[172:175], v199 offset:26624\n"
"v_mfma_f32_16x16x32_bf16 v[32:35], a[160:163], v[160:163], v[32:35]\n"
"s_waitcnt lgkmcnt(5)\n"
"v_mfma_f32_16x16x32_bf16 v[32:35], a[164:167], v[164:167], v[32:35]\n"
"buffer_load_dwordx4 v207, s[24:27], s42 offen offset:3072 nt lds\n"
"v_mfma_f32_16x16x32_bf16 v[36:39], a[168:171], v[160:163], v[36:39]\n"
"v_mfma_f32_16x16x32_bf16 v[36:39], a[172:175], v[164:167], v[36:39]\n"
"v_mfma_f32_16x16x32_bf16 v[40:43], a[176:179], v[160:163], v[40:43]\n"
"v_mfma_f32_16x16x32_bf16 v[40:43], a[180:183], v[164:167], v[40:43]\n"
"v_mfma_f32_16x16x32_bf16 v[44:47], a[184:187], v[160:163], v[44:47]\n"
"s_waitcnt vmcnt(13)\n"
"ds_read_b128 v[160:163], v198 offset:28672\n"
"ds_read_b128 a[112:115], v200 offset:4224\n"
"v_mfma_f32_16x16x32_bf16 v[44:47], a[188:191], v[164:167], v[44:47]\n"
"s_waitcnt lgkmcnt(3)\n"
"ds_read_b128 v[164:167], v199 offset:28672\n"
"ds_read_b128 a[116:119], v201 offset:4224\n"
"v_mfma_f32_16x16x32_bf16 v[48:51], a[160:163], v[168:171], v[48:51]\n"
"s_waitcnt lgkmcnt(4)\n"
"ds_read_b128 a[120:123], v202 offset:4224\n"
"v_mfma_f32_16x16x32_bf16 v[48:51], a[164:167], v[172:175], v[48:51]\n"
"v_mfma_f32_16x16x32_bf16 v[52:55], a[168:171], v[168:171], v[52:55]\n"
"ds_read_b128 a[124:127], v203 offset:4224\n"
"v_mfma_f32_16x16x32_bf16 v[52:55], a[172:175], v[172:175], v[52:55]\n"
"v_mfma_f32_16x16x32_bf16 v[56:59], a[176:179], v[168:171], v[56:59]\n"
"v_mfma_f32_16x16x32_bf16 v[56:59], a[180:183], v[172:175], v[56:59]\n"
"v_mfma_f32_16x16x32_bf16 v[60:63], a[184:187], v[168:171], v[60:63]\n"
"ds_read_b128 v[168:171], v198 offset:30720\n"
"v_mfma_f32_16x16x32_bf16 v[60:63], a[188:191], v[172:175], v[60:63]\n"
"s_waitcnt lgkmcnt(6)\n"
"ds_read_b128 v[172:175], v199 offset:30720\n"
"v_mfma_f32_16x16x32_bf16 v[64:67], a[160:163], v[160:163], v[64:67]\n"
"s_waitcnt lgkmcnt(5)\n"
"v_mfma_f32_16x16x32_bf16 v[64:67], a[164:167], v[164:167], v[64:67]\n"
"v_mfma_f32_16x16x32_bf16 v[68:71], a[168:171], v[160:163], v[68:71]\n"
"s_waitcnt vmcnt(11)\n"
"ds_read_b64_tr_b16 a[194:195], v196 offset:8448\n"
"v_mfma_f32_16x16x32_bf16 v[68:71], a[172:175], v[164:167], v[68:71]\n"
"ds_read_b64_tr_b16 a[202:203], v196 offset:8456\n"
"v_mfma_f32_16x16x32_bf16 v[72:75], a[176:179], v[160:163], v[72:75]\n"
"ds_read_b64_tr_b16 a[210:211], v197 offset:8448\n"
"v_mfma_f32_16x16x32_bf16 v[72:75], a[180:183], v[164:167], v[72:75]\n"
"v_mfma_f32_16x16x32_bf16 v[76:79], a[184:187], v[160:163], v[76:79]\n"
"s_add_u32 m0, s40, 0x1080\n"
"ds_read_b128 v[160:163], v198 offset:32768\n"
"ds_read_b64_tr_b16 a[218:219], v197 offset:8456\n"
"v_mfma_f32_16x16x32_bf16 v[76:79], a[188:191], v[164:167], v[76:79]\n"
"s_waitcnt lgkmcnt(6)\n"
"ds_read_b128 v[164:167], v199 offset:32768\n"
"v_mfma_f32_16x16x32_bf16 v[80:83], a[160:163], v[168:171], v[80:83]\n"
"s_waitcnt lgkmcnt(6)\n"
"v_mfma_f32_16x16x32_bf16 v[80:83], a[164:167], v[172:175], v[80:83]\n"
"s_mov_b32 s42, 0x32100\n"
"buffer_load_dwordx4 v204, s[24:27], s42 offen nt lds\n"
"v_mfma_f32_16x16x32_bf16 v[84:87], a[168:171], v[168:171], v[84:87]\n"
"v_mfma_f32_16x16x32_bf16 v[84:87], a[172:175], v[172:175], v[84:87]\n"
"v_mfma_f32_16x16x32_bf16 v[88:91], a[176:179], v[168:171], v[88:91]\n"
"v_mfma_f32_16x16x32_bf16 v[88:91], a[180:183], v[172:175], v[88:91]\n"
"v_mfma_f32_16x16x32_bf16 v[92:95], a[184:187], v[168:171], v[92:95]\n"
"ds_read_b128 v[168:171], v198 offset:34816\n"
"v_mfma_f32_16x16x32_bf16 v[92:95], a[188:191], v[172:175], v[92:95]\n"
"s_waitcnt lgkmcnt(3)\n"
"ds_read_b128 v[172:175], v199 offset:34816\n"
"v_mfma_f32_16x16x32_bf16 v[96:99], a[160:163], v[160:163], v[96:99]\n"
"s_waitcnt lgkmcnt(2)\n"
"v_mfma_f32_16x16x32_bf16 v[96:99], a[164:167], v[164:167], v[96:99]\n"
"buffer_load_dwordx4 v205, s[24:27], s42 offen offset:1024 nt lds\n"
"v_mfma_f32_16x16x32_bf16 v[100:103], a[168:171], v[160:163], v[100:103]\n"
"s_waitcnt vmcnt(9)\n"
"ds_read_b64_tr_b16 a[196:197], v196 offset:10560\n"
"v_mfma_f32_16x16x32_bf16 v[100:103], a[172:175], v[164:167], v[100:103]\n"
"ds_read_b64_tr_b16 a[204:205], v196 offset:10568\n"
"v_mfma_f32_16x16x32_bf16 v[104:107], a[176:179], v[160:163], v[104:107]\n"
"ds_read_b64_tr_b16 a[212:213], v197 offset:10560\n"
"v_mfma_f32_16x16x32_bf16 v[104:107], a[180:183], v[164:167], v[104:107]\n"
"v_mfma_f32_16x16x32_bf16 v[108:111], a[184:187], v[160:163], v[108:111]\n"
"ds_read_b128 v[160:163], v198 offset:36864\n"
"ds_read_b64_tr_b16 a[220:221], v197 offset:10568\n"
"v_mfma_f32_16x16x32_bf16 v[108:111], a[188:191], v[164:167], v[108:111]\n"
"s_waitcnt lgkmcnt(6)\n"
"ds_read_b128 v[164:167], v199 offset:36864\n"
"v_mfma_f32_16x16x32_bf16 v[112:115], a[160:163], v[168:171], v[112:115]\n"
"s_waitcnt lgkmcnt(6)\n"
"v_mfma_f32_16x16x32_bf16 v[112:115], a[164:167], v[172:175], v[112:115]\n"
"buffer_load_dwordx4 v206, s[24:27], s42 offen offset:2048 nt lds\n"
"v_mfma_f32_16x16x32_bf16 v[116:119], a[168:171], v[168:171], v[116:119]\n"
"v_mfma_f32_16x16x32_bf16 v[116:119], a[172:175], v[172:175], v[116:119]\n"
"v_mfma_f32_16x16x32_bf16 v[120:123], a[176:179], v[168:171], v[120:123]\n"
"v_mfma_f32_16x16x32_bf16 v[120:123], a[180:183], v[172:175], v[120:123]\n"
"v_mfma_f32_16x16x32_bf16 v[124:127], a[184:187], v[168:171], v[124:127]\n"
"s_waitcnt vmcnt(7)\n"
"ds_read_b128 v[168:171], v198 offset:38912\n"
"ds_read_b64_tr_b16 a[198:199], v196 offset:14784\n"
"v_mfma_f32_16x16x32_bf16 v[124:127], a[188:191], v[172:175], v[124:127]\n"
"s_waitcnt lgkmcnt(4)\n"
"ds_read_b128 v[172:175], v199 offset:38912\n"
"ds_read_b64_tr_b16 a[206:207], v196 offset:14792\n"
"v_mfma_f32_16x16x32_bf16 v[128:131], a[160:163], v[160:163], v[128:131]\n"
"s_waitcnt lgkmcnt(4)\n"
"ds_read_b64_tr_b16 a[214:215], v197 offset:14784\n"
"v_mfma_f32_16x16x32_bf16 v[128:131], a[164:167], v[164:167], v[128:131]\n"
"buffer_load_dwordx4 v207, s[24:27], s42 offen offset:3072 nt lds\n"
"v_mfma_f32_16x16x32_bf16 v[132:135], a[168:171], v[160:163], v[132:135]\n"
"ds_read_b64_tr_b16 a[222:223], v197 offset:14792\n"
"v_mfma_f32_16x16x32_bf16 v[132:135], a[172:175], v[164:167], v[132:135]\n"
"v_mfma_f32_16x16x32_bf16 v[136:139], a[176:179], v[160:163], v[136:139]\n"
"v_mfma_f32_16x16x32_bf16 v[136:139], a[180:183], v[164:167], v[136:139]\n"
"v_mfma_f32_16x16x32_bf16 v[140:143], a[184:187], v[160:163], v[140:143]\n"
"s_barrier\n"
"ds_read_b128 v[160:163], v198 offset:40960\n"
"v_mfma_f32_16x16x32_bf16 v[140:143], a[188:191], v[164:167], v[140:143]\n"
"s_waitcnt lgkmcnt(6)\n"
"ds_read_b128 v[164:167], v199 offset:40960\n"
"v_mfma_f32_16x16x32_bf16 v[144:147], a[160:163], v[168:171], v[144:147]\n"
"s_waitcnt lgkmcnt(5)\n"
"v_mfma_f32_16x16x32_bf16 v[144:147], a[164:167], v[172:175], v[144:147]\n"
"v_mfma_f32_16x16x32_bf16 v[148:151], a[168:171], v[168:171], v[148:151]\n"
"s_waitcnt vmcnt(4)\n"
"ds_read_b128 a[224:227], v200 offset:0\n"
"v_mfma_f32_16x16x32_bf16 v[148:151], a[172:175], v[172:175], v[148:151]\n"
"ds_read_b128 a[228:231], v201 offset:0\n"
"v_mfma_f32_16x16x32_bf16 v[152:155], a[176:179], v[168:171], v[152:155]\n"
"ds_read_b128 a[232:235], v202 offset:0\n"
"v_mfma_f32_16x16x32_bf16 v[152:155], a[180:183], v[172:175], v[152:155]\n"
"v_mfma_f32_16x16x32_bf16 v[156:159], a[184:187], v[168:171], v[156:159]\n"
"ds_read_b128 v[168:171], v198 offset:43008\n"
"ds_read_b128 a[236:239], v203 offset:0\n"
"v_mfma_f32_16x16x32_bf16 v[156:159], a[188:191], v[172:175], v[156:159]\n"
"s_waitcnt lgkmcnt(6)\n"
"ds_read_b128 v[172:175], v199 offset:43008\n"
"v_mfma_f32_16x16x32_bf16 v[0:3], a[192:195], v[160:163], v[0:3]\n"
"s_waitcnt lgkmcnt(6)\n"
"v_mfma_f32_16x16x32_bf16 v[0:3], a[196:199], v[164:167], v[0:3]\n"
"v_mfma_f32_16x16x32_bf16 v[4:7], a[200:203], v[160:163], v[4:7]\n"
"v_mfma_f32_16x16x32_bf16 v[4:7], a[204:207], v[164:167], v[4:7]\n"
"v_mfma_f32_16x16x32_bf16 v[8:11], a[208:211], v[160:163], v[8:11]\n"
"v_mfma_f32_16x16x32_bf16 v[8:11], a[212:215], v[164:167], v[8:11]\n"
"v_mfma_f32_16x16x32_bf16 v[12:15], a[216:219], v[160:163], v[12:15]\n"
"ds_read_b128 v[160:163], v198 offset:45056\n"
"v_mfma_f32_16x16x32_bf16 v[12:15], a[220:223], v[164:167], v[12:15]\n"
"s_waitcnt lgkmcnt(3)\n"
"ds_read_b128 v[164:167], v199 offset:45056\n"
"v_mfma_f32_16x16x32_bf16 v[16:19], a[192:195], v[168:171], v[16:19]\n"
"s_waitcnt lgkmcnt(2)\n"
"v_mfma_f32_16x16x32_bf16 v[16:19], a[196:199], v[172:175], v[16:19]\n"
"v_mfma_f32_16x16x32_bf16 v[20:23], a[200:203], v[168:171], v[20:23]\n"
"v_mfma_f32_16x16x32_bf16 v[20:23], a[204:207], v[172:175], v[20:23]\n"
"v_mfma_f32_16x16x32_bf16 v[24:27], a[208:211], v[168:171], v[24:27]\n"
"s_add_u32 m0, s40, 0\n"
"v_mfma_f32_16x16x32_bf16 v[24:27], a[212:215], v[172:175], v[24:27]\n"
"s_mov_b32 s42, 0x20000\n"
"buffer_load_dwordx4 v204, s[24:27], s42 offen nt lds\n"
"v_mfma_f32_16x16x32_bf16 v[28:31], a[216:219], v[168:171], v[28:31]\n"
"ds_read_b128 v[168:171], v198 offset:47104\n"
"v_mfma_f32_16x16x32_bf16 v[28:31], a[220:223], v[172:175], v[28:31]\n"
"s_waitcnt lgkmcnt(2)\n"
"ds_read_b128 v[172:175], v199 offset:47104\n"
"v_mfma_f32_16x16x32_bf16 v[32:35], a[192:195], v[160:163], v[32:35]\n"
"s_waitcnt lgkmcnt(2)\n"
"v_mfma_f32_16x16x32_bf16 v[32:35], a[196:199], v[164:167], v[32:35]\n"
"v_mfma_f32_16x16x32_bf16 v[36:39], a[200:203], v[160:163], v[36:39]\n"
"v_cvt_pk_bf16_f32 v0, v0, v1\n"
"v_mfma_f32_16x16x32_bf16 v[36:39], a[204:207], v[164:167], v[36:39]\n"
"v_cvt_pk_bf16_f32 v1, v2, v3\n"
"v_cvt_pk_bf16_f32 v2, v4, v5\n"
"v_mfma_f32_16x16x32_bf16 v[40:43], a[208:211], v[160:163], v[40:43]\n"
"v_cvt_pk_bf16_f32 v3, v6, v7\n"
"v_mfma_f32_16x16x32_bf16 v[40:43], a[212:215], v[164:167], v[40:43]\n"
"buffer_load_dwordx4 v205, s[24:27], s42 offen offset:1024 nt lds\n"
"v_mfma_f32_16x16x32_bf16 v[44:47], a[216:219], v[160:163], v[44:47]\n"
"ds_read_b128 v[160:163], v198 offset:49152\n"
"v_mfma_f32_16x16x32_bf16 v[44:47], a[220:223], v[164:167], v[44:47]\n"
"s_waitcnt lgkmcnt(2)\n"
"ds_read_b128 v[164:167], v199 offset:49152\n"
"v_cvt_pk_bf16_f32 v8, v8, v9\n"
"v_mfma_f32_16x16x32_bf16 v[48:51], a[192:195], v[168:171], v[48:51]\n"
"s_waitcnt lgkmcnt(2)\n"
"v_cvt_pk_bf16_f32 v9, v10, v11\n"
"v_mfma_f32_16x16x32_bf16 v[48:51], a[196:199], v[172:175], v[48:51]\n"
"ds_write_b128 v212, v[0:3] offset:0\n"
"v_mfma_f32_16x16x32_bf16 v[52:55], a[200:203], v[168:171], v[52:55]\n"
"v_mfma_f32_16x16x32_bf16 v[52:55], a[204:207], v[172:175], v[52:55]\n"
"v_mfma_f32_16x16x32_bf16 v[56:59], a[208:211], v[168:171], v[56:59]\n"
"v_cvt_pk_bf16_f32 v10, v12, v13\n"
"v_mfma_f32_16x16x32_bf16 v[56:59], a[212:215], v[172:175], v[56:59]\n"
"buffer_load_dwordx4 v206, s[24:27], s42 offen offset:2048 nt lds\n"
"v_mfma_f32_16x16x32_bf16 v[60:63], a[216:219], v[168:171], v[60:63]\n"
"ds_read_b128 v[168:171], v198 offset:51200\n"
"v_mfma_f32_16x16x32_bf16 v[60:63], a[220:223], v[172:175], v[60:63]\n"
"s_waitcnt lgkmcnt(3)\n"
"ds_read_b128 v[172:175], v199 offset:51200\n"
"v_cvt_pk_bf16_f32 v11, v14, v15\n"
"v_mfma_f32_16x16x32_bf16 v[64:67], a[192:195], v[160:163], v[64:67]\n"
"s_waitcnt lgkmcnt(3)\n"
"v_cvt_pk_bf16_f32 v16, v16, v17\n"
"v_mfma_f32_16x16x32_bf16 v[64:67], a[196:199], v[164:167], v[64:67]\n"
"ds_write_b128 v212, v[8:11] offset:1024\n"
"v_mfma_f32_16x16x32_bf16 v[68:71], a[200:203], v[160:163], v[68:71]\n"
"v_mfma_f32_16x16x32_bf16 v[68:71], a[204:207], v[164:167], v[68:71]\n"
"v_mfma_f32_16x16x32_bf16 v[72:75], a[208:211], v[160:163], v[72:75]\n"
"v_cvt_pk_bf16_f32 v17, v18, v19\n"
"v_mfma_f32_16x16x32_bf16 v[72:75], a[212:215], v[164:167], v[72:75]\n"
"buffer_load_dwordx4 v207, s[24:27], s42 offen offset:3072 nt lds\n"
"v_mfma_f32_16x16x32_bf16 v[76:79], a[216:219], v[160:163], v[76:79]\n"
"ds_read_b128 v[160:163], v198 offset:53248\n"
"v_mfma_f32_16x16x32_bf16 v[76:79], a[220:223], v[164:167], v[76:79]\n"
"s_waitcnt lgkmcnt(3)\n"
"ds_read_b128 v[164:167], v199 offset:53248\n"
"v_cvt_pk_bf16_f32 v18, v20, v21\n"
"v_mfma_f32_16x16x32_bf16 v[80:83], a[192:195], v[168:171], v[80:83]\n"
"s_waitcnt lgkmcnt(3)\n"
"v_cvt_pk_bf16_f32 v19, v22, v23\n"
"v_mfma_f32_16x16x32_bf16 v[80:83], a[196:199], v[172:175], v[80:83]\n"
"ds_write_b128 v212, v[16:19] offset:8192\n"
"v_mfma_f32_16x16x32_bf16 v[84:87], a[200:203], v[168:171], v[84:87]\n"
"v_mfma_f32_16x16x32_bf16 v[84:87], a[204:207], v[172:175], v[84:87]\n"
"v_mfma_f32_16x16x32_bf16 v[88:91], a[208:211], v[168:171], v[88:91]\n"
"v_cvt_pk_bf16_f32 v24, v24, v25\n"
"v_mfma_f32_16x16x32_bf16 v[88:91], a[212:215], v[172:175], v[88:91]\n"
"v_cvt_pk_bf16_f32 v25, v26, v27\n"
"v_cvt_pk_bf16_f32 v26, v28, v29\n"
"v_mfma_f32_16x16x32_bf16 v[92:95], a[216:219], v[168:171], v[92:95]\n"
"ds_read_b128 v[168:171], v198 offset:55296\n"
"v_mfma_f32_16x16x32_bf16 v[92:95], a[220:223], v[172:175], v[92:95]\n"
"s_waitcnt lgkmcnt(3)\n"
"ds_read_b128 v[172:175], v199 offset:55296\n"
"v_cvt_pk_bf16_f32 v27, v30, v31\n"
"v_mfma_f32_16x16x32_bf16 v[96:99], a[192:195], v[160:163], v[96:99]\n"
"s_waitcnt lgkmcnt(3)\n"
"v_cvt_pk_bf16_f32 v32, v32, v33\n"
"v_mfma_f32_16x16x32_bf16 v[96:99], a[196:199], v[164:167], v[96:99]\n"
"ds_write_b128 v212, v[24:27] offset:9216\n"
"v_mfma_f32_16x16x32_bf16 v[100:103], a[200:203], v[160:163], v[100:103]\n"
"v_mfma_f32_16x16x32_bf16 v[100:103], a[204:207], v[164:167], v[100:103]\n"
"v_mfma_f32_16x16x32_bf16 v[104:107], a[208:211], v[160:163], v[104:107]\n"
"v_cvt_pk_bf16_f32 v33, v34, v35\n"
"v_mfma_f32_16x16x32_bf16 v[104:107], a[212:215], v[164:167], v[104:107]\n"
"v_cvt_pk_bf16_f32 v34, v36, v37\n"
"v_cvt_pk_bf16_f32 v35, v38, v39\n"
"v_mfma_f32_16x16x32_bf16 v[108:111], a[216:219], v[160:163], v[108:111]\n"
"ds_read_b128 v[160:163], v198 offset:57344\n"
"v_mfma_f32_16x16x32_bf16 v[108:111], a[220:223], v[164:167], v[108:111]\n"
"s_waitcnt lgkmcnt(3)\n"
"ds_read_b128 v[164:167], v199 offset:57344\n"
"v_cvt_pk_bf16_f32 v40, v40, v41\n"
"v_mfma_f32_16x16x32_bf16 v[112:115], a[192:195], v[168:171], v[112:115]\n"
"s_waitcnt lgkmcnt(3)\n"
"v_cvt_pk_bf16_f32 v41, v42, v43\n"
"v_mfma_f32_16x16x32_bf16 v[112:115], a[196:199], v[172:175], v[112:115]\n"
"ds_write_b128 v212, v[32:35] offset:16384\n"
"v_mfma_f32_16x16x32_bf16 v[116:119], a[200:203], v[168:171], v[116:119]\n"
"v_mfma_f32_16x16x32_bf16 v[116:119], a[204:207], v[172:175], v[116:119]\n"
"v_mfma_f32_16x16x32_bf16 v[120:123], a[208:211], v[168:171], v[120:123]\n"
"v_cvt_pk_bf16_f32 v42, v44, v45\n"
"v_cvt_pk_bf16_f32 v43, v46, v47\n"
"v_mfma_f32_16x16x32_bf16 v[120:123], a[212:215], v[172:175], v[120:123]\n"
"ds_write_b128 v212, v[40:43] offset:17408\n"
"v_mfma_f32_16x16x32_bf16 v[124:127], a[216:219], v[168:171], v[124:127]\n"
"ds_read_b128 v[168:171], v198 offset:59392\n"
"v_mfma_f32_16x16x32_bf16 v[124:127], a[220:223], v[172:175], v[124:127]\n"
"s_waitcnt lgkmcnt(4)\n"
"ds_read_b128 v[172:175], v199 offset:59392\n"
"v_mfma_f32_16x16x32_bf16 v[128:131], a[192:195], v[160:163], v[128:131]\n"
"s_waitcnt lgkmcnt(4)\n"
"v_mfma_f32_16x16x32_bf16 v[128:131], a[196:199], v[164:167], v[128:131]\n"
"v_mfma_f32_16x16x32_bf16 v[132:135], a[200:203], v[160:163], v[132:135]\n"
"v_mfma_f32_16x16x32_bf16 v[132:135], a[204:207], v[164:167], v[132:135]\n"
"v_mfma_f32_16x16x32_bf16 v[136:139], a[208:211], v[160:163], v[136:139]\n"
"s_add_u32 m0, s40, 0x3180\n"
"v_mfma_f32_16x16x32_bf16 v[136:139], a[212:215], v[164:167], v[136:139]\n"
"s_mov_b32 s42, 0x20100\n"
"buffer_load_dwordx4 v204, s[24:27], s42 offen nt lds\n"
"v_mfma_f32_16x16x32_bf16 v[140:143], a[216:219], v[160:163], v[140:143]\n"
"s_waitcnt lgkmcnt(0)\n"
"s_barrier\n"
"ds_read_b128 v[160:163], v194 offset:0\n"
"v_mfma_f32_16x16x32_bf16 v[140:143], a[220:223], v[164:167], v[140:143]\n"
"ds_read_b128 v[164:167], v194 offset:1024\n"
"v_cvt_pk_bf16_f32 v48, v48, v49\n"
"v_mfma_f32_16x16x32_bf16 v[144:147], a[192:195], v[168:171], v[144:147]\n"
"v_cvt_pk_bf16_f32 v49, v50, v51\n"
"v_cvt_pk_bf16_f32 v50, v52, v53\n"
"v_mfma_f32_16x16x32_bf16 v[144:147], a[196:199], v[172:175], v[144:147]\n"
"v_cvt_pk_bf16_f32 v51, v54, v55\n"
"v_cvt_pk_bf16_f32 v56, v56, v57\n"
"v_mfma_f32_16x16x32_bf16 v[148:151], a[200:203], v[168:171], v[148:151]\n"
"v_cvt_pk_bf16_f32 v57, v58, v59\n"
"v_cvt_pk_bf16_f32 v58, v60, v61\n"
"v_mfma_f32_16x16x32_bf16 v[148:151], a[204:207], v[172:175], v[148:151]\n"
"ds_write_b128 v212, v[48:51] offset:24576\n"
"v_mfma_f32_16x16x32_bf16 v[152:155], a[208:211], v[168:171], v[152:155]\n"
"v_cvt_pk_bf16_f32 v59, v62, v63\n"
"v_cvt_pk_bf16_f32 v64, v64, v65\n"
"v_mfma_f32_16x16x32_bf16 v[152:155], a[212:215], v[172:175], v[152:155]\n"
"buffer_load_dwordx4 v205, s[24:27], s42 offen offset:1024 nt lds\n"
"v_mfma_f32_16x16x32_bf16 v[156:159], a[216:219], v[168:171], v[156:159]\n"
"ds_read_b128 v[168:171], v194 offset:2048\n"
"v_cvt_pk_bf16_f32 v65, v66, v67\n"
"v_mfma_f32_16x16x32_bf16 v[156:159], a[220:223], v[172:175], v[156:159]\n"
"s_waitcnt lgkmcnt(3)\n"
"ds_read_b128 v[172:175], v194 offset:3072\n"
"v_mfma_f32_16x16x32_bf16 v[0:3], a[0:3], v[160:163], 0\n"
"s_waitcnt lgkmcnt(3)\n"
"v_cvt_pk_bf16_f32 v66, v68, v69\n"
"v_cvt_pk_bf16_f32 v67, v70, v71\n"
"v_mfma_f32_16x16x32_bf16 v[0:3], a[4:7], v[164:167], v[0:3]\n"
"ds_write_b128 v212, v[56:59] offset:25600\n"
"v_mfma_f32_16x16x32_bf16 v[4:7], a[32:35], v[160:163], 0\n"
"v_cvt_pk_bf16_f32 v72, v72, v73\n"
"v_cvt_pk_bf16_f32 v73, v74, v75\n"
"v_mfma_f32_16x16x32_bf16 v[4:7], a[36:39], v[164:167], v[4:7]\n"
"v_mfma_f32_16x16x32_bf16 v[8:11], a[64:67], v[160:163], 0\n"
"v_cvt_pk_bf16_f32 v74, v76, v77\n"
"v_cvt_pk_bf16_f32 v75, v78, v79\n"
"v_mfma_f32_16x16x32_bf16 v[8:11], a[68:71], v[164:167], v[8:11]\n"
"buffer_load_dwordx4 v206, s[24:27], s42 offen offset:2048 nt lds\n"
"v_mfma_f32_16x16x32_bf16 v[12:15], a[96:99], v[160:163], 0\n"
"ds_read_b128 v[160:163], v194 offset:4096\n"
"v_cvt_pk_bf16_f32 v80, v80, v81\n"
"v_mfma_f32_16x16x32_bf16 v[12:15], a[100:103], v[164:167], v[12:15]\n"
"s_waitcnt lgkmcnt(3)\n"
"ds_read_b128 v[164:167], v194 offset:5120\n"
"v_cvt_pk_bf16_f32 v81, v82, v83\n"
"v_mfma_f32_16x16x32_bf16 v[0:3], a[8:11], v[168:171], v[0:3]\n"
"s_waitcnt lgkmcnt(3)\n"
"v_cvt_pk_bf16_f32 v82, v84, v85\n"
"v_cvt_pk_bf16_f32 v83, v86, v87\n"
"v_mfma_f32_16x16x32_bf16 v[0:3], a[12:15], v[172:175], v[0:3]\n"
"ds_write_b128 v212, v[64:67] offset:32768\n"
"v_mfma_f32_16x16x32_bf16 v[4:7], a[40:43], v[168:171], v[4:7]\n"
"v_cvt_pk_bf16_f32 v88, v88, v89\n"
"v_cvt_pk_bf16_f32 v89, v90, v91\n"
"v_mfma_f32_16x16x32_bf16 v[4:7], a[44:47], v[172:175], v[4:7]\n"
"v_mfma_f32_16x16x32_bf16 v[8:11], a[72:75], v[168:171], v[8:11]\n"
"v_cvt_pk_bf16_f32 v90, v92, v93\n"
"v_cvt_pk_bf16_f32 v91, v94, v95\n"
"v_mfma_f32_16x16x32_bf16 v[8:11], a[76:79], v[172:175], v[8:11]\n"
"buffer_load_dwordx4 v207, s[24:27], s42 offen offset:3072 nt lds\n"
"v_cvt_pk_bf16_f32 v96, v96, v97\n"
"v_mfma_f32_16x16x32_bf16 v[12:15], a[104:107], v[168:171], v[12:15]\n"
"ds_read_b128 v[168:171], v194 offset:6144\n"
"v_cvt_pk_bf16_f32 v97, v98, v99\n"
"v_mfma_f32_16x16x32_bf16 v[12:15], a[108:111], v[172:175], v[12:15]\n"
"s_waitcnt vmcnt(8) lgkmcnt(3)\n"
"ds_read_b128 v[172:175], v194 offset:7168\n"
"ds_read_b128 a[240:243], v200 offset:4224\n"
"v_mfma_f32_16x16x32_bf16 v[0:3], a[16:19], v[160:163], v[0:3]\n"
"s_waitcnt lgkmcnt(4)\n"
"ds_read_b128 a[244:247], v201 offset:4224\n"
"v_cvt_pk_bf16_f32 v98, v100, v101\n"
"v_mfma_f32_16x16x32_bf16 v[0:3], a[20:23], v[164:167], v[0:3]\n"
"ds_write_b128 v212, v[72:75] offset:33792\n"
"v_mfma_f32_16x16x32_bf16 v[4:7], a[48:51], v[160:163], v[4:7]\n"
"ds_read_b128 a[248:251], v202 offset:4224\n"
"v_cvt_pk_bf16_f32 v99, v102, v103\n"
"v_mfma_f32_16x16x32_bf16 v[4:7], a[52:55], v[164:167], v[4:7]\n"
"ds_read_b128 a[252:255], v203 offset:4224\n"
"v_mfma_f32_16x16x32_bf16 v[8:11], a[80:83], v[160:163], v[8:11]\n"
"v_cvt_pk_bf16_f32 v104, v104, v105\n"
"v_cvt_pk_bf16_f32 v105, v106, v107\n"
"v_mfma_f32_16x16x32_bf16 v[8:11], a[84:87], v[164:167], v[8:11]\n"
"ds_write_b128 v212, v[80:83] offset:40960\n"
"v_mfma_f32_16x16x32_bf16 v[12:15], a[112:115], v[160:163], v[12:15]\n"
"ds_read_b128 v[160:163], v194 offset:8192\n"
"v_cvt_pk_bf16_f32 v106, v108, v109\n"
"v_mfma_f32_16x16x32_bf16 v[12:15], a[116:119], v[164:167], v[12:15]\n"
"s_waitcnt lgkmcnt(8)\n"
"ds_read_b128 v[164:167], v194 offset:9216\n"
"v_mfma_f32_16x16x32_bf16 v[0:3], a[24:27], v[168:171], v[0:3]\n"
"s_waitcnt lgkmcnt(8)\n"
"v_cvt_pk_bf16_f32 v107, v110, v111\n"
"v_cvt_pk_bf16_f32 v112, v112, v113\n"
"v_mfma_f32_16x16x32_bf16 v[0:3], a[28:31], v[172:175], v[0:3]\n"
"v_mfma_f32_16x16x32_bf16 v[4:7], a[56:59], v[168:171], v[4:7]\n"
"v_cvt_pk_bf16_f32 v113, v114, v115\n"
"v_cvt_pk_bf16_f32 v114, v116, v117\n"
"v_mfma_f32_16x16x32_bf16 v[4:7], a[60:63], v[172:175], v[4:7]\n"
"ds_write_b128 v212, v[88:91] offset:41984\n"
"v_mfma_f32_16x16x32_bf16 v[8:11], a[88:91], v[168:171], v[8:11]\n"
"v_cvt_pk_bf16_f32 v115, v118, v119\n"
"v_cvt_pk_bf16_f32 v120, v120, v121\n"
"v_mfma_f32_16x16x32_bf16 v[8:11], a[92:95], v[172:175], v[8:11]\n"
"v_mfma_f32_16x16x32_bf16 v[12:15], a[120:123], v[168:171], v[12:15]\n"
"ds_read_b128 v[168:171], v194 offset:10240\n"
"v_cvt_pk_bf16_f32 v121, v122, v123\n"
"v_mfma_f32_16x16x32_bf16 v[12:15], a[124:127], v[172:175], v[12:15]\n"
"s_waitcnt lgkmcnt(3)\n"
"ds_read_b128 v[172:175], v194 offset:11264\n"
"v_cvt_pk_bf16_f32 v122, v124, v125\n"
"v_cvt_pk_bf16_f32 v123, v126, v127\n"
"v_mfma_f32_16x16x32_bf16 v[16:19], a[0:3], v[160:163], 0\n"
"s_waitcnt lgkmcnt(3)\n"
"v_cvt_pk_bf16_f32 v128, v128, v129\n"
"v_cvt_pk_bf16_f32 v129, v130, v131\n"
"v_mfma_f32_16x16x32_bf16 v[16:19], a[4:7], v[164:167], v[16:19]\n"
"ds_write_b128 v212, v[96:99] offset:49152\n"
"v_mfma_f32_16x16x32_bf16 v[20:23], a[32:35], v[160:163], 0\n"
"v_cvt_pk_bf16_f32 v130, v132, v133\n"
"v_cvt_pk_bf16_f32 v131, v134, v135\n"
"v_mfma_f32_16x16x32_bf16 v[20:23], a[36:39], v[164:167], v[20:23]\n"
"v_mfma_f32_16x16x32_bf16 v[24:27], a[64:67], v[160:163], 0\n"
"v_cvt_pk_bf16_f32 v136, v136, v137\n"
"v_cvt_pk_bf16_f32 v137, v138, v139\n"
"v_mfma_f32_16x16x32_bf16 v[24:27], a[68:71], v[164:167], v[24:27]\n"
"ds_write_b128 v212, v[104:107] offset:50176\n"
"v_mfma_f32_16x16x32_bf16 v[28:31], a[96:99], v[160:163], 0\n"
"s_add_u32 m0, s40, 0x1080\n"
"ds_read_b128 v[160:163], v194 offset:12288\n"
"v_cvt_pk_bf16_f32 v138, v140, v141\n"
"v_mfma_f32_16x16x32_bf16 v[28:31], a[100:103], v[164:167], v[28:31]\n"
"s_waitcnt lgkmcnt(4)\n"
"ds_read_b128 v[164:167], v194 offset:13312\n"
"v_mfma_f32_16x16x32_bf16 v[16:19], a[8:11], v[168:171], v[16:19]\n"
"s_waitcnt lgkmcnt(4)\n"
"v_cvt_pk_bf16_f32 v139, v142, v143\n"
"v_cvt_pk_bf16_f32 v144, v144, v145\n"
"v_mfma_f32_16x16x32_bf16 v[16:19], a[12:15], v[172:175], v[16:19]\n"
"s_mov_b32 s42, 0x22000\n"
"buffer_load_dwordx4 v204, s[24:27], s42 offen nt lds\n"
"v_mfma_f32_16x16x32_bf16 v[20:23], a[40:43], v[168:171], v[20:23]\n"
"v_cvt_pk_bf16_f32 v145, v146, v147\n"
"v_cvt_pk_bf16_f32 v146, v148, v149\n"
"v_mfma_f32_16x16x32_bf16 v[20:23], a[44:47], v[172:175], v[20:23]\n"
"v_cvt_pk_bf16_f32 v147, v150, v151\n"
"v_mfma_f32_16x16x32_bf16 v[24:27], a[72:75], v[168:171], v[24:27]\n"
"v_max_f32 v218, v0, v1\n"
"v_max3_f32 v218, v218, v2, v3\n"
"v_mfma_f32_16x16x32_bf16 v[24:27], a[76:79], v[172:175], v[24:27]\n"
"ds_write_b128 v212, v[112:115] offset:57344\n"
"v_mfma_f32_16x16x32_bf16 v[28:31], a[104:107], v[168:171], v[28:31]\n"
"ds_read_b128 v[168:171], v194 offset:14336\n"
"v_cvt_pk_bf16_f32 v152, v152, v153\n"
"v_mfma_f32_16x16x32_bf16 v[28:31], a[108:111], v[172:175], v[28:31]\n"
"s_waitcnt lgkmcnt(3)\n"
"ds_read_b128 v[172:175], v194 offset:15360\n"
".Lpad2_%=:\n"
"s_cbranch_vccnz .Lpad6_%=\n"
"v_mfma_f32_16x16x32_bf16 v[16:19], a[16:19], v[160:163], v[16:19]\n"
"s_waitcnt lgkmcnt(3)\n"
"v_cvt_pk_bf16_f32 v153, v154, v155\n"
"v_cvt_pk_bf16_f32 v154, v156, v157\n"
"v_mfma_f32_16x16x32_bf16 v[16:19], a[20:23], v[164:167], v[16:19]\n"
"buffer_load_dwordx4 v205, s[24:27], s42 offen offset:1024 nt lds\n"
"v_mfma_f32_16x16x32_bf16 v[20:23], a[48:51], v[160:163], v[20:23]\n"
"v_cvt_pk_bf16_f32 v155, v158, v159\n"
"v_mfma_f32_16x16x32_bf16 v[20:23], a[52:55], v[164:167], v[20:23]\n"
"ds_write_b128 v212, v[120:123] offset:58368\n"
"v_mfma_f32_16x16x32_bf16 v[24:27], a[80:83], v[160:163], v[24:27]\n"
"v_mfma_f32_16x16x32_bf16 v[24:27], a[84:87], v[164:167], v[24:27]\n"
"v_mfma_f32_16x16x32_bf16 v[28:31], a[112:115], v[160:163], v[28:31]\n"
"ds_read_b128 v[160:163], v194 offset:16384\n"
"v_mfma_f32_16x16x32_bf16 v[28:31], a[116:119], v[164:167], v[28:31]\n"
"s_waitcnt lgkmcnt(3)\n"
"ds_read_b128 v[164:167], v194 offset:17408\n"
"v_mfma_f32_16x16x32_bf16 v[16:19], a[24:27], v[168:171], v[16:19]\n"
"s_waitcnt lgkmcnt(3)\n"
"v_max3_f32 v218, v218, v4, v5\n"
"v_max3_f32 v218, v218, v6, v7\n"
"v_mfma_f32_16x16x32_bf16 v[16:19], a[28:31], v[172:175], v[16:19]\n"
"buffer_load_dwordx4 v206, s[24:27], s42 offen offset:2048 nt lds\n"
"v_mfma_f32_16x16x32_bf16 v[20:23], a[56:59], v[168:171], v[20:23]\n"
"v_max3_f32 v218, v218, v8, v9\n"
"v_max3_f32 v218, v218, v10, v11\n"
"v_mfma_f32_16x16x32_bf16 v[20:23], a[60:63], v[172:175], v[20:23]\n"
"ds_write_b128 v213, v[128:131] offset:0\n"
"v_mfma_f32_16x16x32_bf16 v[24:27], a[88:91], v[168:171], v[24:27]\n"
"v_mfma_f32_16x16x32_bf16 v[24:27], a[92:95], v[172:175], v[24:27]\n"
"v_mfma_f32_16x16x32_bf16 v[28:31], a[120:123], v[168:171], v[28:31]\n"
"ds_read_b128 v[168:171], v194 offset:18432\n"
"v_mfma_f32_16x16x32_bf16 v[28:31], a[124:127], v[172:175], v[28:31]\n"
"s_waitcnt lgkmcnt(3)\n"
"ds_read_b128 v[172:175], v194 offset:19456\n"
"v_max3_f32 v218, v218, v12, v13\n"
"v_max3_f32 v218, v218, v14, v15\n"
"v_mfma_f32_16x16x32_bf16 v[32:35], a[0:3], v[160:163], 0\n"
"s_waitcnt lgkmcnt(3)\n"
"v_mfma_f32_16x16x32_bf16 v[32:35], a[4:7], v[164:167], v[32:35]\n"
"buffer_load_dwordx4 v207, s[24:27], s42 offen offset:3072 nt lds\n"
"v_mfma_f32_16x16x32_bf16 v[36:39], a[32:35], v[160:163], 0\n"
"v_mfma_f32_16x16x32_bf16 v[36:39], a[36:39], v[164:167], v[36:39]\n"
"s_waitcnt vmcnt(8)\n"
"ds_read_b128 a[128:131], v200 offset:0\n"
"v_mfma_f32_16x16x32_bf16 v[40:43], a[64:67], v[160:163], 0\n"
"ds_read_b128 a[132:135], v201 offset:0\n"
"v_mfma_f32_16x16x32_bf16 v[40:43], a[68:71], v[164:167], v[40:43]\n"
"ds_write_b128 v213, v[136:139] offset:1024\n"
"v_mfma_f32_16x16x32_bf16 v[44:47], a[96:99], v[160:163], 0\n"
"ds_read_b128 v[160:163], v194 offset:20480\n"
"ds_read_b128 a[136:139], v202 offset:0\n"
"v_mfma_f32_16x16x32_bf16 v[44:47], a[100:103], v[164:167], v[44:47]\n"
"s_waitcnt lgkmcnt(6)\n"
"ds_read_b128 v[164:167], v194 offset:21504\n"
"ds_read_b128 a[140:143], v203 offset:0\n"
"v_mfma_f32_16x16x32_bf16 v[32:35], a[8:11], v[168:171], v[32:35]\n"
"s_waitcnt lgkmcnt(7)\n"
"v_mfma_f32_16x16x32_bf16 v[32:35], a[12:15], v[172:175], v[32:35]\n"
"ds_write_b128 v213, v[144:147] offset:8192\n"
"v_mfma_f32_16x16x32_bf16 v[36:39], a[40:43], v[168:171], v[36:39]\n"
"v_mfma_f32_16x16x32_bf16 v[36:39], a[44:47], v[172:175], v[36:39]\n"
"v_mfma_f32_16x16x32_bf16 v[40:43], a[72:75], v[168:171], v[40:43]\n"
"v_mfma_f32_16x16x32_bf16 v[40:43], a[76:79], v[172:175], v[40:43]\n"
"ds_write_b128 v213, v[152:155] offset:9216\n"
"v_mfma_f32_16x16x32_bf16 v[44:47], a[104:107], v[168:171], v[44:47]\n"
"ds_read_b128 v[168:171], v194 offset:22528\n"
"v_mfma_f32_16x16x32_bf16 v[44:47], a[108:111], v[172:175], v[44:47]\n"
"s_waitcnt lgkmcnt(6)\n"
"ds_read_b128 v[172:175], v194 offset:23552\n"
"v_mfma_f32_16x16x32_bf16 v[32:35], a[16:19], v[160:163], v[32:35]\n"
"s_waitcnt lgkmcnt(5)\n"
"v_mfma_f32_16x16x32_bf16 v[32:35], a[20:23], v[164:167], v[32:35]\n"
"v_mfma_f32_16x16x32_bf16 v[36:39], a[48:51], v[160:163], v[36:39]\n"
"v_mfma_f32_16x16x32_bf16 v[36:39], a[52:55], v[164:167], v[36:39]\n"
"v_mfma_f32_16x16x32_bf16 v[40:43], a[80:83], v[160:163], v[40:43]\n"
"v_max_f32 v219, v16, v17\n"
"v_mfma_f32_16x16x32_bf16 v[40:43], a[84:87], v[164:167], v[40:43]\n"
"v_max3_f32 v219, v219, v18, v19\n"
"v_mfma_f32_16x16x32_bf16 v[44:47], a[112:115], v[160:163], v[44:47]\n"
"s_waitcnt lgkmcnt(2)\n"
"s_barrier\n"
"ds_read_b128 v[160:163], v194 offset:24576\n"
"v_mfma_f32_16x16x32_bf16 v[44:47], a[116:119], v[164:167], v[44:47]\n"
"s_waitcnt lgkmcnt(2)\n"
"ds_read_b128 v[164:167], v194 offset:25600\n"
"v_mfma_f32_16x16x32_bf16 v[32:35], a[24:27], v[168:171], v[32:35]\n"
"s_waitcnt lgkmcnt(2)\n"
"v_max3_f32 v219, v219, v20, v21\n"
"v_mfma_f32_16x16x32_bf16 v[32:35], a[28:31], v[172:175], v[32:35]\n"
"v_max3_f32 v219, v219, v22, v23\n"
"v_mfma_f32_16x16x32_bf16 v[36:39], a[56:59], v[168:171], v[36:39]\n"
"v_max3_f32 v219, v219, v24, v25\n"
"v_mfma_f32_16x16x32_bf16 v[36:39], a[60:63], v[172:175], v[36:39]\n"
"v_max3_f32 v219, v219, v26, v27\n"
"v_mfma_f32_16x16x32_bf16 v[40:43], a[88:91], v[168:171], v[40:43]\n"
"s_add_u32 m0, s40, 0\n"
"v_max3_f32 v219, v219, v28, v29\n"
"v_mfma_f32_16x16x32_bf16 v[40:43], a[92:95], v[172:175], v[40:43]\n"
"s_mov_b32 s42, 0x22100\n"
"buffer_load_dwordx4 v204, s[24:27], s42 offen nt lds\n"
"v_mfma_f32_16x16x32_bf16 v[44:47], a[120:123], v[168:171], v[44:47]\n"
"ds_read_b128 v[168:171], v194 offset:26624\n"
"v_mfma_f32_16x16x32_bf16 v[44:47], a[124:127], v[172:175], v[44:47]\n"
"s_waitcnt lgkmcnt(2)\n"
"ds_read_b128 v[172:175], v194 offset:27648\n"
"v_max3_f32 v219, v219, v30, v31\n"
"v_mfma_f32_16x16x32_bf16 v[48:51], a[0:3], v[160:163], 0\n"
"s_waitcnt lgkmcnt(2)\n"
"v_mfma_f32_16x16x32_bf16 v[48:51], a[4:7], v[164:167], v[48:51]\n"
"v_mfma_f32_16x16x32_bf16 v[52:55], a[32:35], v[160:163], 0\n"
"v_mfma_f32_16x16x32_bf16 v[52:55], a[36:39], v[164:167], v[52:55]\n"
"v_mfma_f32_16x16x32_bf16 v[56:59], a[64:67], v[160:163], 0\n"
"v_mfma_f32_16x16x32_bf16 v[56:59], a[68:71], v[164:167], v[56:59]\n"
"buffer_load_dwordx4 v205, s[24:27], s42 offen offset:1024 nt lds\n"
"v_mfma_f32_16x16x32_bf16 v[60:63], a[96:99], v[160:163], 0\n"
"ds_read_b128 v[160:163], v194 offset:28672\n"
"v_mfma_f32_16x16x32_bf16 v[60:63], a[100:103], v[164:167], v[60:63]\n"
"s_waitcnt lgkmcnt(2)\n"
"ds_read_b128 v[164:167], v194 offset:29696\n"
"v_mfma_f32_16x16x32_bf16 v[48:51], a[8:11], v[168:171], v[48:51]\n"
"s_waitcnt lgkmcnt(2)\n"
"v_mfma_f32_16x16x32_bf16 v[48:51], a[12:15], v[172:175], v[48:51]\n"
"v_max_f32 v220, v32, v33\n"
"v_mfma_f32_16x16x32_bf16 v[52:55], a[40:43], v[168:171], v[52:55]\n"
"v_max3_f32 v220, v220, v34, v35\n"
"v_mfma_f32_16x16x32_bf16 v[52:55], a[44:47], v[172:175], v[52:55]\n"
"v_max3_f32 v220, v220, v36, v37\n"
"v_mfma_f32_16x16x32_bf16 v[56:59], a[72:75], v[168:171], v[56:59]\n"
"v_mfma_f32_16x16x32_bf16 v[56:59], a[76:79], v[172:175], v[56:59]\n"
"buffer_load_dwordx4 v206, s[24:27], s42 offen offset:2048 nt lds\n"
"v_mfma_f32_16x16x32_bf16 v[60:63], a[104:107], v[168:171], v[60:63]\n"
"ds_read_b128 v[168:171], v194 offset:30720\n"
"v_mfma_f32_16x16x32_bf16 v[60:63], a[108:111], v[172:175], v[60:63]\n"
"s_waitcnt lgkmcnt(2)\n"
"ds_read_b128 v[172:175], v194 offset:31744\n"
"v_mfma_f32_16x16x32_bf16 v[48:51], a[16:19], v[160:163], v[48:51]\n"
"s_waitcnt lgkmcnt(2)\n"
"v_mfma_f32_16x16x32_bf16 v[48:51], a[20:23], v[164:167], v[48:51]\n"
"v_max3_f32 v220, v220, v38, v39\n"
"v_mfma_f32_16x16x32_bf16 v[52:55], a[48:51], v[160:163], v[52:55]\n"
"v_mfma_f32_16x16x32_bf16 v[52:55], a[52:55], v[164:167], v[52:55]\n"
"v_max3_f32 v220, v220, v40, v41\n"
"v_mfma_f32_16x16x32_bf16 v[56:59], a[80:83], v[160:163], v[56:59]\n"
"v_max3_f32 v220, v220, v42, v43\n"
"v_mfma_f32_16x16x32_bf16 v[56:59], a[84:87], v[164:167], v[56:59]\n"
"buffer_load_dwordx4 v207, s[24:27], s42 offen offset:3072 nt lds\n"
"v_mfma_f32_16x16x32_bf16 v[60:63], a[112:115], v[160:163], v[60:63]\n"
"ds_read_b128 v[160:163], v194 offset:32768\n"
"v_mfma_f32_16x16x32_bf16 v[60:63], a[116:119], v[164:167], v[60:63]\n"
"s_waitcnt lgkmcnt(2)\n"
"ds_read_b128 v[164:167], v194 offset:33792\n"
"v_mfma_f32_16x16x32_bf16 v[48:51], a[24:27], v[168:171], v[48:51]\n"
"s_waitcnt lgkmcnt(2)\n"
"v_mfma_f32_16x16x32_bf16 v[48:51], a[28:31], v[172:175], v[48:51]\n"
"v_mfma_f32_16x16x32_bf16 v[52:55], a[56:59], v[168:171], v[52:55]\n"
"v_max3_f32 v220, v220, v44, v45\n"
"v_mfma_f32_16x16x32_bf16 v[52:55], a[60:63], v[172:175], v[52:55]\n"
"s_waitcnt vmcnt(8)\n"
"ds_read_b128 a[144:147], v200 offset:12672\n"
"v_mfma_f32_16x16x32_bf16 v[56:59], a[88:91], v[168:171], v[56:59]\n"
"ds_read_b128 a[148:151], v201 offset:12672\n"
"v_mfma_f32_16x16x32_bf16 v[56:59], a[92:95], v[172:175], v[56:59]\n"
"v_max3_f32 v220, v220, v46, v47\n"
"v_mfma_f32_16x16x32_bf16 v[60:63], a[120:123], v[168:171], v[60:63]\n"
"ds_read_b128 v[168:171], v194 offset:34816\n"
"ds_read_b128 a[152:155], v202 offset:12672\n"
"v_mfma_f32_16x16x32_bf16 v[60:63], a[124:127], v[172:175], v[60:63]\n"
"s_waitcnt lgkmcnt(5)\n"
"ds_read_b128 v[172:175], v194 offset:35840\n"
"ds_read_b128 a[156:159], v203 offset:12672\n"
"v_mfma_f32_16x16x32_bf16 v[64:67], a[0:3], v[160:163], 0\n"
"s_waitcnt lgkmcnt(6)\n"
"v_mfma_f32_16x16x32_bf16 v[64:67], a[4:7], v[164:167], v[64:67]\n"
"v_mfma_f32_16x16x32_bf16 v[68:71], a[32:35], v[160:163], 0\n"
"v_mfma_f32_16x16x32_bf16 v[68:71], a[36:39], v[164:167], v[68:71]\n"
"v_mfma_f32_16x16x32_bf16 v[72:75], a[64:67], v[160:163], 0\n"
"v_mfma_f32_16x16x32_bf16 v[72:75], a[68:71], v[164:167], v[72:75]\n"
"v_mfma_f32_16x16x32_bf16 v[76:79], a[96:99], v[160:163], 0\n"
"ds_read_b128 v[160:163], v194 offset:36864\n"
"v_mfma_f32_16x16x32_bf16 v[76:79], a[100:103], v[164:167], v[76:79]\n"
"s_waitcnt lgkmcnt(4)\n"
"ds_read_b128 v[164:167], v194 offset:37888\n"
"v_mfma_f32_16x16x32_bf16 v[64:67], a[8:11], v[168:171], v[64:67]\n"
"s_waitcnt lgkmcnt(3)\n"
"v_mfma_f32_16x16x32_bf16 v[64:67], a[12:15], v[172:175], v[64:67]\n"
"v_mfma_f32_16x16x32_bf16 v[68:71], a[40:43], v[168:171], v[68:71]\n"
"v_max_f32 v221, v48, v49\n"
"v_mfma_f32_16x16x32_bf16 v[68:71], a[44:47], v[172:175], v[68:71]\n"
"v_max3_f32 v221, v221, v50, v51\n"
"v_mfma_f32_16x16x32_bf16 v[72:75], a[72:75], v[168:171], v[72:75]\n"
"v_mfma_f32_16x16x32_bf16 v[72:75], a[76:79], v[172:175], v[72:75]\n"
"v_max3_f32 v221, v221, v52, v53\n"
"v_mfma_f32_16x16x32_bf16 v[76:79], a[104:107], v[168:171], v[76:79]\n"
"ds_read_b128 v[168:171], v194 offset:38912\n"
"v_mfma_f32_16x16x32_bf16 v[76:79], a[108:111], v[172:175], v[76:79]\n"
"s_waitcnt lgkmcnt(2)\n"
"ds_read_b128 v[172:175], v194 offset:39936\n"
"v_mfma_f32_16x16x32_bf16 v[64:67], a[16:19], v[160:163], v[64:67]\n"
"s_waitcnt lgkmcnt(2)\n"
"v_mfma_f32_16x16x32_bf16 v[64:67], a[20:23], v[164:167], v[64:67]\n"
"v_max3_f32 v221, v221, v54, v55\n"
"v_mfma_f32_16x16x32_bf16 v[68:71], a[48:51], v[160:163], v[68:71]\n"
"v_max3_f32 v221, v221, v56, v57\n"
"v_mfma_f32_16x16x32_bf16 v[68:71], a[52:55], v[164:167], v[68:71]\n"
"v_mfma_f32_16x16x32_bf16 v[72:75], a[80:83], v[160:163], v[72:75]\n"
"s_add_u32 m0, s40, 0x3180\n"
"v_max3_f32 v221, v221, v58, v59\n"
"v_mfma_f32_16x16x32_bf16 v[72:75], a[84:87], v[164:167], v[72:75]\n"
"s_mov_b32 s42, 0x30000\n"
"buffer_load_dwordx4 v204, s[24:27], s42 offen nt lds\n"
"v_mfma_f32_16x16x32_bf16 v[76:79], a[112:115], v[160:163], v[76:79]\n"
"ds_read_b128 v[160:163], v194 offset:40960\n"
"v_mfma_f32_16x16x32_bf16 v[76:79], a[116:119], v[164:167], v[76:79]\n"
"s_waitcnt lgkmcnt(2)\n"
"ds_read_b128 v[164:167], v194 offset:41984\n"
"v_mfma_f32_16x16x32_bf16 v[64:67], a[24:27], v[168:171], v[64:67]\n"
"s_waitcnt lgkmcnt(2)\n"
"v_mfma_f32_16x16x32_bf16 v[64:67], a[28:31], v[172:175], v[64:67]\n"
"v_mfma_f32_16x16x32_bf16 v[68:71], a[56:59], v[168:171], v[68:71]\n"
"v_mfma_f32_16x16x32_bf16 v[68:71], a[60:63], v[172:175], v[68:71]\n"
"v_max3_f32 v221, v221, v60, v61\n"
"v_mfma_f32_16x16x32_bf16 v[72:75], a[88:91], v[168:171], v[72:75]\n"
"v_max3_f32 v221, v221, v62, v63\n"
"v_mfma_f32_16x16x32_bf16 v[72:75], a[92:95], v[172:175], v[72:75]\n"
"buffer_load_dwordx4 v205, s[24:27], s42 offen offset:1024 nt lds\n"
"v_mfma_f32_16x16x32_bf16 v[76:79], a[120:123], v[168:171], v[76:79]\n"
"ds_read_b128 v[168:171], v194 offset:43008\n"
"v_mfma_f32_16x16x32_bf16 v[76:79], a[124:127], v[172:175], v[76:79]\n"
"s_waitcnt lgkmcnt(2)\n"
"ds_read_b128 v[172:175], v194 offset:44032\n"
"v_mfma_f32_16x16x32_bf16 v[80:83], a[0:3], v[160:163], 0\n"
"s_waitcnt lgkmcnt(2)\n"
"v_mfma_f32_16x16x32_bf16 v[80:83], a[4:7], v[164:167], v[80:83]\n"
"v_mfma_f32_16x16x32_bf16 v[84:87], a[32:35], v[160:163], 0\n"
"v_mfma_f32_16x16x32_bf16 v[84:87], a[36:39], v[164:167], v[84:87]\n"
"v_mfma_f32_16x16x32_bf16 v[88:91], a[64:67], v[160:163], 0\n"
"v_mfma_f32_16x16x32_bf16 v[88:91], a[68:71], v[164:167], v[88:91]\n"
"buffer_load_dwordx4 v206, s[24:27], s42 offen offset:2048 nt lds\n"
"v_mfma_f32_16x16x32_bf16 v[92:95], a[96:99], v[160:163], 0\n"
"ds_read_b128 v[160:163], v194 offset:45056\n"
"v_mfma_f32_16x16x32_bf16 v[92:95], a[100:103], v[164:167], v[92:95]\n"
"s_waitcnt lgkmcnt(2)\n"
"ds_read_b128 v[164:167], v194 offset:46080\n"
"v_mfma_f32_16x16x32_bf16 v[80:83], a[8:11], v[168:171], v[80:83]\n"
"s_waitcnt lgkmcnt(2)\n"
"v_mfma_f32_16x16x32_bf16 v[80:83], a[12:15], v[172:175], v[80:83]\n"
"v_max_f32 v222, v64, v65\n"
"v_mfma_f32_16x16x32_bf16 v[84:87], a[40:43], v[168:171], v[84:87]\n"
"v_max3_f32 v222, v222, v66, v67\n"
"v_mfma_f32_16x16x32_bf16 v[84:87], a[44:47], v[172:175], v[84:87]\n"
"v_mfma_f32_16x16x32_bf16 v[88:91], a[72:75], v[168:171], v[88:91]\n"
"v_max3_f32 v222, v222, v68, v69\n"
"v_mfma_f32_16x16x32_bf16 v[88:91], a[76:79], v[172:175], v[88:91]\n"
"buffer_load_dwordx4 v207, s[24:27], s42 offen offset:3072 nt lds\n"
"v_mfma_f32_16x16x32_bf16 v[92:95], a[104:107], v[168:171], v[92:95]\n"
"ds_read_b128 v[168:171], v194 offset:47104\n"
"v_mfma_f32_16x16x32_bf16 v[92:95], a[108:111], v[172:175], v[92:95]\n"
"s_waitcnt vmcnt(8) lgkmcnt(2)\n"
"ds_read_b128 v[172:175], v194 offset:48128\n"
"ds_read_b128 a[160:163], v200 offset:4224\n"
"v_mfma_f32_16x16x32_bf16 v[80:83], a[16:19], v[160:163], v[80:83]\n"
"s_waitcnt lgkmcnt(3)\n"
"ds_read_b128 a[164:167], v201 offset:4224\n"
"v_mfma_f32_16x16x32_bf16 v[80:83], a[20:23], v[164:167], v[80:83]\n"
"v_mfma_f32_16x16x32_bf16 v[84:87], a[48:51], v[160:163], v[84:87]\n"
"ds_read_b128 a[168:171], v202 offset:4224\n"
"v_mfma_f32_16x16x32_bf16 v[84:87], a[52:55], v[164:167], v[84:87]\n"
"ds_read_b128 a[172:175], v203 offset:4224\n"
"v_mfma_f32_16x16x32_bf16 v[88:91], a[80:83], v[160:163], v[88:91]\n"
"v_mfma_f32_16x16x32_bf16 v[88:91], a[84:87], v[164:167], v[88:91]\n"
"v_mfma_f32_16x16x32_bf16 v[92:95], a[112:115], v[160:163], v[92:95]\n"
"ds_read_b128 v[160:163], v194 offset:49152\n"
"v_mfma_f32_16x16x32_bf16 v[92:95], a[116:119], v[164:167], v[92:95]\n"
"s_waitcnt lgkmcnt(6)\n"
"ds_read_b128 v[164:167], v194 offset:50176\n"
"v_mfma_f32_16x16x32_bf16 v[80:83], a[24:27], v[168:171], v[80:83]\n"
"s_waitcnt lgkmcnt(6)\n"
"v_mfma_f32_16x16x32_bf16 v[80:83], a[28:31], v[172:175], v[80:83]\n"
"v_max3_f32 v222, v222, v70, v71\n"
"v_mfma_f32_16x16x32_bf16 v[84:87], a[56:59], v[168:171], v[84:87]\n"
"v_mfma_f32_16x16x32_bf16 v[84:87], a[60:63], v[172:175], v[84:87]\n"
"v_max3_f32 v222, v222, v72, v73\n"
"v_mfma_f32_16x16x32_bf16 v[88:91], a[88:91], v[168:171], v[88:91]\n"
"v_max3_f32 v222, v222, v74, v75\n"
"v_mfma_f32_16x16x32_bf16 v[88:91], a[92:95], v[172:175], v[88:91]\n"
"v_max3_f32 v222, v222, v76, v77\n"
"v_mfma_f32_16x16x32_bf16 v[92:95], a[120:123], v[168:171], v[92:95]\n"
"ds_read_b128 v[168:171], v194 offset:51200\n"
"v_mfma_f32_16x16x32_bf16 v[92:95], a[124:127], v[172:175], v[92:95]\n"
"s_waitcnt lgkmcnt(2)\n"
"ds_read_b128 v[172:175], v194 offset:52224\n"
"v_max3_f32 v222, v222, v78, v79\n"
"v_mfma_f32_16x16x32_bf16 v[96:99], a[0:3], v[160:163], 0\n"
"s_waitcnt lgkmcnt(2)\n"
"v_mfma_f32_16x16x32_bf16 v[96:99], a[4:7], v[164:167], v[96:99]\n"
"v_mfma_f32_16x16x32_bf16 v[100:103], a[32:35], v[160:163], 0\n"
"v_mfma_f32_16x16x32_bf16 v[100:103], a[36:39], v[164:167], v[100:103]\n"
"v_mfma_f32_16x16x32_bf16 v[104:107], a[64:67], v[160:163], 0\n"
"v_mfma_f32_16x16x32_bf16 v[104:107], a[68:71], v[164:167], v[104:107]\n"
"v_mfma_f32_16x16x32_bf16 v[108:111], a[96:99], v[160:163], 0\n"
"s_add_u32 m0, s40, 0x1080\n"
"ds_read_b128 v[160:163], v194 offset:53248\n"
"v_mfma_f32_16x16x32_bf16 v[108:111], a[100:103], v[164:167], v[108:111]\n"
"s_waitcnt lgkmcnt(2)\n"
"ds_read_b128 v[164:167], v194 offset:54272\n"
"v_mfma_f32_16x16x32_bf16 v[96:99], a[8:11], v[168:171], v[96:99]\n"
"s_waitcnt lgkmcnt(2)\n"
"v_mfma_f32_16x16x32_bf16 v[96:99], a[12:15], v[172:175], v[96:99]\n"
"s_mov_b32 s42, 0x30100\n"
"buffer_load_dwordx4 v204, s[24:27], s42 offen nt lds\n"
"v_mfma_f32_16x16x32_bf16 v[100:103], a[40:43], v[168:171], v[100:103]\n"
"v_mfma_f32_16x16x32_bf16 v[100:103], a[44:47], v[172:175], v[100:103]\n"
"v_mfma_f32_16x16x32_bf16 v[104:107], a[72:75], v[168:171], v[104:107]\n"
"v_max_f32 v223, v80, v81\n"
"v_mfma_f32_16x16x32_bf16 v[104:107], a[76:79], v[172:175], v[104:107]\n"
"v_max3_f32 v223, v223, v82, v83\n"
"v_mfma_f32_16x16x32_bf16 v[108:111], a[104:107], v[168:171], v[108:111]\n"
"ds_read_b128 v[168:171], v194 offset:55296\n"
"v_mfma_f32_16x16x32_bf16 v[108:111], a[108:111], v[172:175], v[108:111]\n"
"s_waitcnt lgkmcnt(2)\n"
"ds_read_b128 v[172:175], v194 offset:56320\n"
"v_mfma_f32_16x16x32_bf16 v[96:99], a[16:19], v[160:163], v[96:99]\n"
"s_waitcnt lgkmcnt(2)\n"
"v_mfma_f32_16x16x32_bf16 v[96:99], a[20:23], v[164:167], v[96:99]\n"
"buffer_load_dwordx4 v205, s[24:27], s42 offen offset:1024 nt lds\n"
"v_mfma_f32_16x16x32_bf16 v[100:103], a[48:51], v[160:163], v[100:103]\n"
"v_mfma_f32_16x16x32_bf16 v[100:103], a[52:55], v[164:167], v[100:103]\n"
"v_max3_f32 v223, v223, v84, v85\n"
"v_mfma_f32_16x16x32_bf16 v[104:107], a[80:83], v[160:163], v[104:107]\n"
"v_max3_f32 v223, v223, v86, v87\n"
"v_mfma_f32_16x16x32_bf16 v[104:107], a[84:87], v[164:167], v[104:107]\n"
"v_mfma_f32_16x16x32_bf16 v[108:111], a[112:115], v[160:163], v[108:111]\n"
"ds_read_b128 v[160:163], v194 offset:57344\n"
"v_mfma_f32_16x16x32_bf16 v[108:111], a[116:119], v[164:167], v[108:111]\n"
"s_waitcnt lgkmcnt(2)\n"
"ds_read_b128 v[164:167], v194 offset:58368\n"
"v_mfma_f32_16x16x32_bf16 v[96:99], a[24:27], v[168:171], v[96:99]\n"
"s_waitcnt lgkmcnt(2)\n"
"v_max3_f32 v223, v223, v88, v89\n"
"v_mfma_f32_16x16x32_bf16 v[96:99], a[28:31], v[172:175], v[96:99]\n"
"buffer_load_dwordx4 v206, s[24:27], s42 offen offset:2048 nt lds\n"
"v_mfma_f32_16x16x32_bf16 v[100:103], a[56:59], v[168:171], v[100:103]\n"
"v_mfma_f32_16x16x32_bf16 v[100:103], a[60:63], v[172:175], v[100:103]\n"
"v_mfma_f32_16x16x32_bf16 v[104:107], a[88:91], v[168:171], v[104:107]\n"
"v_max3_f32 v223, v223, v90, v91\n"
"v_mfma_f32_16x16x32_bf16 v[104:107], a[92:95], v[172:175], v[104:107]\n"
"v_max3_f32 v223, v223, v92, v93\n"
"v_mfma_f32_16x16x32_bf16 v[108:111], a[120:123], v[168:171], v[108:111]\n"
"ds_read_b128 v[168:171], v194 offset:59392\n"
"v_mfma_f32_16x16x32_bf16 v[108:111], a[124:127], v[172:175], v[108:111]\n"
"s_waitcnt lgkmcnt(2)\n"
"ds_read_b128 v[172:175], v194 offset:60416\n"
"v_max3_f32 v223, v223, v94, v95\n"
"v_mfma_f32_16x16x32_bf16 v[112:115], a[0:3], v[160:163], 0\n"
"s_waitcnt lgkmcnt(2)\n"
"v_mfma_f32_16x16x32_bf16 v[112:115], a[4:7], v[164:167], v[112:115]\n"
"buffer_load_dwordx4 v207, s[24:27], s42 offen offset:3072 nt lds\n"
"v_mfma_f32_16x16x32_bf16 v[116:119], a[32:35], v[160:163], 0\n"
"v_lshrrev_b32 v244, 3, v211\n"
"v_mfma_f32_16x16x32_bf16 v[116:119], a[36:39], v[164:167], v[116:119]\n"
"s_waitcnt vmcnt(8)\n"
"ds_read_b128 a[176:179], v200 offset:0\n"
"v_mfma_f32_16x16x32_bf16 v[120:123], a[64:67], v[160:163], 0\n"
"ds_read_b128 a[180:183], v201 offset:0\n"
"v_mfma_f32_16x16x32_bf16 v[120:123], a[68:71], v[164:167], v[120:123]\n"
"v_lshrrev_b32 v245, 4, v211\n"
"v_mfma_f32_16x16x32_bf16 v[124:127], a[96:99], v[160:163], 0\n"
"ds_read_b128 v[160:163], v194 offset:61440\n"
"ds_read_b128 a[184:187], v202 offset:0\n"
"v_mfma_f32_16x16x32_bf16 v[124:127], a[100:103], v[164:167], v[124:127]\n"
"s_waitcnt lgkmcnt(5)\n"
"ds_read_b128 v[164:167], v194 offset:62464\n"
"ds_read_b128 a[188:191], v203 offset:0\n"
"v_mfma_f32_16x16x32_bf16 v[112:115], a[8:11], v[168:171], v[112:115]\n"
"s_waitcnt lgkmcnt(6)\n"
"v_mfma_f32_16x16x32_bf16 v[112:115], a[12:15], v[172:175], v[112:115]\n"
"v_mfma_f32_16x16x32_bf16 v[116:119], a[40:43], v[168:171], v[116:119]\n"
"v_and_b32 v246, 7, v211\n"
"v_mfma_f32_16x16x32_bf16 v[116:119], a[44:47], v[172:175], v[116:119]\n"
"v_and_b32 v247, 1, v245\n"
"v_mfma_f32_16x16x32_bf16 v[120:123], a[72:75], v[168:171], v[120:123]\n"
"v_max_f32 v224, v96, v97\n"
"v_mfma_f32_16x16x32_bf16 v[120:123], a[76:79], v[172:175], v[120:123]\n"
"v_lshlrev_b32 v247, 2, v247\n"
"v_mfma_f32_16x16x32_bf16 v[124:127], a[104:107], v[168:171], v[124:127]\n"
"ds_read_b128 v[168:171], v194 offset:63488\n"
"v_mfma_f32_16x16x32_bf16 v[124:127], a[108:111], v[172:175], v[124:127]\n"
"s_waitcnt lgkmcnt(4)\n"
"ds_read_b128 v[172:175], v194 offset:64512\n"
"v_xor_b32 v247, v247, v246\n"
"v_mfma_f32_16x16x32_bf16 v[112:115], a[16:19], v[160:163], v[112:115]\n"
"s_waitcnt lgkmcnt(3)\n"
"v_max3_f32 v224, v224, v98, v99\n"
"v_mfma_f32_16x16x32_bf16 v[112:115], a[20:23], v[164:167], v[112:115]\n"
"v_lshlrev_b32 v247, 4, v247\n"
"v_mfma_f32_16x16x32_bf16 v[116:119], a[48:51], v[160:163], v[116:119]\n"
"v_max3_f32 v224, v224, v100, v101\n"
"v_mfma_f32_16x16x32_bf16 v[116:119], a[52:55], v[164:167], v[116:119]\n"
"v_max3_f32 v224, v224, v102, v103\n"
"v_mfma_f32_16x16x32_bf16 v[120:123], a[80:83], v[160:163], v[120:123]\n"
"v_lshrrev_b32 v248, 5, v211\n"
"v_mfma_f32_16x16x32_bf16 v[120:123], a[84:87], v[164:167], v[120:123]\n"
"v_max3_f32 v224, v224, v104, v105\n"
"v_mfma_f32_16x16x32_bf16 v[124:127], a[112:115], v[160:163], v[124:127]\n"
"s_add_u32 m0, s40, 0\n"
"ds_read_b128 v[160:163], v195 offset:0\n"
"v_mfma_f32_16x16x32_bf16 v[124:127], a[116:119], v[164:167], v[124:127]\n"
"s_waitcnt lgkmcnt(2)\n"
"ds_read_b128 v[164:167], v195 offset:1024\n"
"v_mfma_f32_16x16x32_bf16 v[112:115], a[24:27], v[168:171], v[112:115]\n"
"s_waitcnt lgkmcnt(2)\n"
"v_and_b32 v249, 3, v244\n"
"v_mfma_f32_16x16x32_bf16 v[112:115], a[28:31], v[172:175], v[112:115]\n"
"s_mov_b32 s42, 0\n"
"buffer_load_dwordx4 v238, s[28:31], s42 offen nt lds\n"
"v_mfma_f32_16x16x32_bf16 v[116:119], a[56:59], v[168:171], v[116:119]\n"
"v_mfma_f32_16x16x32_bf16 v[116:119], a[60:63], v[172:175], v[116:119]\n"
"v_max3_f32 v224, v224, v106, v107\n"
"v_mfma_f32_16x16x32_bf16 v[120:123], a[88:91], v[168:171], v[120:123]\n"
"v_lshl_add_u32 v248, v248, 3, v249\n"
"v_mfma_f32_16x16x32_bf16 v[120:123], a[92:95], v[172:175], v[120:123]\n"
"v_max3_f32 v224, v224, v108, v109\n"
"v_mul_u32_u24 v248, 0x380, v248\n"
"v_mfma_f32_16x16x32_bf16 v[124:127], a[120:123], v[168:171], v[124:127]\n"
"ds_read_b128 v[168:171], v195 offset:2048\n"
"v_mfma_f32_16x16x32_bf16 v[124:127], a[124:127], v[172:175], v[124:127]\n"
"s_waitcnt lgkmcnt(2)\n"
"ds_read_b128 v[172:175], v195 offset:3072\n"
"v_max3_f32 v224, v224, v110, v111\n"
"v_mfma_f32_16x16x32_bf16 v[128:131], a[0:3], v[160:163], 0\n"
"s_waitcnt lgkmcnt(2)\n"
"v_mfma_f32_16x16x32_bf16 v[128:131], a[4:7], v[164:167], v[128:131]\n"
"buffer_load_dwordx4 v239, s[28:31], s42 offen offset:1056 nt lds\n"
"v_mfma_f32_16x16x32_bf16 v[132:135], a[32:35], v[160:163], 0\n"
"v_mfma_f32_16x16x32_bf16 v[132:135], a[36:39], v[164:167], v[132:135]\n"
"v_add_u32 v248, v248, v247\n"
"v_mfma_f32_16x16x32_bf16 v[136:139], a[64:67], v[160:163], 0\n"
"v_add_u32 v204, s46, v248\n"
"v_mfma_f32_16x16x32_bf16 v[136:139], a[68:71], v[164:167], v[136:139]\n"
"v_add_u32 v205, 0x9e0, v204\n"
"v_mfma_f32_16x16x32_bf16 v[140:143], a[96:99], v[160:163], 0\n"
"ds_read_b128 v[160:163], v195 offset:4096\n"
"v_mfma_f32_16x16x32_bf16 v[140:143], a[100:103], v[164:167], v[140:143]\n"
"s_waitcnt lgkmcnt(2)\n"
"ds_read_b128 v[164:167], v195 offset:5120\n"
"v_mfma_f32_16x16x32_bf16 v[128:131], a[8:11], v[168:171], v[128:131]\n"
"s_waitcnt lgkmcnt(2)\n"
"s_lshr_b32 s45, s44, 1\n"
"v_mfma_f32_16x16x32_bf16 v[128:131], a[12:15], v[172:175], v[128:131]\n"
"s_mul_i32 s45, s45, 0xc0\n"
"v_mfma_f32_16x16x32_bf16 v[132:135], a[40:43], v[168:171], v[132:135]\n"
"v_max_f32 v225, v112, v113\n"
"v_mfma_f32_16x16x32_bf16 v[132:135], a[44:47], v[172:175], v[132:135]\n"
"v_max3_f32 v225, v225, v114, v115\n"
"v_add_u32 v248, s45, v248\n"
"v_mfma_f32_16x16x32_bf16 v[136:139], a[72:75], v[168:171], v[136:139]\n"
"s_and_b32 s45, s44, 1\n"
"v_mfma_f32_16x16x32_bf16 v[136:139], a[76:79], v[172:175], v[136:139]\n"
"v_max3_f32 v225, v225, v116, v117\n"
"v_mfma_f32_16x16x32_bf16 v[140:143], a[104:107], v[168:171], v[140:143]\n"
"ds_read_b128 v[168:171], v195 offset:6144\n"
"v_mfma_f32_16x16x32_bf16 v[140:143], a[108:111], v[172:175], v[140:143]\n"
"s_waitcnt lgkmcnt(2)\n"
"ds_read_b128 v[172:175], v195 offset:7168\n"
"v_mfma_f32_16x16x32_bf16 v[128:131], a[16:19], v[160:163], v[128:131]\n"
"s_waitcnt lgkmcnt(2)\n"
"s_lshl_b32 s45, s45, 6\n"
"v_mfma_f32_16x16x32_bf16 v[128:131], a[20:23], v[164:167], v[128:131]\n"
"v_max3_f32 v225, v225, v118, v119\n"
"v_mfma_f32_16x16x32_bf16 v[132:135], a[48:51], v[160:163], v[132:135]\n"
"v_add_u32 v248, s45, v248\n"
"v_mfma_f32_16x16x32_bf16 v[132:135], a[52:55], v[164:167], v[132:135]\n"
"v_max3_f32 v225, v225, v120, v121\n"
"v_mfma_f32_16x16x32_bf16 v[136:139], a[80:83], v[160:163], v[136:139]\n"
"v_max3_f32 v225, v225, v122, v123\n"
"v_mfma_f32_16x16x32_bf16 v[136:139], a[84:87], v[164:167], v[136:139]\n"
"v_add_u32 v206, 0x200, v248\n"
"v_mfma_f32_16x16x32_bf16 v[140:143], a[112:115], v[160:163], v[140:143]\n"
"ds_read_b128 v[160:163], v195 offset:8192\n"
"v_mfma_f32_16x16x32_bf16 v[140:143], a[116:119], v[164:167], v[140:143]\n"
"s_waitcnt lgkmcnt(2)\n"
"ds_read_b128 v[164:167], v195 offset:9216\n"
"v_mfma_f32_16x16x32_bf16 v[128:131], a[24:27], v[168:171], v[128:131]\n"
"s_waitcnt lgkmcnt(2)\n"
"v_add_u32 v207, 0xbe0, v248\n"
"v_mfma_f32_16x16x32_bf16 v[128:131], a[28:31], v[172:175], v[128:131]\n"
"v_max3_f32 v225, v225, v124, v125\n"
"v_lshrrev_b32 v248, 5, v211\n"
"v_mfma_f32_16x16x32_bf16 v[132:135], a[56:59], v[168:171], v[132:135]\n"
"v_max3_f32 v225, v225, v126, v127\n"
"v_mfma_f32_16x16x32_bf16 v[132:135], a[60:63], v[172:175], v[132:135]\n"
"s_waitcnt vmcnt(6)\n"
"ds_read_b128 a[192:195], v200 offset:12672\n"
"v_mfma_f32_16x16x32_bf16 v[136:139], a[88:91], v[168:171], v[136:139]\n"
"s_add_u32 m0, s40, 0x840\n"
"ds_read_b128 a[196:199], v201 offset:12672\n"
"v_mfma_f32_16x16x32_bf16 v[136:139], a[92:95], v[172:175], v[136:139]\n"
"s_mov_b32 s42, 0x2000\n"
"buffer_load_dwordx4 v238, s[28:31], s42 offen nt lds\n"
"v_mfma_f32_16x16x32_bf16 v[140:143], a[120:123], v[168:171], v[140:143]\n"
"ds_read_b128 v[168:171], v195 offset:10240\n"
"ds_read_b128 a[200:203], v202 offset:12672\n"
"v_mfma_f32_16x16x32_bf16 v[140:143], a[124:127], v[172:175], v[140:143]\n"
"s_waitcnt lgkmcnt(5)\n"
"ds_read_b128 v[172:175], v195 offset:11264\n"
"ds_read_b128 a[204:207], v203 offset:12672\n"
"v_mfma_f32_16x16x32_bf16 v[144:147], a[0:3], v[160:163], 0\n"
"s_waitcnt lgkmcnt(6)\n"
"v_mfma_f32_16x16x32_bf16 v[144:147], a[4:7], v[164:167], v[144:147]\n"
"v_mfma_f32_16x16x32_bf16 v[148:151], a[32:35], v[160:163], 0\n"
"v_mfma_f32_16x16x32_bf16 v[148:151], a[36:39], v[164:167], v[148:151]\n"
"v_bfe_u32 v249, v211, 2, 2\n"
"v_mfma_f32_16x16x32_bf16 v[152:155], a[64:67], v[160:163], 0\n"
"v_lshl_add_u32 v248, v248, 2, v249\n"
"v_mfma_f32_16x16x32_bf16 v[152:155], a[68:71], v[164:167], v[152:155]\n"
"buffer_load_dwordx4 v239, s[28:31], s42 offen offset:1056 nt lds\n"
"v_mfma_f32_16x16x32_bf16 v[156:159], a[96:99], v[160:163], 0\n"
"ds_read_b128 v[160:163], v195 offset:12288\n"
"v_mfma_f32_16x16x32_bf16 v[156:159], a[100:103], v[164:167], v[156:159]\n"
"s_waitcnt lgkmcnt(4)\n"
"ds_read_b128 v[164:167], v195 offset:13312\n"
"v_mfma_f32_16x16x32_bf16 v[144:147], a[8:11], v[168:171], v[144:147]\n"
"s_waitcnt lgkmcnt(3)\n"
"v_lshlrev_b32 v248, 7, v248\n"
"v_mfma_f32_16x16x32_bf16 v[144:147], a[12:15], v[172:175], v[144:147]\n"
"v_bfe_u32 v249, v211, 3, 1\n"
"v_mfma_f32_16x16x32_bf16 v[148:151], a[40:43], v[168:171], v[148:151]\n"
"v_lshl_add_u32 v248, v249, 6, v248\n"
"v_mfma_f32_16x16x32_bf16 v[148:151], a[44:47], v[172:175], v[148:151]\n"
"v_max_f32 v226, v128, v129\n"
"v_mfma_f32_16x16x32_bf16 v[152:155], a[72:75], v[168:171], v[152:155]\n"
"v_max3_f32 v226, v226, v130, v131\n"
"v_mfma_f32_16x16x32_bf16 v[152:155], a[76:79], v[172:175], v[152:155]\n"
"v_bfe_u32 v249, v211, 4, 1\n"
"v_mfma_f32_16x16x32_bf16 v[156:159], a[104:107], v[168:171], v[156:159]\n"
"ds_read_b128 v[168:171], v195 offset:14336\n"
"v_mfma_f32_16x16x32_bf16 v[156:159], a[108:111], v[172:175], v[156:159]\n"
"s_waitcnt lgkmcnt(2)\n"
"ds_read_b128 v[172:175], v195 offset:15360\n"
"v_mfma_f32_16x16x32_bf16 v[144:147], a[16:19], v[160:163], v[144:147]\n"
"s_waitcnt lgkmcnt(2)\n"
"v_mul_u32_u24 v246, 0x420, v249\n"
"v_mfma_f32_16x16x32_bf16 v[144:147], a[20:23], v[164:167], v[144:147]\n"
"v_max3_f32 v226, v226, v132, v133\n"
"v_mfma_f32_16x16x32_bf16 v[148:151], a[48:51], v[160:163], v[148:151]\n"
"v_add_u32 v246, s40, v246\n"
"v_mfma_f32_16x16x32_bf16 v[148:151], a[52:55], v[164:167], v[148:151]\n"
"v_max3_f32 v226, v226, v134, v135\n"
"v_mfma_f32_16x16x32_bf16 v[152:155], a[80:83], v[160:163], v[152:155]\n"
"v_and_b32 v249, 3, v211\n"
"v_mfma_f32_16x16x32_bf16 v[152:155], a[84:87], v[164:167], v[152:155]\n"
"v_max3_f32 v226, v226, v136, v137\n"
"v_xor_b32 v248, s45, v248\n"
"v_mfma_f32_16x16x32_bf16 v[156:159], a[112:115], v[160:163], v[156:159]\n"
"ds_read_b128 v[160:163], v194 offset:0\n"
"v_mfma_f32_16x16x32_bf16 v[156:159], a[116:119], v[164:167], v[156:159]\n"
"s_waitcnt lgkmcnt(2)\n"
"ds_read_b128 v[164:167], v194 offset:1024\n"
"v_mfma_f32_16x16x32_bf16 v[144:147], a[24:27], v[168:171], v[144:147]\n"
"s_waitcnt lgkmcnt(2)\n"
"v_add_u32 v198, v248, v246\n"
"v_mfma_f32_16x16x32_bf16 v[144:147], a[28:31], v[172:175], v[144:147]\n"
"v_lshl_add_u32 v198, v249, 4, v198\n"
".Lpad3_%=:\n"
"s_cbranch_vccnz .Lpad7_%=\n"
"v_mfma_f32_16x16x32_bf16 v[148:151], a[56:59], v[168:171], v[148:151]\n"
"v_max3_f32 v226, v226, v138, v139\n"
"v_mfma_f32_16x16x32_bf16 v[148:151], a[60:63], v[172:175], v[148:151]\n"
"v_xor_b32 v248, 64, v248\n"
"v_mfma_f32_16x16x32_bf16 v[152:155], a[88:91], v[168:171], v[152:155]\n"
"v_max3_f32 v226, v226, v140, v141\n"
"v_mfma_f32_16x16x32_bf16 v[152:155], a[92:95], v[172:175], v[152:155]\n"
"v_max3_f32 v226, v226, v142, v143\n"
"v_mfma_f32_16x16x32_bf16 v[156:159], a[120:123], v[168:171], v[156:159]\n"
"s_waitcnt vmcnt(4)\n"
"s_add_u32 m0, s40, 0x3180\n"
"ds_read_b128 v[168:171], v194 offset:2048\n"
"ds_read_b128 a[208:211], v200 offset:4224\n"
"v_mfma_f32_16x16x32_bf16 v[156:159], a[124:127], v[172:175], v[156:159]\n"
"s_waitcnt lgkmcnt(3)\n"
"ds_read_b128 v[172:175], v194 offset:3072\n"
"ds_read_b128 a[212:215], v201 offset:4224\n"
"v_mfma_f32_16x16x32_bf16 v[176:179], a[128:131], v[160:163], 0\n"
"s_waitcnt lgkmcnt(4)\n"
"ds_read_b128 a[216:219], v202 offset:4224\n"
"v_mfma_f32_16x16x32_bf16 v[176:179], a[132:135], v[164:167], v[176:179]\n"
"s_mov_b32 s42, 0x4000\n"
"buffer_load_dwordx4 v238, s[28:31], s42 offen nt lds\n"
"v_mfma_f32_16x16x32_bf16 v[180:183], a[160:163], v[160:163], 0\n"
"ds_read_b128 a[220:223], v203 offset:4224\n"
"v_mfma_f32_16x16x32_bf16 v[180:183], a[164:167], v[164:167], v[180:183]\n"
"v_mfma_f32_16x16x32_bf16 v[184:187], a[192:195], v[160:163], 0\n"
"v_mfma_f32_16x16x32_bf16 v[184:187], a[196:199], v[164:167], v[184:187]\n"
"v_add_u32 v248, v248, v246\n"
"v_mfma_f32_16x16x32_bf16 v[188:191], a[224:227], v[160:163], 0\n"
"ds_read_b128 v[160:163], v194 offset:4096\n"
"v_mfma_f32_16x16x32_bf16 v[188:191], a[228:231], v[164:167], v[188:191]\n"
"s_waitcnt lgkmcnt(6)\n"
"ds_read_b128 v[164:167], v194 offset:5120\n"
"v_mfma_f32_16x16x32_bf16 v[176:179], a[136:139], v[168:171], v[176:179]\n"
"s_waitcnt lgkmcnt(5)\n"
"v_lshl_add_u32 v248, v249, 3, v248\n"
"v_mfma_f32_16x16x32_bf16 v[176:179], a[140:143], v[172:175], v[176:179]\n"
"buffer_load_dwordx4 v239, s[28:31], s42 offen offset:1056 nt lds\n"
"v_mfma_f32_16x16x32_bf16 v[180:183], a[168:171], v[168:171], v[180:183]\n"
"v_mfma_f32_16x16x32_bf16 v[180:183], a[172:175], v[172:175], v[180:183]\n"
"s_lshr_b32 s45, s45, 1\n"
"v_mfma_f32_16x16x32_bf16 v[184:187], a[200:203], v[168:171], v[184:187]\n"
"v_add_u32 v199, s45, v248\n"
"v_mfma_f32_16x16x32_bf16 v[184:187], a[204:207], v[172:175], v[184:187]\n"
"v_max_f32 v227, v144, v145\n"
"v_mfma_f32_16x16x32_bf16 v[188:191], a[232:235], v[168:171], v[188:191]\n"
"ds_read_b128 v[168:171], v194 offset:6144\n"
"v_mfma_f32_16x16x32_bf16 v[188:191], a[236:239], v[172:175], v[188:191]\n"
"s_waitcnt lgkmcnt(2)\n"
"ds_read_b128 v[172:175], v194 offset:7168\n"
"v_mfma_f32_16x16x32_bf16 v[176:179], a[144:147], v[160:163], v[176:179]\n"
"s_waitcnt lgkmcnt(2)\n"
"s_and_b32 s45, %7, 28\n"
"v_mfma_f32_16x16x32_bf16 v[176:179], a[148:151], v[164:167], v[176:179]\n"
"s_add_u32 s45, s45, s44\n"
"s_mul_i32 s45, s45, 0x1c00\n"
"v_mfma_f32_16x16x32_bf16 v[180:183], a[176:179], v[160:163], v[180:183]\n"
"v_max3_f32 v227, v227, v146, v147\n"
"v_mfma_f32_16x16x32_bf16 v[180:183], a[180:183], v[164:167], v[180:183]\n"
"v_max3_f32 v227, v227, v148, v149\n"
"v_mfma_f32_16x16x32_bf16 v[184:187], a[208:211], v[160:163], v[184:187]\n"
"v_min_u32 v248, 55, v211\n"
"v_mfma_f32_16x16x32_bf16 v[184:187], a[212:215], v[164:167], v[184:187]\n"
"v_max3_f32 v227, v227, v150, v151\n"
"v_mfma_f32_16x16x32_bf16 v[188:191], a[240:243], v[160:163], v[188:191]\n"
"ds_read_b128 v[160:163], v194 offset:8192\n"
"v_mfma_f32_16x16x32_bf16 v[188:191], a[244:247], v[164:167], v[188:191]\n"
"s_waitcnt lgkmcnt(2)\n"
"ds_read_b128 v[164:167], v194 offset:9216\n"
"v_mfma_f32_16x16x32_bf16 v[176:179], a[152:155], v[168:171], v[176:179]\n"
"s_waitcnt lgkmcnt(2)\n"
"v_lshl_add_u32 v244, v248, 7, s45\n"
"v_mfma_f32_16x16x32_bf16 v[176:179], a[156:159], v[172:175], v[176:179]\n"
"v_max3_f32 v227, v227, v152, v153\n"
"v_mfma_f32_16x16x32_bf16 v[180:183], a[184:187], v[168:171], v[180:183]\n"
"v_mov_b32 v208, 0x3f803f80\n"
"v_mfma_f32_16x16x32_bf16 v[180:183], a[188:191], v[172:175], v[180:183]\n"
"v_max3_f32 v227, v227, v154, v155\n"
"v_mfma_f32_16x16x32_bf16 v[184:187], a[216:219], v[168:171], v[184:187]\n"
"v_max3_f32 v227, v227, v156, v157\n"
"v_mfma_f32_16x16x32_bf16 v[184:187], a[220:223], v[172:175], v[184:187]\n"
"v_mov_b32 v209, 0x3f803f80\n"
"v_mfma_f32_16x16x32_bf16 v[188:191], a[248:251], v[168:171], v[188:191]\n"
"ds_read_b128 v[168:171], v194 offset:10240\n"
"v_mov_b32 v210, 0x3f803f80\n"
"v_mov_b32 v211, 0x3f803f80\n"
"v_mfma_f32_16x16x32_bf16 v[188:191], a[252:255], v[172:175], v[188:191]\n"
"s_waitcnt lgkmcnt(2)\n"
"ds_read_b128 v[172:175], v194 offset:11264\n"
"v_max3_f32 v227, v227, v158, v159\n"
"v_mfma_f32_16x16x32_bf16 a[0:3], a[128:131], v[160:163], 0\n"
"s_waitcnt lgkmcnt(2)\n"
"v_mfma_f32_16x16x32_bf16 a[0:3], a[132:135], v[164:167], a[0:3]\n"
"v_mfma_f32_16x16x32_bf16 a[4:7], a[160:163], v[160:163], 0\n"
"v_mfma_f32_16x16x32_bf16 a[4:7], a[164:167], v[164:167], a[4:7]\n"
"v_mfma_f32_16x16x32_bf16 a[8:11], a[192:195], v[160:163], 0\n"
"s_add_u32 m0, s40, 0x1080\n"
"v_mfma_f32_16x16x32_bf16 a[8:11], a[196:199], v[164:167], a[8:11]\n"
"s_mov_b32 s42, 0x6000\n"
"buffer_load_dwordx4 v238, s[28:31], s42 offen nt lds\n"
"v_mfma_f32_16x16x32_bf16 a[12:15], a[224:227], v[160:163], 0\n"
"ds_read_b128 v[160:163], v194 offset:12288\n"
"v_mfma_f32_16x16x32_bf16 a[12:15], a[228:231], v[164:167], a[12:15]\n"
"s_waitcnt lgkmcnt(2)\n"
"ds_read_b128 v[164:167], v194 offset:13312\n"
"v_mfma_f32_16x16x32_bf16 a[0:3], a[136:139], v[168:171], a[0:3]\n"
"s_waitcnt lgkmcnt(2)\n"
"v_mfma_f32_16x16x32_bf16 a[0:3], a[140:143], v[172:175], a[0:3]\n"
"v_mfma_f32_16x16x32_bf16 a[4:7], a[168:171], v[168:171], a[4:7]\n"
"v_mfma_f32_16x16x32_bf16 a[4:7], a[172:175], v[172:175], a[4:7]\n"
"v_mfma_f32_16x16x32_bf16 a[8:11], a[200:203], v[168:171], a[8:11]\n"
"v_mfma_f32_16x16x32_bf16 a[8:11], a[204:207], v[172:175], a[8:11]\n"
"buffer_load_dwordx4 v239, s[28:31], s42 offen offset:1056 nt lds\n"
"v_mfma_f32_16x16x32_bf16 a[12:15], a[232:235], v[168:171], a[12:15]\n"
"ds_read_b128 v[168:171], v194 offset:14336\n"
"v_mfma_f32_16x16x32_bf16 a[12:15], a[236:239], v[172:175], a[12:15]\n"
"s_waitcnt lgkmcnt(2)\n"
"ds_read_b128 v[172:175], v194 offset:15360\n"
"v_mfma_f32_16x16x32_bf16 a[0:3], a[144:147], v[160:163], a[0:3]\n"
"s_waitcnt lgkmcnt(2)\n"
"v_mfma_f32_16x16x32_bf16 a[0:3], a[148:151], v[164:167], a[0:3]\n"
"v_mfma_f32_16x16x32_bf16 a[4:7], a[176:179], v[160:163], a[4:7]\n"
"v_mfma_f32_16x16x32_bf16 a[4:7], a[180:183], v[164:167], a[4:7]\n"
"v_max3_f32 v218, v218, v176, v177\n"
"v_mfma_f32_16x16x32_bf16 a[8:11], a[208:211], v[160:163], a[8:11]\n"
"v_mfma_f32_16x16x32_bf16 a[8:11], a[212:215], v[164:167], a[8:11]\n"
"v_max3_f32 v218, v218, v178, v179\n"
"v_mfma_f32_16x16x32_bf16 a[12:15], a[240:243], v[160:163], a[12:15]\n"
"ds_read_b128 v[160:163], v194 offset:16384\n"
"v_mfma_f32_16x16x32_bf16 a[12:15], a[244:247], v[164:167], a[12:15]\n"
"s_waitcnt lgkmcnt(2)\n"
"ds_read_b128 v[164:167], v194 offset:17408\n"
"v_mfma_f32_16x16x32_bf16 a[0:3], a[152:155], v[168:171], a[0:3]\n"
"s_waitcnt lgkmcnt(2)\n"
"v_mfma_f32_16x16x32_bf16 a[0:3], a[156:159], v[172:175], a[0:3]\n"
"v_mfma_f32_16x16x32_bf16 a[4:7], a[184:187], v[168:171], a[4:7]\n"
"v_max3_f32 v218, v218, v180, v181\n"
"v_mfma_f32_16x16x32_bf16 a[4:7], a[188:191], v[172:175], a[4:7]\n"
"v_max3_f32 v218, v218, v182, v183\n"
"v_mfma_f32_16x16x32_bf16 a[8:11], a[216:219], v[168:171], a[8:11]\n"
"v_mfma_f32_16x16x32_bf16 a[8:11], a[220:223], v[172:175], a[8:11]\n"
"v_max3_f32 v218, v218, v184, v185\n"
"v_mfma_f32_16x16x32_bf16 a[12:15], a[248:251], v[168:171], a[12:15]\n"
"ds_read_b128 v[168:171], v194 offset:18432\n"
"v_mfma_f32_16x16x32_bf16 a[12:15], a[252:255], v[172:175], a[12:15]\n"
"s_waitcnt lgkmcnt(2)\n"
"ds_read_b128 v[172:175], v194 offset:19456\n"
"v_mfma_f32_16x16x32_bf16 a[16:19], a[128:131], v[160:163], 0\n"
"s_waitcnt lgkmcnt(2)\n"
"v_mfma_f32_16x16x32_bf16 a[16:19], a[132:135], v[164:167], a[16:19]\n"
"v_mfma_f32_16x16x32_bf16 a[20:23], a[160:163], v[160:163], 0\n"
"v_max3_f32 v218, v218, v186, v187\n"
"v_mfma_f32_16x16x32_bf16 a[20:23], a[164:167], v[164:167], a[20:23]\n"
"v_max3_f32 v218, v218, v188, v189\n"
"v_max3_f32 v218, v218, v190, v191\n"
"v_mfma_f32_16x16x32_bf16 a[24:27], a[192:195], v[160:163], 0\n"
"v_accvgpr_read_b32 v240, a0\n"
"v_accvgpr_read_b32 v241, a1\n"
"v_mfma_f32_16x16x32_bf16 a[24:27], a[196:199], v[164:167], a[24:27]\n"
"v_max3_f32 v219, v219, v240, v241\n"
"v_accvgpr_read_b32 v242, a2\n"
"v_accvgpr_read_b32 v243, a3\n"
"v_max3_f32 v219, v219, v242, v243\n"
"v_mfma_f32_16x16x32_bf16 a[28:31], a[224:227], v[160:163], 0\n"
"s_add_u32 m0, s40, 0x18c0\n"
"ds_read_b128 v[160:163], v194 offset:20480\n"
"v_accvgpr_read_b32 v240, a4\n"
"v_mfma_f32_16x16x32_bf16 a[28:31], a[228:231], v[164:167], a[28:31]\n"
"s_waitcnt lgkmcnt(2)\n"
"ds_read_b128 v[164:167], v194 offset:21504\n"
"v_accvgpr_read_b32 v241, a5\n"
"v_max3_f32 v219, v219, v240, v241\n"
"v_accvgpr_read_b32 v242, a6\n"
"v_mfma_f32_16x16x32_bf16 a[16:19], a[136:139], v[168:171], a[16:19]\n"
"s_waitcnt lgkmcnt(2)\n"
"v_accvgpr_read_b32 v243, a7\n"
"v_max3_f32 v219, v219, v242, v243\n"
"v_mfma_f32_16x16x32_bf16 a[16:19], a[140:143], v[172:175], a[16:19]\n"
"s_mov_b32 s42, 0x8000\n"
"buffer_load_dwordx4 v238, s[28:31], s42 offen nt lds\n"
"v_accvgpr_read_b32 v240, a8\n"
"v_accvgpr_read_b32 v241, a9\n"
"v_mfma_f32_16x16x32_bf16 a[20:23], a[168:171], v[168:171], a[20:23]\n"
"v_max3_f32 v219, v219, v240, v241\n"
"v_accvgpr_read_b32 v242, a10\n"
"v_mfma_f32_16x16x32_bf16 a[20:23], a[172:175], v[172:175], a[20:23]\n"
"v_accvgpr_read_b32 v243, a11\n"
"v_max3_f32 v219, v219, v242, v243\n"
"v_accvgpr_read_b32 v240, a12\n"
"v_accvgpr_read_b32 v241, a13\n"
"v_mfma_f32_16x16x32_bf16 a[24:27], a[200:203], v[168:171], a[24:27]\n"
"v_max3_f32 v219, v219, v240, v241\n"
"v_accvgpr_read_b32 v242, a14\n"
"v_mfma_f32_16x16x32_bf16 a[24:27], a[204:207], v[172:175], a[24:27]\n"
"v_accvgpr_read_b32 v243, a15\n"
"v_max3_f32 v219, v219, v242, v243\n"
"s_nop 1\n"
"v_permlane32_swap_b32 v218, v219\n"
"v_max_f32 v242, v218, v219\n"
"v_mfma_f32_16x16x32_bf16 a[28:31], a[232:235], v[168:171], a[28:31]\n"
"ds_read_b128 v[168:171], v194 offset:22528\n"
"v_max_f32 v243, v218, v219\n"
"v_mfma_f32_16x16x32_bf16 a[28:31], a[236:239], v[172:175], a[28:31]\n"
"s_waitcnt lgkmcnt(2)\n"
"ds_read_b128 v[172:175], v194 offset:23552\n"
"v_permlane16_swap_b32 v242, v243\n"
"v_max_f32 v242, v242, v243\n"
"ds_write_b32 v216, v242 offset:0\n"
"v_mfma_f32_16x16x32_bf16 a[16:19], a[144:147], v[160:163], a[16:19]\n"
"s_waitcnt lgkmcnt(3)\n"
"v_mfma_f32_16x16x32_bf16 a[16:19], a[148:151], v[164:167], a[16:19]\n"
"buffer_load_dwordx4 v239, s[28:31], s42 offen offset:1056 nt lds\n"
"v_mfma_f32_16x16x32_bf16 a[20:23], a[176:179], v[160:163], a[20:23]\n"
"v_mfma_f32_16x16x32_bf16 a[20:23], a[180:183], v[164:167], a[20:23]\n"
"v_mfma_f32_16x16x32_bf16 a[24:27], a[208:211], v[160:163], a[24:27]\n"
"s_waitcnt lgkmcnt(0)\n"
"s_barrier\n"
"ds_read_b128 v[252:255], v217 offset:0\n"
"ds_read_b128 v[200:203], v217 offset:256\n"
"v_mfma_f32_16x16x32_bf16 a[24:27], a[212:215], v[164:167], a[24:27]\n"
"v_mfma_f32_16x16x32_bf16 a[28:31], a[240:243], v[160:163], a[28:31]\n"
"ds_read_b128 v[160:163], v194 offset:24576\n"
"v_mfma_f32_16x16x32_bf16 a[28:31], a[244:247], v[164:167], a[28:31]\n"
"ds_read_b128 v[164:167], v194 offset:25600\n"
"v_mfma_f32_16x16x32_bf16 a[16:19], a[152:155], v[168:171], a[16:19]\n"
"v_mfma_f32_16x16x32_bf16 a[16:19], a[156:159], v[172:175], a[16:19]\n"
"v_mfma_f32_16x16x32_bf16 a[20:23], a[184:187], v[168:171], a[20:23]\n"
"s_waitcnt lgkmcnt(3)\n"
"v_max3_f32 v218, v252, v253, v254\n"
"v_mfma_f32_16x16x32_bf16 a[20:23], a[188:191], v[172:175], a[20:23]\n"
"v_max_f32 v218, v218, v255\n"
"v_mul_f32 v218, s43, v218\n"
"v_mfma_f32_16x16x32_bf16 a[24:27], a[216:219], v[168:171], a[24:27]\n"
"s_waitcnt lgkmcnt(2)\n"
"v_max3_f32 v219, v200, v201, v202\n"
"v_max_f32 v219, v219, v203\n"
"v_mul_f32 v219, s43, v219\n"
"v_mfma_f32_16x16x32_bf16 a[24:27], a[220:223], v[172:175], a[24:27]\n"
"v_fma_f32 v0, v0, s43, -v218\n"
"v_exp_f32 v0, v0\n"
"v_mfma_f32_16x16x32_bf16 a[28:31], a[248:251], v[168:171], a[28:31]\n"
"ds_read_b128 v[168:171], v194 offset:26624\n"
"v_fma_f32 v1, v1, s43, -v218\n"
"v_mfma_f32_16x16x32_bf16 a[28:31], a[252:255], v[172:175], a[28:31]\n"
"s_waitcnt lgkmcnt(2)\n"
"ds_read_b128 v[172:175], v194 offset:27648\n"
"v_exp_f32 v1, v1\n"
"v_fma_f32 v2, v2, s43, -v218\n"
"v_mfma_f32_16x16x32_bf16 a[32:35], a[128:131], v[160:163], 0\n"
"s_waitcnt lgkmcnt(2)\n"
"v_exp_f32 v2, v2\n"
"v_mfma_f32_16x16x32_bf16 a[32:35], a[132:135], v[164:167], a[32:35]\n"
"v_cvt_pk_bf16_f32 v0, v0, v1\n"
"v_fma_f32 v3, v3, s43, -v218\n"
"v_exp_f32 v3, v3\n"
"v_mfma_f32_16x16x32_bf16 a[36:39], a[160:163], v[160:163], 0\n"
"v_fma_f32 v4, v4, s43, -v218\n"
"v_cvt_pk_bf16_f32 v1, v2, v3\n"
"v_mfma_f32_16x16x32_bf16 a[36:39], a[164:167], v[164:167], a[36:39]\n"
"v_exp_f32 v4, v4\n"
"v_fma_f32 v5, v5, s43, -v218\n"
"v_mfma_f32_16x16x32_bf16 a[40:43], a[192:195], v[160:163], 0\n"
"v_exp_f32 v5, v5\n"
"v_mfma_f32_16x16x32_bf16 a[40:43], a[196:199], v[164:167], a[40:43]\n"
"v_accvgpr_read_b32 v240, a16\n"
"v_fma_f32 v6, v6, s43, -v218\n"
"v_exp_f32 v6, v6\n"
"v_mfma_f32_16x16x32_bf16 a[44:47], a[224:227], v[160:163], 0\n"
"ds_read_b128 v[160:163], v194 offset:28672\n"
"v_cvt_pk_bf16_f32 v2, v4, v5\n"
"v_mfma_f32_16x16x32_bf16 a[44:47], a[228:231], v[164:167], a[44:47]\n"
"s_waitcnt lgkmcnt(2)\n"
"ds_read_b128 v[164:167], v194 offset:29696\n"
"v_fma_f32 v7, v7, s43, -v218\n"
"v_exp_f32 v7, v7\n"
"v_mfma_f32_16x16x32_bf16 a[32:35], a[136:139], v[168:171], a[32:35]\n"
"s_waitcnt lgkmcnt(2)\n"
"v_accvgpr_read_b32 v241, a17\n"
"v_cvt_pk_bf16_f32 v3, v6, v7\n"
"v_mfma_f32_16x16x32_bf16 a[32:35], a[140:143], v[172:175], a[32:35]\n"
"ds_write_b128 v214, v[0:3] offset:0\n"
"v_max3_f32 v220, v220, v240, v241\n"
"v_mfma_f32_16x16x32_bf16 v[248:251], v[208:211], v[0:3], 0\n"
"v_fma_f32 v8, v8, s43, -v218\n"
"v_fma_f32 v9, v9, s43, -v218\n"
"v_mfma_f32_16x16x32_bf16 a[36:39], a[168:171], v[168:171], a[36:39]\n"
"v_accvgpr_read_b32 v242, a18\n"
"v_fma_f32 v10, v10, s43, -v218\n"
"v_mfma_f32_16x16x32_bf16 a[36:39], a[172:175], v[172:175], a[36:39]\n"
"v_accvgpr_read_b32 v243, a19\n"
"v_exp_f32 v8, v8\n"
"v_mfma_f32_16x16x32_bf16 a[40:43], a[200:203], v[168:171], a[40:43]\n"
"s_add_u32 m0, s40, 0x39c0\n"
"v_max3_f32 v220, v220, v242, v243\n"
"v_accvgpr_read_b32 v240, a20\n"
"v_mfma_f32_16x16x32_bf16 a[40:43], a[204:207], v[172:175], a[40:43]\n"
"s_mov_b32 s42, 0xa000\n"
"buffer_load_dwordx4 v238, s[28:31], s42 offen nt lds\n"
"v_exp_f32 v9, v9\n"
"v_mfma_f32_16x16x32_bf16 a[44:47], a[232:235], v[168:171], a[44:47]\n"
"ds_read_b128 v[168:171], v194 offset:30720\n"
"v_cvt_pk_bf16_f32 v8, v8, v9\n"
"v_mfma_f32_16x16x32_bf16 a[44:47], a[236:239], v[172:175], a[44:47]\n"
"s_waitcnt lgkmcnt(3)\n"
"ds_read_b128 v[172:175], v194 offset:31744\n"
"v_exp_f32 v10, v10\n"
"v_fma_f32 v11, v11, s43, -v218\n"
"v_mfma_f32_16x16x32_bf16 a[32:35], a[144:147], v[160:163], a[32:35]\n"
"s_waitcnt lgkmcnt(3)\n"
"v_exp_f32 v11, v11\n"
"v_mfma_f32_16x16x32_bf16 a[32:35], a[148:151], v[164:167], a[32:35]\n"
"v_accvgpr_read_b32 v241, a21\n"
"v_fma_f32 v12, v12, s43, -v218\n"
"v_cvt_pk_bf16_f32 v9, v10, v11\n"
"v_mfma_f32_16x16x32_bf16 a[36:39], a[176:179], v[160:163], a[36:39]\n"
"v_max3_f32 v220, v220, v240, v241\n"
"v_add_f32 v228, v228, v248\n"
"v_mfma_f32_16x16x32_bf16 a[36:39], a[180:183], v[164:167], a[36:39]\n"
"v_accvgpr_read_b32 v242, a22\n"
"v_exp_f32 v12, v12\n"
"v_fma_f32 v13, v13, s43, -v218\n"
"v_mfma_f32_16x16x32_bf16 a[40:43], a[208:211], v[160:163], a[40:43]\n"
"v_accvgpr_read_b32 v243, a23\n"
"v_max3_f32 v220, v220, v242, v243\n"
"v_mfma_f32_16x16x32_bf16 a[40:43], a[212:215], v[164:167], a[40:43]\n"
"buffer_load_dwordx4 v239, s[28:31], s42 offen offset:1056 nt lds\n"
"v_exp_f32 v13, v13\n"
"v_mfma_f32_16x16x32_bf16 a[44:47], a[240:243], v[160:163], a[44:47]\n"
"ds_read_b128 v[160:163], v194 offset:32768\n"
"v_fma_f32 v14, v14, s43, -v218\n"
"v_mfma_f32_16x16x32_bf16 a[44:47], a[244:247], v[164:167], a[44:47]\n"
"s_waitcnt lgkmcnt(2)\n"
"ds_read_b128 v[164:167], v194 offset:33792\n"
"v_exp_f32 v14, v14\n"
"v_mfma_f32_16x16x32_bf16 a[32:35], a[152:155], v[168:171], a[32:35]\n"
"s_waitcnt lgkmcnt(2)\n"
"v_cvt_pk_bf16_f32 v10, v12, v13\n"
"v_fma_f32 v15, v15, s43, -v218\n"
"v_mfma_f32_16x16x32_bf16 a[32:35], a[156:159], v[172:175], a[32:35]\n"
"v_accvgpr_read_b32 v240, a24\n"
"v_exp_f32 v15, v15\n"
"s_nop 0\n"
"v_cvt_pk_bf16_f32 v11, v14, v15\n"
"v_mfma_f32_16x16x32_bf16 a[36:39], a[184:187], v[168:171], a[36:39]\n"
"v_accvgpr_read_b32 v241, a25\n"
"v_mfma_f32_16x16x32_bf16 a[36:39], a[188:191], v[172:175], a[36:39]\n"
"ds_write_b128 v214, v[8:11] offset:4096\n"
"v_max3_f32 v220, v220, v240, v241\n"
"v_mfma_f32_16x16x32_bf16 a[40:43], a[216:219], v[168:171], a[40:43]\n"
"v_accvgpr_read_b32 v242, a26\n"
"v_accvgpr_read_b32 v243, a27\n"
"v_mfma_f32_16x16x32_bf16 a[40:43], a[220:223], v[172:175], a[40:43]\n"
"v_max3_f32 v220, v220, v242, v243\n"
"v_mfma_f32_16x16x32_bf16 a[44:47], a[248:251], v[168:171], a[44:47]\n"
"ds_read_b128 v[168:171], v194 offset:34816\n"
"v_fma_f32 v16, v16, s43, -v219\n"
"v_mfma_f32_16x16x32_bf16 a[44:47], a[252:255], v[172:175], a[44:47]\n"
"ds_read_b128 v[172:175], v194 offset:35840\n"
"v_exp_f32 v16, v16\n"
"v_mfma_f32_16x16x32_bf16 v[248:251], v[208:211], v[8:11], 0\n"
"s_waitcnt lgkmcnt(4)\n"
"v_fma_f32 v17, v17, s43, -v219\n"
"v_exp_f32 v17, v17\n"
"v_mfma_f32_16x16x32_bf16 v[0:3], a[128:131], v[160:163], 0\n"
"s_waitcnt lgkmcnt(3)\n"
"v_accvgpr_read_b32 v240, a28\n"
"v_fma_f32 v18, v18, s43, -v219\n"
"v_mfma_f32_16x16x32_bf16 v[0:3], a[132:135], v[164:167], v[0:3]\n"
"v_accvgpr_read_b32 v241, a29\n"
"v_exp_f32 v18, v18\n"
"v_cvt_pk_bf16_f32 v16, v16, v17\n"
"v_fma_f32 v19, v19, s43, -v219\n"
"v_fma_f32 v20, v20, s43, -v219\n"
"v_mfma_f32_16x16x32_bf16 v[4:7], a[160:163], v[160:163], 0\n"
"v_max3_f32 v220, v220, v240, v241\n"
"v_add_f32 v228, v228, v248\n"
"v_mfma_f32_16x16x32_bf16 v[4:7], a[164:167], v[164:167], v[4:7]\n"
"v_accvgpr_read_b32 v242, a30\n"
"v_accvgpr_read_b32 v243, a31\n"
"v_max3_f32 v220, v220, v242, v243\n"
"v_exp_f32 v19, v19\n"
"s_nop 0\n"
"v_cvt_pk_bf16_f32 v17, v18, v19\n"
"v_mfma_f32_16x16x32_bf16 v[8:11], a[192:195], v[160:163], 0\n"
"v_accvgpr_read_b32 v240, a32\n"
"v_accvgpr_read_b32 v241, a33\n"
"v_mfma_f32_16x16x32_bf16 v[8:11], a[196:199], v[164:167], v[8:11]\n"
"v_max3_f32 v221, v221, v240, v241\n"
"v_accvgpr_read_b32 v242, a34\n"
"v_accvgpr_read_b32 v243, a35\n"
"v_exp_f32 v20, v20\n"
"v_fma_f32 v21, v21, s43, -v219\n"
"v_mfma_f32_16x16x32_bf16 v[12:15], a[224:227], v[160:163], 0\n"
"ds_read_b128 v[160:163], v194 offset:36864\n"
"v_max3_f32 v221, v221, v242, v243\n"
"v_mfma_f32_16x16x32_bf16 v[12:15], a[228:231], v[164:167], v[12:15]\n"
"s_waitcnt lgkmcnt(2)\n"
"ds_read_b128 v[164:167], v194 offset:37888\n"
"v_accvgpr_read_b32 v240, a36\n"
"v_accvgpr_read_b32 v241, a37\n"
"v_max3_f32 v221, v221, v240, v241\n"
"v_exp_f32 v21, v21\n"
"v_mfma_f32_16x16x32_bf16 v[0:3], a[136:139], v[168:171], v[0:3]\n"
"s_waitcnt lgkmcnt(2)\n"
"v_accvgpr_read_b32 v242, a38\n"
"v_accvgpr_read_b32 v243, a39\n"
"v_mfma_f32_16x16x32_bf16 v[0:3], a[140:143], v[172:175], v[0:3]\n"
"v_max3_f32 v221, v221, v242, v243\n"
"v_accvgpr_read_b32 v240, a40\n"
"v_accvgpr_read_b32 v241, a41\n"
"v_max3_f32 v221, v221, v240, v241\n"
"v_fma_f32 v22, v22, s43, -v219\n"
"v_cvt_pk_bf16_f32 v18, v20, v21\n"
"v_mfma_f32_16x16x32_bf16 v[4:7], a[168:171], v[168:171], v[4:7]\n"
"v_accvgpr_read_b32 v242, a42\n"
"v_accvgpr_read_b32 v243, a43\n"
"v_mfma_f32_16x16x32_bf16 v[4:7], a[172:175], v[172:175], v[4:7]\n"
"v_max3_f32 v221, v221, v242, v243\n"
"v_accvgpr_read_b32 v240, a44\n"
"v_accvgpr_read_b32 v241, a45\n"
"v_exp_f32 v22, v22\n"
"v_fma_f32 v23, v23, s43, -v219\n"
"v_mfma_f32_16x16x32_bf16 v[8:11], a[200:203], v[168:171], v[8:11]\n"
"v_max3_f32 v221, v221, v240, v241\n"
"v_accvgpr_read_b32 v242, a46\n"
"v_mfma_f32_16x16x32_bf16 v[8:11], a[204:207], v[172:175], v[8:11]\n"
"v_accvgpr_read_b32 v243, a47\n"
"v_max3_f32 v221, v221, v242, v243\n"
"s_nop 1\n"
"v_permlane32_swap_b32 v220, v221\n"
"v_max_f32 v242, v220, v221\n"
"v_exp_f32 v23, v23\n"
"v_mfma_f32_16x16x32_bf16 v[12:15], a[232:235], v[168:171], v[12:15]\n"
"s_add_u32 m0, s40, 0x2940\n"
"ds_read_b128 v[168:171], v194 offset:38912\n"
"v_max_f32 v243, v220, v221\n"
"v_mfma_f32_16x16x32_bf16 v[12:15], a[236:239], v[172:175], v[12:15]\n"
"s_waitcnt lgkmcnt(2)\n"
"ds_read_b128 v[172:175], v194 offset:39936\n"
"v_permlane16_swap_b32 v242, v243\n"
"v_max_f32 v242, v242, v243\n"
"ds_write_b32 v216, v242 offset:512\n"
"v_cvt_pk_bf16_f32 v19, v22, v23\n"
"v_mfma_f32_16x16x32_bf16 v[0:3], a[144:147], v[160:163], v[0:3]\n"
"s_waitcnt lgkmcnt(3)\n"
"v_fma_f32 v24, v24, s43, -v219\n"
"v_fma_f32 v25, v25, s43, -v219\n"
"v_mfma_f32_16x16x32_bf16 v[0:3], a[148:151], v[164:167], v[0:3]\n"
"s_mov_b32 s42, 0xc000\n"
"buffer_load_dwordx4 v238, s[28:31], s42 offen nt lds\n"
"v_exp_f32 v24, v24\n"
"v_mfma_f32_16x16x32_bf16 v[248:251], v[208:211], v[16:19], 0\n"
"v_exp_f32 v25, v25\n"
"v_mfma_f32_16x16x32_bf16 v[4:7], a[176:179], v[160:163], v[4:7]\n"
"v_fma_f32 v26, v26, s43, -v219\n"
"v_cvt_pk_bf16_f32 v24, v24, v25\n"
"v_mfma_f32_16x16x32_bf16 v[4:7], a[180:183], v[164:167], v[4:7]\n"
"ds_write_b128 v214, v[16:19] offset:8192\n"
"v_fma_f32 v27, v27, s43, -v219\n"
"v_mfma_f32_16x16x32_bf16 v[8:11], a[208:211], v[160:163], v[8:11]\n"
"s_waitcnt lgkmcnt(1)\n"
"s_barrier\n"
"ds_read_b128 v[252:255], v217 offset:512\n"
"ds_read_b128 v[200:203], v217 offset:768\n"
"v_mfma_f32_16x16x32_bf16 v[8:11], a[212:215], v[164:167], v[8:11]\n"
"v_exp_f32 v26, v26\n"
"v_exp_f32 v27, v27\n"
"v_fma_f32 v28, v28, s43, -v219\n"
"v_cvt_pk_bf16_f32 v25, v26, v27\n"
"v_mfma_f32_16x16x32_bf16 v[12:15], a[240:243], v[160:163], v[12:15]\n"
"ds_read_b128 v[160:163], v194 offset:40960\n"
"v_add_f32 v229, v229, v248\n"
"v_mfma_f32_16x16x32_bf16 v[12:15], a[244:247], v[164:167], v[12:15]\n"
"ds_read_b128 v[164:167], v194 offset:41984\n"
"v_exp_f32 v28, v28\n"
"v_fma_f32 v29, v29, s43, -v219\n"
"v_exp_f32 v29, v29\n"
"v_mfma_f32_16x16x32_bf16 v[0:3], a[152:155], v[168:171], v[0:3]\n"
"v_fma_f32 v30, v30, s43, -v219\n"
"v_cvt_pk_bf16_f32 v26, v28, v29\n"
"v_mfma_f32_16x16x32_bf16 v[0:3], a[156:159], v[172:175], v[0:3]\n"
"buffer_load_dwordx4 v239, s[28:31], s42 offen offset:1056 nt lds\n"
"v_exp_f32 v30, v30\n"
"v_fma_f32 v31, v31, s43, -v219\n"
"v_exp_f32 v31, v31\n"
"v_mfma_f32_16x16x32_bf16 v[4:7], a[184:187], v[168:171], v[4:7]\n"
"s_waitcnt lgkmcnt(3)\n"
"v_max3_f32 v220, v252, v253, v254\n"
"v_cvt_pk_bf16_f32 v27, v30, v31\n"
"v_mfma_f32_16x16x32_bf16 v[4:7], a[188:191], v[172:175], v[4:7]\n"
"ds_write_b128 v214, v[24:27] offset:12288\n"
"v_max_f32 v220, v220, v255\n"
"v_mul_f32 v220, s43, v220\n"
"v_mfma_f32_16x16x32_bf16 v[8:11], a[216:219], v[168:171], v[8:11]\n"
"s_waitcnt lgkmcnt(3)\n"
"v_max3_f32 v221, v200, v201, v202\n"
"v_max_f32 v221, v221, v203\n"
"v_mul_f32 v221, s43, v221\n"
"v_mfma_f32_16x16x32_bf16 v[8:11], a[220:223], v[172:175], v[8:11]\n"
"v_fma_f32 v32, v32, s43, -v220\n"
"v_fma_f32 v33, v33, s43, -v220\n"
"v_mfma_f32_16x16x32_bf16 v[248:251], v[208:211], v[24:27], 0\n"
"v_exp_f32 v32, v32\n"
"v_mfma_f32_16x16x32_bf16 v[12:15], a[248:251], v[168:171], v[12:15]\n"
"ds_read_b128 v[168:171], v194 offset:43008\n"
"v_fma_f32 v34, v34, s43, -v220\n"
"v_mfma_f32_16x16x32_bf16 v[12:15], a[252:255], v[172:175], v[12:15]\n"
"s_waitcnt lgkmcnt(3)\n"
"ds_read_b128 v[172:175], v194 offset:44032\n"
"v_fma_f32 v35, v35, s43, -v220\n"
"v_mfma_f32_16x16x32_bf16 v[16:19], a[128:131], v[160:163], 0\n"
"s_waitcnt lgkmcnt(3)\n"
"v_exp_f32 v33, v33\n"
"v_mfma_f32_16x16x32_bf16 v[16:19], a[132:135], v[164:167], v[16:19]\n"
"v_exp_f32 v34, v34\n"
"v_cvt_pk_bf16_f32 v32, v32, v33\n"
"v_mfma_f32_16x16x32_bf16 v[20:23], a[160:163], v[160:163], 0\n"
"v_exp_f32 v35, v35\n"
"v_mfma_f32_16x16x32_bf16 v[20:23], a[164:167], v[164:167], v[20:23]\n"
"v_fma_f32 v36, v36, s43, -v220\n"
"v_exp_f32 v36, v36\n"
"v_mfma_f32_16x16x32_bf16 v[24:27], a[192:195], v[160:163], 0\n"
"v_cvt_pk_bf16_f32 v33, v34, v35\n"
"v_add_f32 v229, v229, v248\n"
"v_mfma_f32_16x16x32_bf16 v[24:27], a[196:199], v[164:167], v[24:27]\n"
"v_fma_f32 v37, v37, s43, -v220\n"
"v_exp_f32 v37, v37\n"
"v_mfma_f32_16x16x32_bf16 v[28:31], a[224:227], v[160:163], 0\n"
"ds_read_b128 v[160:163], v194 offset:45056\n"
"v_fma_f32 v38, v38, s43, -v220\n"
"v_mfma_f32_16x16x32_bf16 v[28:31], a[228:231], v[164:167], v[28:31]\n"
"s_waitcnt lgkmcnt(2)\n"
"ds_read_b128 v[164:167], v194 offset:46080\n"
"v_exp_f32 v38, v38\n"
"v_mfma_f32_16x16x32_bf16 v[16:19], a[136:139], v[168:171], v[16:19]\n"
"s_waitcnt lgkmcnt(2)\n"
"v_cvt_pk_bf16_f32 v34, v36, v37\n"
"v_fma_f32 v39, v39, s43, -v220\n"
"v_mfma_f32_16x16x32_bf16 v[16:19], a[140:143], v[172:175], v[16:19]\n"
"v_max3_f32 v222, v222, v0, v1\n"
"v_exp_f32 v39, v39\n"
"v_mfma_f32_16x16x32_bf16 v[20:23], a[168:171], v[168:171], v[20:23]\n"
"v_cvt_pk_bf16_f32 v35, v38, v39\n"
"v_fma_f32 v40, v40, s43, -v220\n"
"v_mfma_f32_16x16x32_bf16 v[20:23], a[172:175], v[172:175], v[20:23]\n"
"ds_write_b128 v214, v[32:35] offset:16384\n"
"v_max3_f32 v222, v222, v2, v3\n"
"v_mfma_f32_16x16x32_bf16 v[248:251], v[208:211], v[32:35], 0\n"
"v_exp_f32 v40, v40\n"
"v_mfma_f32_16x16x32_bf16 v[24:27], a[200:203], v[168:171], v[24:27]\n"
"v_fma_f32 v41, v41, s43, -v220\n"
"v_fma_f32 v42, v42, s43, -v220\n"
"v_mfma_f32_16x16x32_bf16 v[24:27], a[204:207], v[172:175], v[24:27]\n"
"v_max3_f32 v222, v222, v4, v5\n"
"v_fma_f32 v43, v43, s43, -v220\n"
"v_mfma_f32_16x16x32_bf16 v[28:31], a[232:235], v[168:171], v[28:31]\n"
"ds_read_b128 v[168:171], v194 offset:47104\n"
"v_fma_f32 v44, v44, s43, -v220\n"
"v_mfma_f32_16x16x32_bf16 v[28:31], a[236:239], v[172:175], v[28:31]\n"
"s_waitcnt lgkmcnt(3)\n"
"ds_read_b128 v[172:175], v194 offset:48128\n"
"v_exp_f32 v41, v41\n"
"v_mfma_f32_16x16x32_bf16 v[16:19], a[144:147], v[160:163], v[16:19]\n"
"s_waitcnt lgkmcnt(3)\n"
"v_exp_f32 v42, v42\n"
"v_mfma_f32_16x16x32_bf16 v[16:19], a[148:151], v[164:167], v[16:19]\n"
"v_cvt_pk_bf16_f32 v40, v40, v41\n"
"v_exp_f32 v43, v43\n"
"v_mfma_f32_16x16x32_bf16 v[20:23], a[176:179], v[160:163], v[20:23]\n"
"v_max3_f32 v222, v222, v6, v7\n"
"v_cvt_pk_bf16_f32 v41, v42, v43\n"
"v_mfma_f32_16x16x32_bf16 v[20:23], a[180:183], v[164:167], v[20:23]\n"
"v_max3_f32 v222, v222, v8, v9\n"
"v_exp_f32 v44, v44\n"
"v_mfma_f32_16x16x32_bf16 v[24:27], a[208:211], v[160:163], v[24:27]\n"
"s_add_u32 m0, s40, 0x2100\n"
".Lpad4_%=:\n"
"s_cbranch_vccnz .Lpad8_%=\n"
"v_add_f32 v230, v230, v248\n"
"v_fma_f32 v45, v45, s43, -v220\n"
"v_mfma_f32_16x16x32_bf16 v[24:27], a[212:215], v[164:167], v[24:27]\n"
"s_mov_b32 s42, 0xe000\n"
"buffer_load_dwordx4 v238, s[28:31], s42 offen nt lds\n"
"v_fma_f32 v46, v46, s43, -v220\n"
"v_mfma_f32_16x16x32_bf16 v[28:31], a[240:243], v[160:163], v[28:31]\n"
"ds_read_b128 v[160:163], v194 offset:49152\n"
"v_fma_f32 v47, v47, s43, -v220\n"
"v_mfma_f32_16x16x32_bf16 v[28:31], a[244:247], v[164:167], v[28:31]\n"
"s_waitcnt lgkmcnt(2)\n"
"ds_read_b128 v[164:167], v194 offset:50176\n"
"v_exp_f32 v45, v45\n"
"v_mfma_f32_16x16x32_bf16 v[16:19], a[152:155], v[168:171], v[16:19]\n"
"s_waitcnt lgkmcnt(2)\n"
"v_exp_f32 v46, v46\n"
"v_mfma_f32_16x16x32_bf16 v[16:19], a[156:159], v[172:175], v[16:19]\n"
"v_cvt_pk_bf16_f32 v42, v44, v45\n"
"v_exp_f32 v47, v47\n"
"v_mfma_f32_16x16x32_bf16 v[20:23], a[184:187], v[168:171], v[20:23]\n"
"v_cvt_pk_bf16_f32 v43, v46, v47\n"
"v_mfma_f32_16x16x32_bf16 v[20:23], a[188:191], v[172:175], v[20:23]\n"
"ds_write_b128 v214, v[40:43] offset:20480\n"
"v_max3_f32 v222, v222, v10, v11\n"
"v_mfma_f32_16x16x32_bf16 v[24:27], a[216:219], v[168:171], v[24:27]\n"
"v_mfma_f32_16x16x32_bf16 v[24:27], a[220:223], v[172:175], v[24:27]\n"
"buffer_load_dwordx4 v239, s[28:31], s42 offen offset:1056 nt lds\n"
"v_mfma_f32_16x16x32_bf16 v[28:31], a[248:251], v[168:171], v[28:31]\n"
"ds_read_b128 v[168:171], v194 offset:51200\n"
"v_fma_f32 v48, v48, s43, -v221\n"
"v_mfma_f32_16x16x32_bf16 v[28:31], a[252:255], v[172:175], v[28:31]\n"
"ds_read_b128 v[172:175], v194 offset:52224\n"
"v_exp_f32 v48, v48\n"
"v_mfma_f32_16x16x32_bf16 v[248:251], v[208:211], v[40:43], 0\n"
"s_waitcnt lgkmcnt(4)\n"
"v_fma_f32 v49, v49, s43, -v221\n"
"v_fma_f32 v50, v50, s43, -v221\n"
"v_mfma_f32_16x16x32_bf16 v[32:35], a[128:131], v[160:163], 0\n"
"s_waitcnt lgkmcnt(3)\n"
"v_exp_f32 v49, v49\n"
"v_mfma_f32_16x16x32_bf16 v[32:35], a[132:135], v[164:167], v[32:35]\n"
"v_exp_f32 v50, v50\n"
"v_cvt_pk_bf16_f32 v48, v48, v49\n"
"v_fma_f32 v51, v51, s43, -v221\n"
"v_mfma_f32_16x16x32_bf16 v[36:39], a[160:163], v[160:163], 0\n"
"v_exp_f32 v51, v51\n"
"v_mfma_f32_16x16x32_bf16 v[36:39], a[164:167], v[164:167], v[36:39]\n"
"v_max3_f32 v222, v222, v12, v13\n"
"v_max3_f32 v222, v222, v14, v15\n"
"v_fma_f32 v52, v52, s43, -v221\n"
"v_cvt_pk_bf16_f32 v49, v50, v51\n"
"v_mfma_f32_16x16x32_bf16 v[40:43], a[192:195], v[160:163], 0\n"
"v_max3_f32 v223, v223, v16, v17\n"
"v_add_f32 v230, v230, v248\n"
"v_mfma_f32_16x16x32_bf16 v[40:43], a[196:199], v[164:167], v[40:43]\n"
"v_max3_f32 v223, v223, v18, v19\n"
"v_max3_f32 v223, v223, v20, v21\n"
"v_exp_f32 v52, v52\n"
"v_mfma_f32_16x16x32_bf16 v[44:47], a[224:227], v[160:163], 0\n"
"ds_read_b128 v[160:163], v194 offset:53248\n"
"v_fma_f32 v53, v53, s43, -v221\n"
"v_mfma_f32_16x16x32_bf16 v[44:47], a[228:231], v[164:167], v[44:47]\n"
"s_waitcnt lgkmcnt(2)\n"
"ds_read_b128 v[164:167], v194 offset:54272\n"
"v_max3_f32 v223, v223, v22, v23\n"
"v_exp_f32 v53, v53\n"
"v_mfma_f32_16x16x32_bf16 v[32:35], a[136:139], v[168:171], v[32:35]\n"
"s_waitcnt lgkmcnt(2)\n"
"v_max3_f32 v223, v223, v24, v25\n"
"v_fma_f32 v54, v54, s43, -v221\n"
"v_mfma_f32_16x16x32_bf16 v[32:35], a[140:143], v[172:175], v[32:35]\n"
"v_max3_f32 v223, v223, v26, v27\n"
"v_max3_f32 v223, v223, v28, v29\n"
"v_exp_f32 v54, v54\n"
"v_mfma_f32_16x16x32_bf16 v[36:39], a[168:171], v[168:171], v[36:39]\n"
"v_max3_f32 v223, v223, v30, v31\n"
"v_cvt_pk_bf16_f32 v50, v52, v53\n"
"v_mfma_f32_16x16x32_bf16 v[36:39], a[172:175], v[172:175], v[36:39]\n"
"v_permlane32_swap_b32 v222, v223\n"
"v_max_f32 v242, v222, v223\n"
"v_fma_f32 v55, v55, s43, -v221\n"
"v_exp_f32 v55, v55\n"
"v_mfma_f32_16x16x32_bf16 v[40:43], a[200:203], v[168:171], v[40:43]\n"
"v_max_f32 v243, v222, v223\n"
"v_cvt_pk_bf16_f32 v51, v54, v55\n"
"v_mfma_f32_16x16x32_bf16 v[40:43], a[204:207], v[172:175], v[40:43]\n"
"ds_write_b128 v214, v[48:51] offset:24576\n"
"v_permlane16_swap_b32 v242, v243\n"
"v_max_f32 v242, v242, v243\n"
"v_mfma_f32_16x16x32_bf16 v[248:251], v[208:211], v[48:51], 0\n"
"v_fma_f32 v56, v56, s43, -v221\n"
"v_fma_f32 v57, v57, s43, -v221\n"
"v_mfma_f32_16x16x32_bf16 v[44:47], a[232:235], v[168:171], v[44:47]\n"
"ds_read_b128 v[168:171], v194 offset:55296\n"
"v_fma_f32 v58, v58, s43, -v221\n"
"v_mfma_f32_16x16x32_bf16 v[44:47], a[236:239], v[172:175], v[44:47]\n"
"s_waitcnt lgkmcnt(3)\n"
"ds_read_b128 v[172:175], v194 offset:56320\n"
"ds_write_b32 v216, v242 offset:1024\n"
"v_fma_f32 v59, v59, s43, -v221\n"
"v_mfma_f32_16x16x32_bf16 v[32:35], a[144:147], v[160:163], v[32:35]\n"
"s_waitcnt lgkmcnt(4)\n"
"v_exp_f32 v56, v56\n"
"v_mfma_f32_16x16x32_bf16 v[32:35], a[148:151], v[164:167], v[32:35]\n"
"v_exp_f32 v57, v57\n"
"v_exp_f32 v58, v58\n"
"v_mfma_f32_16x16x32_bf16 v[36:39], a[176:179], v[160:163], v[36:39]\n"
"v_cvt_pk_bf16_f32 v56, v56, v57\n"
"v_fma_f32 v60, v60, s43, -v221\n"
"v_mfma_f32_16x16x32_bf16 v[36:39], a[180:183], v[164:167], v[36:39]\n"
"v_exp_f32 v59, v59\n"
"v_exp_f32 v60, v60\n"
"v_mfma_f32_16x16x32_bf16 v[40:43], a[208:211], v[160:163], v[40:43]\n"
"s_waitcnt lgkmcnt(0)\n"
"s_barrier\n"
"ds_read_b128 v[252:255], v217 offset:1024\n"
"ds_read_b128 v[200:203], v217 offset:1280\n"
"v_mfma_f32_16x16x32_bf16 v[40:43], a[212:215], v[164:167], v[40:43]\n"
"v_cvt_pk_bf16_f32 v57, v58, v59\n"
"v_add_f32 v231, v231, v248\n"
"v_fma_f32 v61, v61, s43, -v221\n"
"v_fma_f32 v62, v62, s43, -v221\n"
"v_mfma_f32_16x16x32_bf16 v[44:47], a[240:243], v[160:163], v[44:47]\n"
"s_add_u32 m0, s40, 0x4200\n"
"ds_read_b128 v[160:163], v194 offset:57344\n"
"v_fma_f32 v63, v63, s43, -v221\n"
"v_mfma_f32_16x16x32_bf16 v[44:47], a[244:247], v[164:167], v[44:47]\n"
"ds_read_b128 v[164:167], v194 offset:58368\n"
"v_exp_f32 v61, v61\n"
"s_nop 0\n"
"v_cvt_pk_bf16_f32 v58, v60, v61\n"
"v_mfma_f32_16x16x32_bf16 v[32:35], a[152:155], v[168:171], v[32:35]\n"
"v_exp_f32 v62, v62\n"
"v_mfma_f32_16x16x32_bf16 v[32:35], a[156:159], v[172:175], v[32:35]\n"
"s_mov_b32 s42, 0x10000\n"
"buffer_load_dwordx4 v238, s[28:31], s42 offen nt lds\n"
"v_exp_f32 v63, v63\n"
"s_nop 0\n"
"v_cvt_pk_bf16_f32 v59, v62, v63\n"
"v_mfma_f32_16x16x32_bf16 v[36:39], a[184:187], v[168:171], v[36:39]\n"
"s_waitcnt lgkmcnt(3)\n"
"v_max3_f32 v222, v252, v253, v254\n"
"v_mfma_f32_16x16x32_bf16 v[36:39], a[188:191], v[172:175], v[36:39]\n"
"ds_write_b128 v214, v[56:59] offset:28672\n"
"v_max_f32 v222, v222, v255\n"
"v_mul_f32 v222, s43, v222\n"
"v_mfma_f32_16x16x32_bf16 v[40:43], a[216:219], v[168:171], v[40:43]\n"
"s_waitcnt lgkmcnt(3)\n"
"buffer_load_dword v245, v244, s[32:35], 0 offen\n"
"v_max3_f32 v223, v200, v201, v202\n"
"v_max_f32 v223, v223, v203\n"
"v_mul_f32 v223, s43, v223\n"
"v_mfma_f32_16x16x32_bf16 v[40:43], a[220:223], v[172:175], v[40:43]\n"
"v_fma_f32 v64, v64, s43, -v222\n"
"v_exp_f32 v64, v64\n"
"v_mfma_f32_16x16x32_bf16 v[248:251], v[208:211], v[56:59], 0\n"
"v_fma_f32 v65, v65, s43, -v222\n"
"v_fma_f32 v66, v66, s43, -v222\n"
"v_mfma_f32_16x16x32_bf16 v[44:47], a[248:251], v[168:171], v[44:47]\n"
"ds_read_b128 v[168:171], v194 offset:59392\n"
"v_fma_f32 v67, v67, s43, -v222\n"
"v_mfma_f32_16x16x32_bf16 v[44:47], a[252:255], v[172:175], v[44:47]\n"
"s_waitcnt lgkmcnt(3)\n"
"ds_read_b128 v[172:175], v194 offset:60416\n"
"v_exp_f32 v65, v65\n"
"v_mfma_f32_16x16x32_bf16 v[48:51], a[128:131], v[160:163], 0\n"
"s_waitcnt lgkmcnt(3)\n"
"v_exp_f32 v66, v66\n"
"v_mfma_f32_16x16x32_bf16 v[48:51], a[132:135], v[164:167], v[48:51]\n"
"buffer_load_dwordx4 v239, s[28:31], s42 offen offset:1056 nt lds\n"
"v_cvt_pk_bf16_f32 v64, v64, v65\n"
"v_mfma_f32_16x16x32_bf16 v[52:55], a[160:163], v[160:163], 0\n"
"v_exp_f32 v67, v67\n"
"v_mfma_f32_16x16x32_bf16 v[52:55], a[164:167], v[164:167], v[52:55]\n"
"v_fma_f32 v68, v68, s43, -v222\n"
"v_exp_f32 v68, v68\n"
"v_mfma_f32_16x16x32_bf16 v[56:59], a[192:195], v[160:163], 0\n"
"v_cvt_pk_bf16_f32 v65, v66, v67\n"
"v_add_f32 v231, v231, v248\n"
"v_mfma_f32_16x16x32_bf16 v[56:59], a[196:199], v[164:167], v[56:59]\n"
"v_fma_f32 v69, v69, s43, -v222\n"
"v_exp_f32 v69, v69\n"
"v_mfma_f32_16x16x32_bf16 v[60:63], a[224:227], v[160:163], 0\n"
"ds_read_b128 v[160:163], v194 offset:61440\n"
"v_fma_f32 v70, v70, s43, -v222\n"
"v_mfma_f32_16x16x32_bf16 v[60:63], a[228:231], v[164:167], v[60:63]\n"
"s_waitcnt lgkmcnt(2)\n"
"ds_read_b128 v[164:167], v194 offset:62464\n"
"v_exp_f32 v70, v70\n"
"v_mfma_f32_16x16x32_bf16 v[48:51], a[136:139], v[168:171], v[48:51]\n"
"s_waitcnt lgkmcnt(2)\n"
"v_cvt_pk_bf16_f32 v66, v68, v69\n"
"v_fma_f32 v71, v71, s43, -v222\n"
"v_mfma_f32_16x16x32_bf16 v[48:51], a[140:143], v[172:175], v[48:51]\n"
"v_max3_f32 v224, v224, v32, v33\n"
"v_exp_f32 v71, v71\n"
"v_mfma_f32_16x16x32_bf16 v[52:55], a[168:171], v[168:171], v[52:55]\n"
"v_max3_f32 v224, v224, v34, v35\n"
"v_cvt_pk_bf16_f32 v67, v70, v71\n"
"v_mfma_f32_16x16x32_bf16 v[52:55], a[172:175], v[172:175], v[52:55]\n"
"ds_write_b128 v214, v[64:67] offset:32768\n"
"v_mfma_f32_16x16x32_bf16 v[248:251], v[208:211], v[64:67], 0\n"
"v_fma_f32 v72, v72, s43, -v222\n"
"v_fma_f32 v73, v73, s43, -v222\n"
"v_mfma_f32_16x16x32_bf16 v[56:59], a[200:203], v[168:171], v[56:59]\n"
"v_max3_f32 v224, v224, v36, v37\n"
"v_fma_f32 v74, v74, s43, -v222\n"
"v_mfma_f32_16x16x32_bf16 v[56:59], a[204:207], v[172:175], v[56:59]\n"
"v_max3_f32 v224, v224, v38, v39\n"
"v_exp_f32 v72, v72\n"
"v_mfma_f32_16x16x32_bf16 v[60:63], a[232:235], v[168:171], v[60:63]\n"
"ds_read_b128 v[168:171], v194 offset:63488\n"
"v_fma_f32 v75, v75, s43, -v222\n"
"v_mfma_f32_16x16x32_bf16 v[60:63], a[236:239], v[172:175], v[60:63]\n"
"s_waitcnt lgkmcnt(3)\n"
"ds_read_b128 v[172:175], v194 offset:64512\n"
"v_exp_f32 v73, v73\n"
"v_mfma_f32_16x16x32_bf16 v[48:51], a[144:147], v[160:163], v[48:51]\n"
"s_waitcnt lgkmcnt(3)\n"
"v_exp_f32 v74, v74\n"
"v_mfma_f32_16x16x32_bf16 v[48:51], a[148:151], v[164:167], v[48:51]\n"
"v_cvt_pk_bf16_f32 v72, v72, v73\n"
"v_exp_f32 v75, v75\n"
"v_mfma_f32_16x16x32_bf16 v[52:55], a[176:179], v[160:163], v[52:55]\n"
"s_waitcnt vmcnt(17)\n"
"ds_read_b64_tr_b16 a[48:49], v192 offset:0\n"
"v_fma_f32 v76, v76, s43, -v222\n"
"v_mfma_f32_16x16x32_bf16 v[52:55], a[180:183], v[164:167], v[52:55]\n"
"ds_read_b64_tr_b16 a[56:57], v192 offset:32\n"
"v_exp_f32 v76, v76\n"
"v_mfma_f32_16x16x32_bf16 v[56:59], a[208:211], v[160:163], v[56:59]\n"
"ds_read_b64_tr_b16 a[64:65], v193 offset:0\n"
"v_cvt_pk_bf16_f32 v73, v74, v75\n"
"v_mfma_f32_16x16x32_bf16 v[56:59], a[212:215], v[164:167], v[56:59]\n"
"v_add_f32 v232, v232, v248\n"
"v_fma_f32 v77, v77, s43, -v222\n"
"v_fma_f32 v78, v78, s43, -v222\n"
"v_mfma_f32_16x16x32_bf16 v[60:63], a[240:243], v[160:163], v[60:63]\n"
"ds_read_b128 v[160:163], v195 offset:0\n"
"ds_read_b64_tr_b16 a[72:73], v193 offset:32\n"
"v_mfma_f32_16x16x32_bf16 v[60:63], a[244:247], v[164:167], v[60:63]\n"
"s_waitcnt lgkmcnt(6)\n"
"ds_read_b128 v[164:167], v195 offset:1024\n"
"v_exp_f32 v77, v77\n"
"v_mfma_f32_16x16x32_bf16 v[48:51], a[152:155], v[168:171], v[48:51]\n"
"s_waitcnt lgkmcnt(6)\n"
"v_exp_f32 v78, v78\n"
"v_mfma_f32_16x16x32_bf16 v[48:51], a[156:159], v[172:175], v[48:51]\n"
"v_cvt_pk_bf16_f32 v74, v76, v77\n"
"v_fma_f32 v79, v79, s43, -v222\n"
"v_mfma_f32_16x16x32_bf16 v[52:55], a[184:187], v[168:171], v[52:55]\n"
"v_exp_f32 v79, v79\n"
"v_mfma_f32_16x16x32_bf16 v[52:55], a[188:191], v[172:175], v[52:55]\n"
"v_max3_f32 v224, v224, v40, v41\n"
"v_cvt_pk_bf16_f32 v75, v78, v79\n"
"v_mfma_f32_16x16x32_bf16 v[56:59], a[216:219], v[168:171], v[56:59]\n"
"v_max3_f32 v224, v224, v42, v43\n"
"v_mfma_f32_16x16x32_bf16 v[56:59], a[220:223], v[172:175], v[56:59]\n"
"ds_write_b128 v214, v[72:75] offset:36864\n"
"v_mfma_f32_16x16x32_bf16 v[60:63], a[248:251], v[168:171], v[60:63]\n"
"s_add_u32 m0, s40, 0\n"
"ds_read_b128 v[168:171], v195 offset:2048\n"
"v_fma_f32 v80, v80, s43, -v223\n"
"v_mfma_f32_16x16x32_bf16 v[60:63], a[252:255], v[172:175], v[60:63]\n"
"ds_read_b128 v[172:175], v195 offset:3072\n"
"v_exp_f32 v80, v80\n"
"v_mfma_f32_16x16x32_bf16 v[248:251], v[208:211], v[72:75], 0\n"
"s_waitcnt lgkmcnt(5)\n"
"v_fma_f32 v81, v81, s43, -v223\n"
"v_fma_f32 v82, v82, s43, -v223\n"
"v_mfma_f32_16x16x32_bf16 v[64:67], a[128:131], v[160:163], 0\n"
"s_waitcnt lgkmcnt(3)\n"
"v_exp_f32 v81, v81\n"
"v_mfma_f32_16x16x32_bf16 v[64:67], a[132:135], v[164:167], v[64:67]\n"
"s_mov_b32 s42, 0x12000\n"
"buffer_load_dwordx4 v238, s[28:31], s42 offen nt lds\n"
"v_exp_f32 v82, v82\n"
"v_cvt_pk_bf16_f32 v80, v80, v81\n"
"v_mfma_f32_16x16x32_bf16 v[68:71], a[160:163], v[160:163], 0\n"
"v_fma_f32 v83, v83, s43, -v223\n"
"v_fma_f32 v84, v84, s43, -v223\n"
"v_mfma_f32_16x16x32_bf16 v[68:71], a[164:167], v[164:167], v[68:71]\n"
"v_max3_f32 v224, v224, v44, v45\n"
"v_max3_f32 v224, v224, v46, v47\n"
"v_exp_f32 v83, v83\n"
"s_nop 0\n"
"v_cvt_pk_bf16_f32 v81, v82, v83\n"
"v_mfma_f32_16x16x32_bf16 v[72:75], a[192:195], v[160:163], 0\n"
"s_waitcnt vmcnt(16)\n"
"ds_read_b64_tr_b16 a[50:51], v192 offset:2112\n"
"v_max3_f32 v225, v225, v48, v49\n"
"v_mfma_f32_16x16x32_bf16 v[72:75], a[196:199], v[164:167], v[72:75]\n"
"v_max3_f32 v225, v225, v50, v51\n"
"v_max3_f32 v225, v225, v52, v53\n"
"v_exp_f32 v84, v84\n"
"v_add_f32 v232, v232, v248\n"
"v_mfma_f32_16x16x32_bf16 v[76:79], a[224:227], v[160:163], 0\n"
"ds_read_b128 v[160:163], v195 offset:4096\n"
"ds_read_b64_tr_b16 a[58:59], v192 offset:2144\n"
"v_mfma_f32_16x16x32_bf16 v[76:79], a[228:231], v[164:167], v[76:79]\n"
"s_waitcnt lgkmcnt(4)\n"
"ds_read_b128 v[164:167], v195 offset:5120\n"
"ds_read_b64_tr_b16 a[66:67], v193 offset:2112\n"
"v_fma_f32 v85, v85, s43, -v223\n"
"v_exp_f32 v85, v85\n"
"v_mfma_f32_16x16x32_bf16 v[64:67], a[136:139], v[168:171], v[64:67]\n"
"s_waitcnt lgkmcnt(5)\n"
"ds_read_b64_tr_b16 a[74:75], v193 offset:2144\n"
"v_max3_f32 v225, v225, v54, v55\n"
"v_mfma_f32_16x16x32_bf16 v[64:67], a[140:143], v[172:175], v[64:67]\n"
"buffer_load_dwordx4 v239, s[28:31], s42 offen offset:1056 nt lds\n"
"v_fma_f32 v86, v86, s43, -v223\n"
"v_exp_f32 v86, v86\n"
"v_mfma_f32_16x16x32_bf16 v[68:71], a[168:171], v[168:171], v[68:71]\n"
"v_max3_f32 v225, v225, v56, v57\n"
"v_max3_f32 v225, v225, v58, v59\n"
"v_mfma_f32_16x16x32_bf16 v[68:71], a[172:175], v[172:175], v[68:71]\n"
"v_max3_f32 v225, v225, v60, v61\n"
"v_max3_f32 v225, v225, v62, v63\n"
"v_cvt_pk_bf16_f32 v82, v84, v85\n"
"v_fma_f32 v87, v87, s43, -v223\n"
"v_fma_f32 v88, v88, s43, -v223\n"
"v_mfma_f32_16x16x32_bf16 v[72:75], a[200:203], v[168:171], v[72:75]\n"
"v_permlane32_swap_b32 v224, v225\n"
"v_max_f32 v242, v224, v225\n"
"v_mfma_f32_16x16x32_bf16 v[72:75], a[204:207], v[172:175], v[72:75]\n"
"v_max_f32 v243, v224, v225\n"
"s_nop 1\n"
"v_permlane16_swap_b32 v242, v243\n"
"v_exp_f32 v87, v87\n"
"s_nop 0\n"
"v_cvt_pk_bf16_f32 v83, v86, v87\n"
"v_fma_f32 v89, v89, s43, -v223\n"
"v_mfma_f32_16x16x32_bf16 v[76:79], a[232:235], v[168:171], v[76:79]\n"
"ds_read_b128 v[168:171], v195 offset:6144\n"
"v_max_f32 v242, v242, v243\n"
"v_mfma_f32_16x16x32_bf16 v[76:79], a[236:239], v[172:175], v[76:79]\n"
"ds_read_b128 v[172:175], v195 offset:7168\n"
"ds_write_b32 v216, v242 offset:1536\n"
"v_exp_f32 v88, v88\n"
"v_mfma_f32_16x16x32_bf16 v[248:251], v[208:211], v[80:83], 0\n"
"s_waitcnt lgkmcnt(7)\n"
"v_exp_f32 v89, v89\n"
"v_mfma_f32_16x16x32_bf16 v[64:67], a[144:147], v[160:163], v[64:67]\n"
"s_waitcnt lgkmcnt(5)\n"
"v_fma_f32 v90, v90, s43, -v223\n"
"v_cvt_pk_bf16_f32 v88, v88, v89\n"
"v_mfma_f32_16x16x32_bf16 v[64:67], a[148:151], v[164:167], v[64:67]\n"
"ds_write_b128 v214, v[80:83] offset:40960\n"
"v_mfma_f32_16x16x32_bf16 v[68:71], a[176:179], v[160:163], v[68:71]\n"
"v_exp_f32 v90, v90\n"
"v_mfma_f32_16x16x32_bf16 v[68:71], a[180:183], v[164:167], v[68:71]\n"
"v_fma_f32 v91, v91, s43, -v223\n"
"v_exp_f32 v91, v91\n"
"v_fma_f32 v92, v92, s43, -v223\n"
"v_cvt_pk_bf16_f32 v89, v90, v91\n"
"v_mfma_f32_16x16x32_bf16 v[72:75], a[208:211], v[160:163], v[72:75]\n"
"s_waitcnt lgkmcnt(1)\n"
"s_barrier\n"
"ds_read_b128 v[252:255], v217 offset:1536\n"
"ds_read_b128 v[200:203], v217 offset:1792\n"
"v_mfma_f32_16x16x32_bf16 v[72:75], a[212:215], v[164:167], v[72:75]\n"
"v_exp_f32 v92, v92\n"
"v_add_f32 v233, v233, v248\n"
"v_fma_f32 v93, v93, s43, -v223\n"
"v_fma_f32 v94, v94, s43, -v223\n"
"v_mfma_f32_16x16x32_bf16 v[76:79], a[240:243], v[160:163], v[76:79]\n"
"s_waitcnt vmcnt(15)\n"
"ds_read_b128 v[160:163], v195 offset:8192\n"
"ds_read_b64_tr_b16 a[52:53], v192 offset:12672\n"
"v_mfma_f32_16x16x32_bf16 v[76:79], a[244:247], v[164:167], v[76:79]\n"
"ds_read_b128 v[164:167], v195 offset:9216\n"
"ds_read_b64_tr_b16 a[60:61], v192 offset:12704\n"
"v_exp_f32 v93, v93\n"
"s_nop 0\n"
"v_cvt_pk_bf16_f32 v90, v92, v93\n"
"v_mfma_f32_16x16x32_bf16 v[64:67], a[152:155], v[168:171], v[64:67]\n"
"ds_read_b64_tr_b16 a[68:69], v193 offset:12672\n"
"v_fma_f32 v95, v95, s43, -v223\n"
"v_mfma_f32_16x16x32_bf16 v[64:67], a[156:159], v[172:175], v[64:67]\n"
"v_exp_f32 v94, v94\n"
"v_exp_f32 v95, v95\n"
"s_nop 0\n"
"v_cvt_pk_bf16_f32 v91, v94, v95\n"
"v_mfma_f32_16x16x32_bf16 v[68:71], a[184:187], v[168:171], v[68:71]\n"
"s_waitcnt lgkmcnt(6)\n"
"ds_read_b64_tr_b16 a[76:77], v193 offset:12704\n"
"v_max3_f32 v224, v252, v253, v254\n"
"v_mfma_f32_16x16x32_bf16 v[68:71], a[188:191], v[172:175], v[68:71]\n"
"ds_write_b128 v214, v[88:91] offset:45056\n"
"v_max_f32 v224, v224, v255\n"
"v_mul_f32 v224, s43, v224\n"
"v_mfma_f32_16x16x32_bf16 v[72:75], a[216:219], v[168:171], v[72:75]\n"
"s_waitcnt lgkmcnt(7)\n"
"v_max3_f32 v225, v200, v201, v202\n"
"v_max_f32 v225, v225, v203\n"
"v_mul_f32 v225, s43, v225\n"
"v_mfma_f32_16x16x32_bf16 v[72:75], a[220:223], v[172:175], v[72:75]\n"
"v_fma_f32 v96, v96, s43, -v224\n"
"v_exp_f32 v96, v96\n"
"v_mfma_f32_16x16x32_bf16 v[248:251], v[208:211], v[88:91], 0\n"
"v_fma_f32 v97, v97, s43, -v224\n"
"v_fma_f32 v98, v98, s43, -v224\n"
"v_mfma_f32_16x16x32_bf16 v[76:79], a[248:251], v[168:171], v[76:79]\n"
"s_add_u32 m0, s40, 0x840\n"
"ds_read_b128 v[168:171], v195 offset:10240\n"
"v_fma_f32 v99, v99, s43, -v224\n"
"v_mfma_f32_16x16x32_bf16 v[76:79], a[252:255], v[172:175], v[76:79]\n"
"s_waitcnt lgkmcnt(7)\n"
"ds_read_b128 v[172:175], v195 offset:11264\n"
"v_exp_f32 v97, v97\n"
"v_mfma_f32_16x16x32_bf16 v[80:83], a[128:131], v[160:163], 0\n"
"s_waitcnt lgkmcnt(6)\n"
"v_exp_f32 v98, v98\n"
"v_mfma_f32_16x16x32_bf16 v[80:83], a[132:135], v[164:167], v[80:83]\n"
"s_mov_b32 s42, 0x14000\n"
"buffer_load_dwordx4 v238, s[28:31], s42 offen nt lds\n"
"v_cvt_pk_bf16_f32 v96, v96, v97\n"
"v_mfma_f32_16x16x32_bf16 v[84:87], a[160:163], v[160:163], 0\n"
"v_exp_f32 v99, v99\n"
"v_mfma_f32_16x16x32_bf16 v[84:87], a[164:167], v[164:167], v[84:87]\n"
"v_fma_f32 v100, v100, s43, -v224\n"
"v_exp_f32 v100, v100\n"
"v_mfma_f32_16x16x32_bf16 v[88:91], a[192:195], v[160:163], 0\n"
"v_cvt_pk_bf16_f32 v97, v98, v99\n"
"v_add_f32 v233, v233, v248\n"
"v_mfma_f32_16x16x32_bf16 v[88:91], a[196:199], v[164:167], v[88:91]\n"
"v_fma_f32 v101, v101, s43, -v224\n"
"v_exp_f32 v101, v101\n"
"v_mfma_f32_16x16x32_bf16 v[92:95], a[224:227], v[160:163], 0\n"
"ds_read_b128 v[160:163], v195 offset:12288\n"
"v_fma_f32 v102, v102, s43, -v224\n"
"v_mfma_f32_16x16x32_bf16 v[92:95], a[228:231], v[164:167], v[92:95]\n"
"s_waitcnt lgkmcnt(2)\n"
"ds_read_b128 v[164:167], v195 offset:13312\n"
"v_exp_f32 v102, v102\n"
"v_mfma_f32_16x16x32_bf16 v[80:83], a[136:139], v[168:171], v[80:83]\n"
"s_waitcnt vmcnt(14) lgkmcnt(2)\n"
"ds_read_b64_tr_b16 a[54:55], v192 offset:4224\n"
"v_cvt_pk_bf16_f32 v98, v100, v101\n"
"v_mfma_f32_16x16x32_bf16 v[80:83], a[140:143], v[172:175], v[80:83]\n"
"buffer_load_dwordx4 v239, s[28:31], s42 offen offset:1056 nt lds\n"
"v_mfma_f32_16x16x32_bf16 v[84:87], a[168:171], v[168:171], v[84:87]\n"
"ds_read_b64_tr_b16 a[62:63], v192 offset:4256\n"
"v_fma_f32 v103, v103, s43, -v224\n"
"v_mfma_f32_16x16x32_bf16 v[84:87], a[172:175], v[172:175], v[84:87]\n"
"ds_read_b64_tr_b16 a[70:71], v193 offset:4224\n"
"v_exp_f32 v103, v103\n"
"v_mfma_f32_16x16x32_bf16 v[88:91], a[200:203], v[168:171], v[88:91]\n"
"ds_read_b64_tr_b16 a[78:79], v193 offset:4256\n"
"v_cvt_pk_bf16_f32 v99, v102, v103\n"
"v_mfma_f32_16x16x32_bf16 v[88:91], a[204:207], v[172:175], v[88:91]\n"
"ds_write_b128 v214, v[96:99] offset:49152\n"
"v_mfma_f32_16x16x32_bf16 v[248:251], v[208:211], v[96:99], 0\n"
"v_fma_f32 v112, v112, s43, -v225\n"
"v_fma_f32 v113, v113, s43, -v225\n"
"v_mfma_f32_16x16x32_bf16 v[92:95], a[232:235], v[168:171], v[92:95]\n"
"ds_read_b128 v[168:171], v195 offset:14336\n"
"v_fma_f32 v114, v114, s43, -v225\n"
"v_mfma_f32_16x16x32_bf16 v[92:95], a[236:239], v[172:175], v[92:95]\n"
"s_waitcnt lgkmcnt(7)\n"
"ds_read_b128 v[172:175], v195 offset:15360\n"
"v_exp_f32 v112, v112\n"
"v_mfma_f32_16x16x32_bf16 v[80:83], a[144:147], v[160:163], v[80:83]\n"
"s_waitcnt lgkmcnt(7)\n"
"v_exp_f32 v113, v113\n"
"v_mfma_f32_16x16x32_bf16 v[80:83], a[148:151], v[164:167], v[80:83]\n"
"v_exp_f32 v114, v114\n"
"v_cvt_pk_bf16_f32 v112, v112, v113\n"
"v_mfma_f32_16x16x32_bf16 v[84:87], a[176:179], v[160:163], v[84:87]\n"
"v_max3_f32 v226, v226, v64, v65\n"
"v_fma_f32 v115, v115, s43, -v225\n"
"v_mfma_f32_16x16x32_bf16 v[84:87], a[180:183], v[164:167], v[84:87]\n"
"v_max3_f32 v226, v226, v66, v67\n"
"v_exp_f32 v115, v115\n"
"v_mfma_f32_16x16x32_bf16 v[88:91], a[208:211], v[160:163], v[88:91]\n"
"s_add_u32 m0, s40, 0x3180\n"
"v_fma_f32 v116, v116, s43, -v225\n"
"v_cvt_pk_bf16_f32 v113, v114, v115\n"
"v_mfma_f32_16x16x32_bf16 v[88:91], a[212:215], v[164:167], v[88:91]\n"
"s_mov_b32 s42, 0x16000\n"
"buffer_load_dwordx4 v238, s[28:31], s42 offen nt lds\n"
"v_add_f32 v234, v234, v248\n"
"v_mfma_f32_16x16x32_bf16 v[92:95], a[240:243], v[160:163], v[92:95]\n"
"ds_read_b128 v[160:163], v194 offset:0\n"
"v_fma_f32 v117, v117, s43, -v225\n"
"v_mfma_f32_16x16x32_bf16 v[92:95], a[244:247], v[164:167], v[92:95]\n"
"s_waitcnt lgkmcnt(2)\n"
"ds_read_b128 v[164:167], v194 offset:1024\n"
"v_exp_f32 v116, v116\n"
"v_mfma_f32_16x16x32_bf16 v[80:83], a[152:155], v[168:171], v[80:83]\n"
"s_waitcnt lgkmcnt(2)\n"
"v_exp_f32 v117, v117\n"
"v_mfma_f32_16x16x32_bf16 v[80:83], a[156:159], v[172:175], v[80:83]\n"
"v_fma_f32 v118, v118, s43, -v225\n"
"v_cvt_pk_bf16_f32 v114, v116, v117\n"
"v_mfma_f32_16x16x32_bf16 v[84:87], a[184:187], v[168:171], v[84:87]\n"
"v_max3_f32 v226, v226, v68, v69\n"
"v_fma_f32 v119, v119, s43, -v225\n"
"v_mfma_f32_16x16x32_bf16 v[84:87], a[188:191], v[172:175], v[84:87]\n"
"v_max3_f32 v226, v226, v70, v71\n"
"v_exp_f32 v118, v118\n"
"v_mfma_f32_16x16x32_bf16 v[88:91], a[216:219], v[168:171], v[88:91]\n"
"v_max3_f32 v226, v226, v72, v73\n"
"v_mfma_f32_16x16x32_bf16 v[88:91], a[220:223], v[172:175], v[88:91]\n"
"buffer_load_dwordx4 v239, s[28:31], s42 offen offset:1056 nt lds\n"
"v_mfma_f32_16x16x32_bf16 v[92:95], a[248:251], v[168:171], v[92:95]\n"
"ds_read_b128 v[168:171], v194 offset:8192\n"
"v_mfma_f32_16x16x32_bf16 v[92:95], a[252:255], v[172:175], v[92:95]\n"
"s_waitcnt lgkmcnt(2)\n"
"ds_read_b128 v[172:175], v194 offset:9216\n"
"v_exp_f32 v119, v119\n"
"s_nop 0\n"
"v_cvt_pk_bf16_f32 v115, v118, v119\n"
"v_mfma_f32_16x16x32_bf16 a[96:99], a[48:51], v[160:163], 0\n"
"s_waitcnt lgkmcnt(2)\n"
"v_mfma_f32_16x16x32_bf16 a[96:99], a[52:55], v[164:167], a[96:99]\n"
"ds_write_b128 v214, v[112:115] offset:57344\n"
"v_mfma_f32_16x16x32_bf16 a[100:103], a[56:59], v[160:163], 0\n"
"v_max3_f32 v226, v226, v74, v75\n"
"v_mfma_f32_16x16x32_bf16 a[100:103], a[60:63], v[164:167], a[100:103]\n"
"v_max3_f32 v226, v226, v76, v77\n"
"v_max3_f32 v226, v226, v78, v79\n"
"v_mfma_f32_16x16x32_bf16 a[104:107], a[64:67], v[160:163], 0\n"
"v_max3_f32 v227, v227, v80, v81\n"
"v_fma_f32 v104, v104, s43, -v224\n"
"v_mfma_f32_16x16x32_bf16 a[104:107], a[68:71], v[164:167], a[104:107]\n"
"v_max3_f32 v227, v227, v82, v83\n"
"v_max3_f32 v227, v227, v84, v85\n"
"v_mfma_f32_16x16x32_bf16 v[248:251], v[208:211], v[112:115], 0\n"
"v_exp_f32 v104, v104\n"
"v_mfma_f32_16x16x32_bf16 a[108:111], a[72:75], v[160:163], 0\n"
"ds_read_b128 v[160:163], v194 offset:16384\n"
"v_max3_f32 v227, v227, v86, v87\n"
"v_mfma_f32_16x16x32_bf16 a[108:111], a[76:79], v[164:167], a[108:111]\n"
"s_waitcnt lgkmcnt(3)\n"
"ds_read_b128 v[164:167], v194 offset:17408\n"
"v_fma_f32 v105, v105, s43, -v224\n"
"v_fma_f32 v106, v106, s43, -v224\n"
"v_mfma_f32_16x16x32_bf16 a[112:115], a[48:51], v[168:171], 0\n"
"s_waitcnt lgkmcnt(3)\n"
"v_max3_f32 v227, v227, v88, v89\n"
"v_max3_f32 v227, v227, v90, v91\n"
"v_mfma_f32_16x16x32_bf16 a[112:115], a[52:55], v[172:175], a[112:115]\n"
"v_max3_f32 v227, v227, v92, v93\n"
"v_max3_f32 v227, v227, v94, v95\n"
"v_fma_f32 v107, v107, s43, -v224\n"
"v_mfma_f32_16x16x32_bf16 a[116:119], a[56:59], v[168:171], 0\n"
"v_permlane32_swap_b32 v226, v227\n"
"v_fma_f32 v108, v108, s43, -v224\n"
"v_mfma_f32_16x16x32_bf16 a[116:119], a[60:63], v[172:175], a[116:119]\n"
"v_max_f32 v242, v226, v227\n"
"v_max_f32 v243, v226, v227\n"
"v_mfma_f32_16x16x32_bf16 a[120:123], a[64:67], v[168:171], 0\n"
"s_add_u32 m0, s40, 0x1080\n"
"v_permlane16_swap_b32 v242, v243\n"
"v_max_f32 v242, v242, v243\n"
"v_mfma_f32_16x16x32_bf16 a[120:123], a[68:71], v[172:175], a[120:123]\n"
"s_mov_b32 s42, 0x18000\n"
"buffer_load_dwordx4 v238, s[28:31], s42 offen nt lds\n"
"v_exp_f32 v105, v105\n"
"v_mfma_f32_16x16x32_bf16 a[124:127], a[72:75], v[168:171], 0\n"
"ds_read_b128 v[168:171], v194 offset:24576\n"
"v_cvt_pk_bf16_f32 v104, v104, v105\n"
"v_mfma_f32_16x16x32_bf16 a[124:127], a[76:79], v[172:175], a[124:127]\n"
"s_waitcnt lgkmcnt(2)\n"
"ds_read_b128 v[172:175], v194 offset:25600\n"
"ds_write_b32 v216, v242 offset:2048\n"
"v_mfma_f32_16x16x32_bf16 a[128:131], a[48:51], v[160:163], 0\n"
"s_waitcnt lgkmcnt(3)\n"
"v_exp_f32 v106, v106\n"
"v_mfma_f32_16x16x32_bf16 a[128:131], a[52:55], v[164:167], a[128:131]\n"
"v_exp_f32 v107, v107\n"
"v_exp_f32 v108, v108\n"
"v_mfma_f32_16x16x32_bf16 a[132:135], a[56:59], v[160:163], 0\n"
"v_cvt_pk_bf16_f32 v105, v106, v107\n"
"v_add_f32 v235, v235, v248\n"
"v_mfma_f32_16x16x32_bf16 a[132:135], a[60:63], v[164:167], a[132:135]\n"
"v_fma_f32 v109, v109, s43, -v224\n"
"v_exp_f32 v109, v109\n"
"v_mfma_f32_16x16x32_bf16 a[136:139], a[64:67], v[160:163], 0\n"
"s_waitcnt lgkmcnt(0)\n"
"s_barrier\n"
"ds_read_b128 v[252:255], v217 offset:2048\n"
"ds_read_b128 v[200:203], v217 offset:2304\n"
"v_mfma_f32_16x16x32_bf16 a[136:139], a[68:71], v[164:167], a[136:139]\n"
"buffer_load_dwordx4 v239, s[28:31], s42 offen offset:1056 nt lds\n"
"v_fma_f32 v110, v110, s43, -v224\n"
"v_mfma_f32_16x16x32_bf16 a[140:143], a[72:75], v[160:163], 0\n"
"ds_read_b128 v[160:163], v194 offset:32768\n"
"v_cvt_pk_bf16_f32 v106, v108, v109\n"
"v_mfma_f32_16x16x32_bf16 a[140:143], a[76:79], v[164:167], a[140:143]\n"
"ds_read_b128 v[164:167], v194 offset:33792\n"
".Lpad5_%=:\n"
"s_cbranch_vccnz .Lpad9_%=\n"
"v_exp_f32 v110, v110\n"
"v_mfma_f32_16x16x32_bf16 a[144:147], a[48:51], v[168:171], 0\n"
"v_fma_f32 v111, v111, s43, -v224\n"
"v_mfma_f32_16x16x32_bf16 a[144:147], a[52:55], v[172:175], a[144:147]\n"
"v_exp_f32 v111, v111\n"
"s_nop 0\n"
"v_cvt_pk_bf16_f32 v107, v110, v111\n"
"v_mfma_f32_16x16x32_bf16 a[148:151], a[56:59], v[168:171], 0\n"
"s_waitcnt lgkmcnt(3)\n"
"v_max3_f32 v226, v252, v253, v254\n"
"v_mfma_f32_16x16x32_bf16 a[148:151], a[60:63], v[172:175], a[148:151]\n"
"ds_write_b128 v214, v[104:107] offset:53248\n"
"v_max_f32 v226, v226, v255\n"
"v_mul_f32 v226, s43, v226\n"
"v_mfma_f32_16x16x32_bf16 a[152:155], a[64:67], v[168:171], 0\n"
"s_waitcnt lgkmcnt(3)\n"
"v_max3_f32 v227, v200, v201, v202\n"
"v_max_f32 v227, v227, v203\n"
"v_mul_f32 v227, s43, v227\n"
"v_mfma_f32_16x16x32_bf16 a[152:155], a[68:71], v[172:175], a[152:155]\n"
"v_fma_f32 v128, v128, s43, -v226\n"
"v_exp_f32 v128, v128\n"
"v_mfma_f32_16x16x32_bf16 v[248:251], v[208:211], v[104:107], 0\n"
"v_fma_f32 v129, v129, s43, -v226\n"
"v_fma_f32 v130, v130, s43, -v226\n"
"v_mfma_f32_16x16x32_bf16 a[156:159], a[72:75], v[168:171], 0\n"
"s_waitcnt vmcnt(17)\n"
"ds_read_b128 v[168:171], v194 offset:40960\n"
"ds_read_b64_tr_b16 a[80:81], v192 offset:6336\n"
"v_mfma_f32_16x16x32_bf16 a[156:159], a[76:79], v[172:175], a[156:159]\n"
"s_waitcnt lgkmcnt(4)\n"
"ds_read_b128 v[172:175], v194 offset:41984\n"
"ds_read_b64_tr_b16 a[88:89], v192 offset:6368\n"
"v_exp_f32 v129, v129\n"
"v_exp_f32 v130, v130\n"
"v_mfma_f32_16x16x32_bf16 a[160:163], a[48:51], v[160:163], 0\n"
"s_waitcnt lgkmcnt(5)\n"
"ds_read_b64_tr_b16 v[96:97], v193 offset:6336\n"
"v_cvt_pk_bf16_f32 v128, v128, v129\n"
"v_mfma_f32_16x16x32_bf16 a[160:163], a[52:55], v[164:167], a[160:163]\n"
"v_fma_f32 v131, v131, s43, -v226\n"
"v_exp_f32 v131, v131\n"
"v_fma_f32 v132, v132, s43, -v226\n"
"v_exp_f32 v132, v132\n"
"v_mfma_f32_16x16x32_bf16 a[164:167], a[56:59], v[160:163], 0\n"
"ds_read_b64_tr_b16 v[104:105], v193 offset:6368\n"
"v_cvt_pk_bf16_f32 v129, v130, v131\n"
"v_mfma_f32_16x16x32_bf16 a[164:167], a[60:63], v[164:167], a[164:167]\n"
"v_add_f32 v234, v234, v248\n"
"v_fma_f32 v133, v133, s43, -v226\n"
"v_exp_f32 v133, v133\n"
"v_fma_f32 v134, v134, s43, -v226\n"
"v_cvt_pk_bf16_f32 v130, v132, v133\n"
"v_mfma_f32_16x16x32_bf16 a[168:171], a[64:67], v[160:163], 0\n"
"v_exp_f32 v134, v134\n"
"v_mfma_f32_16x16x32_bf16 a[168:171], a[68:71], v[164:167], a[168:171]\n"
"v_fma_f32 v135, v135, s43, -v226\n"
"v_exp_f32 v135, v135\n"
"s_nop 0\n"
"v_cvt_pk_bf16_f32 v131, v134, v135\n"
"v_fma_f32 v144, v144, s43, -v227\n"
"v_fma_f32 v145, v145, s43, -v227\n"
"v_mfma_f32_16x16x32_bf16 v[248:251], v[208:211], v[128:131], 0\n"
"v_exp_f32 v144, v144\n"
"v_mfma_f32_16x16x32_bf16 a[172:175], a[72:75], v[160:163], 0\n"
"ds_read_b128 v[160:163], v194 offset:49152\n"
"v_fma_f32 v146, v146, s43, -v227\n"
"v_mfma_f32_16x16x32_bf16 a[172:175], a[76:79], v[164:167], a[172:175]\n"
"s_waitcnt lgkmcnt(6)\n"
"ds_read_b128 v[164:167], v194 offset:50176\n"
"v_exp_f32 v145, v145\n"
"v_exp_f32 v146, v146\n"
"v_mfma_f32_16x16x32_bf16 a[176:179], a[48:51], v[168:171], 0\n"
"s_waitcnt vmcnt(15) lgkmcnt(5)\n"
"ds_read_b64_tr_b16 a[82:83], v192 offset:14784\n"
"v_cvt_pk_bf16_f32 v144, v144, v145\n"
"v_mfma_f32_16x16x32_bf16 a[176:179], a[52:55], v[172:175], a[176:179]\n"
"ds_write_b128 v215, v[128:131] offset:0\n"
"v_fma_f32 v147, v147, s43, -v227\n"
"v_mfma_f32_16x16x32_bf16 a[180:183], a[56:59], v[168:171], 0\n"
"ds_read_b64_tr_b16 a[90:91], v192 offset:14816\n"
"v_fma_f32 v148, v148, s43, -v227\n"
"v_mfma_f32_16x16x32_bf16 a[180:183], a[60:63], v[172:175], a[180:183]\n"
"ds_read_b64_tr_b16 v[98:99], v193 offset:14784\n"
"v_exp_f32 v147, v147\n"
"v_exp_f32 v148, v148\n"
"v_cvt_pk_bf16_f32 v145, v146, v147\n"
"v_mfma_f32_16x16x32_bf16 a[184:187], a[64:67], v[168:171], 0\n"
"s_add_u32 m0, s40, 0x18c0\n"
"ds_read_b64_tr_b16 v[106:107], v193 offset:14816\n"
"v_add_f32 v236, v236, v248\n"
"v_mfma_f32_16x16x32_bf16 a[184:187], a[68:71], v[172:175], a[184:187]\n"
"s_waitcnt lgkmcnt(7)\n"
"s_mov_b32 s42, 0x1a000\n"
"buffer_load_dwordx4 v238, s[28:31], s42 offen nt lds\n"
"v_fma_f32 v149, v149, s43, -v227\n"
"v_exp_f32 v149, v149\n"
"v_fma_f32 v150, v150, s43, -v227\n"
"v_mfma_f32_16x16x32_bf16 a[188:191], a[72:75], v[168:171], 0\n"
"ds_read_b128 v[168:171], v194 offset:57344\n"
"v_cvt_pk_bf16_f32 v146, v148, v149\n"
"v_mfma_f32_16x16x32_bf16 a[188:191], a[76:79], v[172:175], a[188:191]\n"
"s_waitcnt lgkmcnt(7)\n"
"ds_read_b128 v[172:175], v194 offset:58368\n"
"v_exp_f32 v150, v150\n"
"v_fma_f32 v151, v151, s43, -v227\n"
"v_exp_f32 v151, v151\n"
"v_mfma_f32_16x16x32_bf16 a[192:195], a[48:51], v[160:163], 0\n"
"s_waitcnt lgkmcnt(7)\n"
"v_cvt_pk_bf16_f32 v147, v150, v151\n"
"v_mfma_f32_16x16x32_bf16 a[192:195], a[52:55], v[164:167], a[192:195]\n"
"ds_write_b128 v215, v[144:147] offset:8192\n"
"v_mfma_f32_16x16x32_bf16 a[196:199], a[56:59], v[160:163], 0\n"
"s_waitcnt vmcnt(14)\n"
"ds_read_b64_tr_b16 a[84:85], v192 offset:10560\n"
"v_fma_f32 v120, v120, s43, -v225\n"
"v_mfma_f32_16x16x32_bf16 a[196:199], a[60:63], v[164:167], a[196:199]\n"
"ds_read_b64_tr_b16 a[92:93], v192 offset:10592\n"
"v_fma_f32 v121, v121, s43, -v225\n"
"v_mfma_f32_16x16x32_bf16 v[248:251], v[208:211], v[144:147], 0\n"
"v_exp_f32 v120, v120\n"
"v_mfma_f32_16x16x32_bf16 a[200:203], a[64:67], v[160:163], 0\n"
"ds_read_b64_tr_b16 v[100:101], v193 offset:10560\n"
"v_fma_f32 v122, v122, s43, -v225\n"
"v_mfma_f32_16x16x32_bf16 a[200:203], a[68:71], v[164:167], a[200:203]\n"
"buffer_load_dwordx4 v239, s[28:31], s42 offen offset:1056 nt lds\n"
"v_mfma_f32_16x16x32_bf16 a[204:207], a[72:75], v[160:163], 0\n"
"s_waitcnt lgkmcnt(3)\n"
"s_add_u32 m0, s40, 0x39c0\n"
"s_barrier\n"
"ds_read_b128 v[160:163], v195 offset:0\n"
"ds_read_b64_tr_b16 v[108:109], v193 offset:10592\n"
"v_mfma_f32_16x16x32_bf16 a[204:207], a[76:79], v[164:167], a[204:207]\n"
"ds_read_b128 v[164:167], v195 offset:1024\n"
"v_fma_f32 v123, v123, s43, -v225\n"
"v_mfma_f32_16x16x32_bf16 a[208:211], a[48:51], v[168:171], 0\n"
"v_exp_f32 v121, v121\n"
"v_mfma_f32_16x16x32_bf16 a[208:211], a[52:55], v[172:175], a[208:211]\n"
"s_mov_b32 s42, 0x1c000\n"
"buffer_load_dwordx4 v238, s[28:31], s42 offen nt lds\n"
"v_mfma_f32_16x16x32_bf16 a[212:215], a[56:59], v[168:171], 0\n"
"v_exp_f32 v122, v122\n"
"v_mfma_f32_16x16x32_bf16 a[212:215], a[60:63], v[172:175], a[212:215]\n"
"v_cvt_pk_bf16_f32 v120, v120, v121\n"
"v_fma_f32 v124, v124, s43, -v225\n"
"v_mfma_f32_16x16x32_bf16 a[216:219], a[64:67], v[168:171], 0\n"
"s_waitcnt vmcnt(14)\n"
"ds_read_b64_tr_b16 a[86:87], v192 offset:8448\n"
"v_add_f32 v237, v237, v248\n"
"v_mfma_f32_16x16x32_bf16 a[216:219], a[68:71], v[172:175], a[216:219]\n"
"v_exp_f32 v123, v123\n"
"v_mfma_f32_16x16x32_bf16 a[220:223], a[72:75], v[168:171], 0\n"
"ds_read_b128 v[168:171], v195 offset:8192\n"
"ds_read_b64_tr_b16 a[94:95], v192 offset:8480\n"
"v_mfma_f32_16x16x32_bf16 a[220:223], a[76:79], v[172:175], a[220:223]\n"
"s_waitcnt lgkmcnt(5)\n"
"ds_read_b128 v[172:175], v195 offset:9216\n"
"ds_read_b64_tr_b16 v[102:103], v193 offset:8448\n"
"v_cvt_pk_bf16_f32 v121, v122, v123\n"
"v_mfma_f32_16x16x32_bf16 a[224:227], a[48:51], v[160:163], 0\n"
"s_waitcnt lgkmcnt(5)\n"
"ds_read_b64_tr_b16 v[110:111], v193 offset:8480\n"
"v_fma_f32 v125, v125, s43, -v225\n"
"v_mfma_f32_16x16x32_bf16 a[224:227], a[52:55], v[164:167], a[224:227]\n"
"buffer_load_dwordx4 v239, s[28:31], s42 offen offset:1056 nt lds\n"
"v_mfma_f32_16x16x32_bf16 a[228:231], a[56:59], v[160:163], 0\n"
"v_exp_f32 v124, v124\n"
"v_mfma_f32_16x16x32_bf16 a[228:231], a[60:63], v[164:167], a[228:231]\n"
"v_exp_f32 v125, v125\n"
"v_mfma_f32_16x16x32_bf16 a[232:235], a[64:67], v[160:163], 0\n"
"s_add_u32 m0, s40, 0x2940\n"
"v_fma_f32 v126, v126, s43, -v225\n"
"v_cvt_pk_bf16_f32 v122, v124, v125\n"
"v_mfma_f32_16x16x32_bf16 a[232:235], a[68:71], v[164:167], a[232:235]\n"
"s_mov_b32 s42, 0\n"
"buffer_load_dwordx4 v204, s[32:35], s42 offen lds\n"
"v_mfma_f32_16x16x32_bf16 a[236:239], a[72:75], v[160:163], 0\n"
"ds_read_b128 v[160:163], v194 offset:2048\n"
"v_fma_f32 v127, v127, s43, -v225\n"
"v_mfma_f32_16x16x32_bf16 a[236:239], a[76:79], v[164:167], a[236:239]\n"
"s_waitcnt lgkmcnt(5)\n"
"ds_read_b128 v[164:167], v194 offset:3072\n"
"v_fma_f32 v136, v136, s43, -v226\n"
"v_mfma_f32_16x16x32_bf16 a[240:243], a[48:51], v[168:171], 0\n"
"s_waitcnt lgkmcnt(4)\n"
"v_exp_f32 v126, v126\n"
"v_mfma_f32_16x16x32_bf16 a[240:243], a[52:55], v[172:175], a[240:243]\n"
"buffer_load_dwordx4 v205, s[32:35], s42 offen offset:1056 lds\n"
"v_mfma_f32_16x16x32_bf16 a[244:247], a[56:59], v[168:171], 0\n"
"v_exp_f32 v127, v127\n"
"v_mfma_f32_16x16x32_bf16 a[244:247], a[60:63], v[172:175], a[244:247]\n"
"v_cvt_pk_bf16_f32 v123, v126, v127\n"
"v_exp_f32 v136, v136\n"
"v_mfma_f32_16x16x32_bf16 a[248:251], a[64:67], v[168:171], 0\n"
"v_fma_f32 v137, v137, s43, -v226\n"
"v_fma_f32 v138, v138, s43, -v226\n"
"v_mfma_f32_16x16x32_bf16 a[248:251], a[68:71], v[172:175], a[248:251]\n"
"ds_write_b128 v214, v[120:123] offset:61440\n"
"v_mfma_f32_16x16x32_bf16 v[248:251], v[208:211], v[120:123], 0\n"
"v_exp_f32 v137, v137\n"
"v_mfma_f32_16x16x32_bf16 a[252:255], a[72:75], v[168:171], 0\n"
"ds_read_b128 v[168:171], v194 offset:10240\n"
"v_cvt_pk_bf16_f32 v136, v136, v137\n"
"v_mfma_f32_16x16x32_bf16 a[252:255], a[76:79], v[172:175], a[252:255]\n"
"s_waitcnt lgkmcnt(3)\n"
"ds_read_b128 v[172:175], v194 offset:11264\n"
"v_fma_f32 v139, v139, s43, -v226\n"
"v_mfma_f32_16x16x32_bf16 a[96:99], a[80:83], v[160:163], a[96:99]\n"
"s_waitcnt lgkmcnt(3)\n"
"v_exp_f32 v138, v138\n"
"v_mfma_f32_16x16x32_bf16 a[96:99], a[84:87], v[164:167], a[96:99]\n"
"v_exp_f32 v139, v139\n"
"v_mfma_f32_16x16x32_bf16 a[100:103], a[88:91], v[160:163], a[100:103]\n"
"s_waitcnt vmcnt(14)\n"
"ds_read_b64_tr_b16 a[48:49], v192 offset:16896\n"
"v_fma_f32 v140, v140, s43, -v226\n"
"v_mfma_f32_16x16x32_bf16 a[100:103], a[92:95], v[164:167], a[100:103]\n"
"ds_read_b64_tr_b16 a[56:57], v192 offset:16928\n"
"v_cvt_pk_bf16_f32 v137, v138, v139\n"
"v_mfma_f32_16x16x32_bf16 a[104:107], v[96:99], v[160:163], a[104:107]\n"
"ds_read_b64_tr_b16 a[64:65], v193 offset:16896\n"
"v_add_f32 v235, v235, v248\n"
"v_mfma_f32_16x16x32_bf16 a[104:107], v[100:103], v[164:167], a[104:107]\n"
"v_exp_f32 v140, v140\n"
"v_mfma_f32_16x16x32_bf16 a[108:111], v[104:107], v[160:163], a[108:111]\n"
"ds_read_b128 v[160:163], v194 offset:18432\n"
"ds_read_b64_tr_b16 a[72:73], v193 offset:16928\n"
"v_mfma_f32_16x16x32_bf16 a[108:111], v[108:111], v[164:167], a[108:111]\n"
"s_waitcnt lgkmcnt(6)\n"
"ds_read_b128 v[164:167], v194 offset:19456\n"
"v_fma_f32 v141, v141, s43, -v226\n"
"v_mfma_f32_16x16x32_bf16 a[112:115], a[80:83], v[168:171], a[112:115]\n"
"s_waitcnt lgkmcnt(6)\n"
"v_exp_f32 v141, v141\n"
"v_mfma_f32_16x16x32_bf16 a[112:115], a[84:87], v[172:175], a[112:115]\n"
"v_fma_f32 v142, v142, s43, -v226\n"
"v_exp_f32 v142, v142\n"
"v_mfma_f32_16x16x32_bf16 a[116:119], a[88:91], v[168:171], a[116:119]\n"
"v_cvt_pk_bf16_f32 v138, v140, v141\n"
"v_fma_f32 v143, v143, s43, -v226\n"
"v_mfma_f32_16x16x32_bf16 a[116:119], a[92:95], v[172:175], a[116:119]\n"
"v_exp_f32 v143, v143\n"
"v_mfma_f32_16x16x32_bf16 a[120:123], v[96:99], v[168:171], a[120:123]\n"
"v_cvt_pk_bf16_f32 v139, v142, v143\n"
"v_fma_f32 v152, v152, s43, -v227\n"
"v_mfma_f32_16x16x32_bf16 a[120:123], v[100:103], v[172:175], a[120:123]\n"
"ds_write_b128 v215, v[136:139] offset:4096\n"
"v_mfma_f32_16x16x32_bf16 v[248:251], v[208:211], v[136:139], 0\n"
"v_exp_f32 v152, v152\n"
"v_mfma_f32_16x16x32_bf16 a[124:127], v[104:107], v[168:171], a[124:127]\n"
"ds_read_b128 v[168:171], v194 offset:26624\n"
"v_fma_f32 v153, v153, s43, -v227\n"
"v_mfma_f32_16x16x32_bf16 a[124:127], v[108:111], v[172:175], a[124:127]\n"
"s_waitcnt lgkmcnt(4)\n"
"ds_read_b128 v[172:175], v194 offset:27648\n"
"v_fma_f32 v154, v154, s43, -v227\n"
"v_mfma_f32_16x16x32_bf16 a[128:131], a[80:83], v[160:163], a[128:131]\n"
"s_waitcnt lgkmcnt(3)\n"
"v_exp_f32 v153, v153\n"
"v_mfma_f32_16x16x32_bf16 a[128:131], a[84:87], v[164:167], a[128:131]\n"
"v_exp_f32 v154, v154\n"
"v_mfma_f32_16x16x32_bf16 a[132:135], a[88:91], v[160:163], a[132:135]\n"
"v_cvt_pk_bf16_f32 v152, v152, v153\n"
"v_fma_f32 v155, v155, s43, -v227\n"
"v_mfma_f32_16x16x32_bf16 a[132:135], a[92:95], v[164:167], a[132:135]\n"
"v_exp_f32 v155, v155\n"
"v_mfma_f32_16x16x32_bf16 a[136:139], v[96:99], v[160:163], a[136:139]\n"
"s_waitcnt vmcnt(12)\n"
"s_add_u32 m0, s40, 0x4200\n"
"ds_read_b64_tr_b16 a[50:51], v192 offset:0\n"
"v_fma_f32 v156, v156, s43, -v227\n"
"v_mfma_f32_16x16x32_bf16 a[136:139], v[100:103], v[164:167], a[136:139]\n"
"s_mov_b32 s42, 0x3800\n"
"buffer_load_dwordx4 v204, s[32:35], s42 offen lds\n"
"v_mfma_f32_16x16x32_bf16 a[140:143], v[104:107], v[160:163], a[140:143]\n"
"ds_read_b128 v[160:163], v194 offset:34816\n"
"ds_read_b64_tr_b16 a[58:59], v192 offset:32\n"
"v_mfma_f32_16x16x32_bf16 a[140:143], v[108:111], v[164:167], a[140:143]\n"
"s_waitcnt lgkmcnt(4)\n"
"ds_read_b128 v[164:167], v194 offset:35840\n"
"ds_read_b64_tr_b16 a[66:67], v193 offset:0\n"
"v_mfma_f32_16x16x32_bf16 a[144:147], a[80:83], v[168:171], a[144:147]\n"
"s_waitcnt lgkmcnt(5)\n"
"ds_read_b64_tr_b16 a[74:75], v193 offset:32\n"
"v_cvt_pk_bf16_f32 v153, v154, v155\n"
"v_mfma_f32_16x16x32_bf16 a[144:147], a[84:87], v[172:175], a[144:147]\n"
"buffer_load_dwordx4 v205, s[32:35], s42 offen offset:1056 lds\n"
"v_add_f32 v236, v236, v248\n"
"v_mfma_f32_16x16x32_bf16 a[148:151], a[88:91], v[168:171], a[148:151]\n"
"v_exp_f32 v156, v156\n"
"v_mfma_f32_16x16x32_bf16 a[148:151], a[92:95], v[172:175], a[148:151]\n"
"v_fma_f32 v157, v157, s43, -v227\n"
"v_fma_f32 v158, v158, s43, -v227\n"
"v_mfma_f32_16x16x32_bf16 a[152:155], v[96:99], v[168:171], a[152:155]\n"
"v_exp_f32 v157, v157\n"
"v_mfma_f32_16x16x32_bf16 a[152:155], v[100:103], v[172:175], a[152:155]\n"
"v_exp_f32 v158, v158\n"
"v_mfma_f32_16x16x32_bf16 a[156:159], v[104:107], v[168:171], a[156:159]\n"
"s_add_u32 m0, s40, 0\n"
"ds_read_b128 v[168:171], v194 offset:43008\n"
"v_cvt_pk_bf16_f32 v154, v156, v157\n"
"v_mfma_f32_16x16x32_bf16 a[156:159], v[108:111], v[172:175], a[156:159]\n"
"s_waitcnt lgkmcnt(5)\n"
"ds_read_b128 v[172:175], v194 offset:44032\n"
"v_fma_f32 v159, v159, s43, -v227\n"
"v_mfma_f32_16x16x32_bf16 a[160:163], a[80:83], v[160:163], a[160:163]\n"
"s_waitcnt lgkmcnt(4)\n"
"v_exp_f32 v159, v159\n"
"v_mfma_f32_16x16x32_bf16 a[160:163], a[84:87], v[164:167], a[160:163]\n"
"s_waitcnt lgkmcnt(2)\n"
"s_mov_b32 s42, 0x1e000\n"
"buffer_load_dwordx4 v238, s[28:31], s42 offen nt lds\n"
"v_mfma_f32_16x16x32_bf16 a[164:167], a[88:91], v[160:163], a[164:167]\n"
"v_cvt_pk_bf16_f32 v155, v158, v159\n"
"v_mfma_f32_16x16x32_bf16 a[164:167], a[92:95], v[164:167], a[164:167]\n"
"ds_write_b128 v215, v[152:155] offset:12288\n"
"v_mfma_f32_16x16x32_bf16 a[168:171], v[96:99], v[160:163], a[168:171]\n"
"v_mfma_f32_16x16x32_bf16 a[168:171], v[100:103], v[164:167], a[168:171]\n"
"buffer_load_dwordx4 v239, s[28:31], s42 offen offset:1056 nt lds\n"
"v_mfma_f32_16x16x32_bf16 a[172:175], v[104:107], v[160:163], a[172:175]\n"
"s_waitcnt vmcnt(14)\n"
"ds_read_b128 v[160:163], v194 offset:51200\n"
"ds_read_b64_tr_b16 a[52:53], v192 offset:2112\n"
"v_mfma_f32_16x16x32_bf16 a[172:175], v[108:111], v[164:167], a[172:175]\n"
"s_waitcnt lgkmcnt(4)\n"
"ds_read_b128 v[164:167], v194 offset:52224\n"
"ds_read_b64_tr_b16 a[60:61], v192 offset:2144\n"
"v_mfma_f32_16x16x32_bf16 a[176:179], a[80:83], v[168:171], a[176:179]\n"
"s_waitcnt lgkmcnt(5)\n"
"ds_read_b64_tr_b16 a[68:69], v193 offset:2112\n"
"v_mfma_f32_16x16x32_bf16 a[176:179], a[84:87], v[172:175], a[176:179]\n"
"v_fma_f32 v176, v176, s43, -v218\n"
"v_mfma_f32_16x16x32_bf16 a[180:183], a[88:91], v[168:171], a[180:183]\n"
"ds_read_b64_tr_b16 a[76:77], v193 offset:2144\n"
"v_fma_f32 v177, v177, s43, -v218\n"
"v_mfma_f32_16x16x32_bf16 a[180:183], a[92:95], v[172:175], a[180:183]\n"
"v_fma_f32 v178, v178, s43, -v218\n"
"v_mfma_f32_16x16x32_bf16 a[184:187], v[96:99], v[168:171], a[184:187]\n"
"v_exp_f32 v176, v176\n"
"v_mfma_f32_16x16x32_bf16 a[184:187], v[100:103], v[172:175], a[184:187]\n"
"v_fma_f32 v179, v179, s43, -v218\n"
"v_mfma_f32_16x16x32_bf16 a[188:191], v[104:107], v[168:171], a[188:191]\n"
"ds_read_b128 v[168:171], v194 offset:59392\n"
"v_mfma_f32_16x16x32_bf16 a[188:191], v[108:111], v[172:175], a[188:191]\n"
"s_waitcnt lgkmcnt(6)\n"
"ds_read_b128 v[172:175], v194 offset:60416\n"
"v_mfma_f32_16x16x32_bf16 a[192:195], a[80:83], v[160:163], a[192:195]\n"
"s_waitcnt lgkmcnt(5)\n"
"v_exp_f32 v177, v177\n"
"v_mfma_f32_16x16x32_bf16 a[192:195], a[84:87], v[164:167], a[192:195]\n"
"v_exp_f32 v178, v178\n"
"v_mfma_f32_16x16x32_bf16 v[248:251], v[208:211], v[152:155], 0\n"
"v_cvt_pk_bf16_f32 v176, v176, v177\n"
"v_fma_f32 v180, v180, s43, -v218\n"
"v_mfma_f32_16x16x32_bf16 a[196:199], a[88:91], v[160:163], a[196:199]\n"
"s_nop 4\n"
"v_add_f32 v237, v237, v248\n"
"v_mfma_f32_16x16x32_bf16 a[196:199], a[92:95], v[164:167], a[196:199]\n"
"v_exp_f32 v179, v179\n"
"v_mfma_f32_16x16x32_bf16 a[200:203], v[96:99], v[160:163], a[200:203]\n"
"v_cvt_pk_bf16_f32 v177, v178, v179\n"
"v_mfma_f32_16x16x32_bf16 a[200:203], v[100:103], v[164:167], a[200:203]\n"
"v_exp_f32 v180, v180\n"
"v_mfma_f32_16x16x32_bf16 a[204:207], v[104:107], v[160:163], a[204:207]\n"
"ds_read_b128 v[160:163], v195 offset:2048\n"
"v_mfma_f32_16x16x32_bf16 a[204:207], v[108:111], v[164:167], a[204:207]\n"
"s_waitcnt lgkmcnt(2)\n"
"ds_read_b128 v[164:167], v195 offset:3072\n"
"v_fma_f32 v181, v181, s43, -v218\n"
"v_mfma_f32_16x16x32_bf16 a[208:211], a[80:83], v[168:171], a[208:211]\n"
"s_waitcnt vmcnt(12) lgkmcnt(2)\n"
"ds_read_b64_tr_b16 a[54:55], v192 offset:12672\n"
"v_mfma_f32_16x16x32_bf16 a[208:211], a[84:87], v[172:175], a[208:211]\n"
"v_exp_f32 v181, v181\n"
"v_mfma_f32_16x16x32_bf16 a[212:215], a[88:91], v[168:171], a[212:215]\n"
"ds_read_b64_tr_b16 a[62:63], v192 offset:12704\n"
"v_mfma_f32_16x16x32_bf16 a[212:215], a[92:95], v[172:175], a[212:215]\n"
"ds_read_b64_tr_b16 a[70:71], v193 offset:12672\n"
"v_fma_f32 v182, v182, s43, -v218\n"
"v_mfma_f32_16x16x32_bf16 a[216:219], v[96:99], v[168:171], a[216:219]\n"
"s_add_u32 m0, s40, 0x840\n"
"ds_read_b64_tr_b16 a[78:79], v193 offset:12704\n"
"v_mfma_f32_16x16x32_bf16 a[216:219], v[100:103], v[172:175], a[216:219]\n"
"s_mov_b32 s42, 0x20000\n"
"buffer_load_dwordx4 v238, s[28:31], s42 offen nt lds\n"
"v_mfma_f32_16x16x32_bf16 a[220:223], v[104:107], v[168:171], a[220:223]\n"
"ds_read_b128 v[168:171], v195 offset:10240\n"
"v_mfma_f32_16x16x32_bf16 a[220:223], v[108:111], v[172:175], a[220:223]\n"
"s_waitcnt lgkmcnt(6)\n"
"ds_read_b128 v[172:175], v195 offset:11264\n"
"v_cvt_pk_bf16_f32 v178, v180, v181\n"
"v_mfma_f32_16x16x32_bf16 a[224:227], a[80:83], v[160:163], a[224:227]\n"
"s_waitcnt lgkmcnt(6)\n"
"v_fma_f32 v183, v183, s43, -v218\n"
"v_mfma_f32_16x16x32_bf16 a[224:227], a[84:87], v[164:167], a[224:227]\n"
"buffer_load_dwordx4 v239, s[28:31], s42 offen offset:1056 nt lds\n"
"v_mfma_f32_16x16x32_bf16 a[228:231], a[88:91], v[160:163], a[228:231]\n"
"v_mfma_f32_16x16x32_bf16 a[228:231], a[92:95], v[164:167], a[228:231]\n"
"v_exp_f32 v182, v182\n"
"v_mfma_f32_16x16x32_bf16 a[232:235], v[96:99], v[160:163], a[232:235]\n"
"s_add_u32 m0, s40, 0x3180\n"
"v_exp_f32 v183, v183\n"
"v_mfma_f32_16x16x32_bf16 a[232:235], v[100:103], v[164:167], a[232:235]\n"
"s_waitcnt lgkmcnt(2)\n"
"s_mov_b32 s42, 0x7000\n"
"buffer_load_dwordx4 v204, s[32:35], s42 offen lds\n"
"v_cvt_pk_bf16_f32 v179, v182, v183\n"
"v_mfma_f32_16x16x32_bf16 a[236:239], v[104:107], v[160:163], a[236:239]\n"
"s_waitcnt lgkmcnt(0)\n"
"s_barrier\n"
"ds_read_b128 v[160:163], v194 offset:4096\n"
"v_mfma_f32_16x16x32_bf16 a[236:239], v[108:111], v[164:167], a[236:239]\n"
"ds_read_b128 v[164:167], v194 offset:5120\n"
"v_accvgpr_read_b32 v240, a0\n"
"v_fma_f32 v240, v240, s43, -v219\n"
"v_mfma_f32_16x16x32_bf16 v[248:251], v[208:211], v[176:179], 0\n"
"v_exp_f32 v240, v240\n"
"v_mfma_f32_16x16x32_bf16 a[240:243], a[80:83], v[168:171], a[240:243]\n"
"v_accvgpr_read_b32 v241, a1\n"
"v_fma_f32 v241, v241, s43, -v219\n"
"v_mfma_f32_16x16x32_bf16 a[240:243], a[84:87], v[172:175], a[240:243]\n"
"buffer_load_dwordx4 v205, s[32:35], s42 offen offset:1056 lds\n"
"v_accvgpr_read_b32 v242, a2\n"
"v_mfma_f32_16x16x32_bf16 a[244:247], a[88:91], v[168:171], a[244:247]\n"
"v_exp_f32 v241, v241\n"
"v_mfma_f32_16x16x32_bf16 a[244:247], a[92:95], v[172:175], a[244:247]\n"
"ds_write_b128 v214, v[176:179] offset:0\n"
"v_mfma_f32_16x16x32_bf16 a[248:251], v[96:99], v[168:171], a[248:251]\n"
"s_add_u32 m0, s40, 0x2100\n"
"v_fma_f32 v242, v242, s43, -v219\n"
"v_cvt_pk_bf16_f32 v244, v240, v241\n"
"v_mfma_f32_16x16x32_bf16 a[248:251], v[100:103], v[172:175], a[248:251]\n"
"s_mov_b32 s42, 0x22000\n"
"buffer_load_dwordx4 v238, s[28:31], s42 offen nt lds\n"
"v_mfma_f32_16x16x32_bf16 a[252:255], v[104:107], v[168:171], a[252:255]\n"
"ds_read_b128 v[168:171], v194 offset:12288\n"
"v_accvgpr_read_b32 v243, a3\n"
"v_mfma_f32_16x16x32_bf16 a[252:255], v[108:111], v[172:175], a[252:255]\n"
"s_waitcnt lgkmcnt(3)\n"
"ds_read_b128 v[172:175], v194 offset:13312\n"
"v_exp_f32 v242, v242\n"
"v_fma_f32 v243, v243, s43, -v219\n"
"v_mfma_f32_16x16x32_bf16 a[96:99], a[48:51], v[160:163], a[96:99]\n"
"s_waitcnt lgkmcnt(3)\n"
"v_add_f32 v228, v228, v248\n"
"v_accvgpr_read_b32 v240, a4\n"
"v_mfma_f32_16x16x32_bf16 a[96:99], a[52:55], v[164:167], a[96:99]\n"
"buffer_load_dwordx4 v239, s[28:31], s42 offen offset:1056 nt lds\n"
"v_fma_f32 v240, v240, s43, -v219\n"
"v_mfma_f32_16x16x32_bf16 a[100:103], a[56:59], v[160:163], a[100:103]\n"
"s_waitcnt vmcnt(16)\n"
"ds_read_b64_tr_b16 a[80:81], v192 offset:4224\n"
"v_accvgpr_read_b32 v241, a5\n"
"v_mfma_f32_16x16x32_bf16 a[100:103], a[60:63], v[164:167], a[100:103]\n"
"ds_read_b64_tr_b16 a[88:89], v192 offset:4256\n"
"v_exp_f32 v243, v243\n"
"s_nop 0\n"
"v_cvt_pk_bf16_f32 v245, v242, v243\n"
"v_mfma_f32_16x16x32_bf16 a[104:107], a[64:67], v[160:163], a[104:107]\n"
"ds_read_b64_tr_b16 v[96:97], v193 offset:4224\n"
"v_fma_f32 v241, v241, s43, -v219\n"
"v_mfma_f32_16x16x32_bf16 a[104:107], a[68:71], v[164:167], a[104:107]\n"
"v_exp_f32 v240, v240\n"
"v_accvgpr_read_b32 v242, a6\n"
"v_mfma_f32_16x16x32_bf16 a[108:111], a[72:75], v[160:163], a[108:111]\n"
"ds_read_b128 v[160:163], v194 offset:20480\n"
"ds_read_b64_tr_b16 v[104:105], v193 offset:4256\n"
"v_mfma_f32_16x16x32_bf16 a[108:111], a[76:79], v[164:167], a[108:111]\n"
"s_waitcnt lgkmcnt(6)\n"
"ds_read_b128 v[164:167], v194 offset:21504\n"
"v_exp_f32 v241, v241\n"
"v_fma_f32 v242, v242, s43, -v219\n"
"v_mfma_f32_16x16x32_bf16 a[112:115], a[48:51], v[168:171], a[112:115]\n"
"s_waitcnt lgkmcnt(6)\n"
"v_exp_f32 v242, v242\n"
"v_mfma_f32_16x16x32_bf16 a[112:115], a[52:55], v[172:175], a[112:115]\n"
"v_cvt_pk_bf16_f32 v246, v240, v241\n"
"v_accvgpr_read_b32 v243, a7\n"
"v_fma_f32 v243, v243, s43, -v219\n"
"v_accvgpr_read_b32 v240, a16\n"
"v_mfma_f32_16x16x32_bf16 a[116:119], a[56:59], v[168:171], a[116:119]\n"
"v_exp_f32 v243, v243\n"
"v_mfma_f32_16x16x32_bf16 a[116:119], a[60:63], v[172:175], a[116:119]\n"
"v_cvt_pk_bf16_f32 v247, v242, v243\n"
"v_fma_f32 v240, v240, s43, -v220\n"
"v_accvgpr_read_b32 v241, a17\n"
"v_mfma_f32_16x16x32_bf16 v[248:251], v[208:211], v[244:247], 0\n"
"v_exp_f32 v240, v240\n"
"v_mfma_f32_16x16x32_bf16 a[120:123], a[64:67], v[168:171], a[120:123]\n"
"v_fma_f32 v241, v241, s43, -v220\n"
"v_accvgpr_read_b32 v242, a18\n"
"v_mfma_f32_16x16x32_bf16 a[120:123], a[68:71], v[172:175], a[120:123]\n"
"ds_write_b128 v214, v[244:247] offset:8192\n"
"v_mfma_f32_16x16x32_bf16 a[124:127], a[72:75], v[168:171], a[124:127]\n"
"s_waitcnt vmcnt(10)\n"
"s_add_u32 m0, s40, 0x1080\n"
"ds_read_b128 v[168:171], v194 offset:28672\n"
"ds_read_b64_tr_b16 v[112:113], v196 offset:10560\n"
"v_mfma_f32_16x16x32_bf16 a[124:127], a[76:79], v[172:175], a[124:127]\n"
"s_waitcnt lgkmcnt(5)\n"
"ds_read_b128 v[172:175], v194 offset:29696\n"
"ds_read_b64_tr_b16 v[116:117], v196 offset:10568\n"
"v_mfma_f32_16x16x32_bf16 a[128:131], a[48:51], v[160:163], a[128:131]\n"
"s_waitcnt lgkmcnt(5)\n"
"ds_read_b64_tr_b16 v[120:121], v197 offset:10560\n"
"v_fma_f32 v242, v242, s43, -v220\n"
"v_mfma_f32_16x16x32_bf16 a[128:131], a[52:55], v[164:167], a[128:131]\n"
"s_mov_b32 s42, 0x24000\n"
"buffer_load_dwordx4 v238, s[28:31], s42 offen nt lds\n"
"v_accvgpr_read_b32 v243, a19\n"
"v_mfma_f32_16x16x32_bf16 a[132:135], a[56:59], v[160:163], a[132:135]\n"
"ds_read_b64_tr_b16 v[124:125], v197 offset:10568\n"
"v_fma_f32 v243, v243, s43, -v220\n"
"v_mfma_f32_16x16x32_bf16 a[132:135], a[60:63], v[164:167], a[132:135]\n"
"v_exp_f32 v241, v241\n"
"v_exp_f32 v242, v242\n"
"v_mfma_f32_16x16x32_bf16 a[136:139], a[64:67], v[160:163], a[136:139]\n"
"ds_read_b64_tr_b16 a[82:83], v192 offset:6336\n"
"v_cvt_pk_bf16_f32 v244, v240, v241\n"
"v_mfma_f32_16x16x32_bf16 a[136:139], a[68:71], v[164:167], a[136:139]\n"
"buffer_load_dwordx4 v239, s[28:31], s42 offen offset:1056 nt lds\n"
"v_add_f32 v229, v229, v248\n"
"v_mfma_f32_16x16x32_bf16 a[140:143], a[72:75], v[160:163], a[140:143]\n"
"ds_read_b128 v[160:163], v194 offset:36864\n"
"ds_read_b64_tr_b16 a[90:91], v192 offset:6368\n"
"v_mfma_f32_16x16x32_bf16 a[140:143], a[76:79], v[164:167], a[140:143]\n"
"s_waitcnt lgkmcnt(8)\n"
"ds_read_b128 v[164:167], v194 offset:37888\n"
"ds_read_b64_tr_b16 v[98:99], v193 offset:6336\n"
"v_exp_f32 v243, v243\n"
"v_mfma_f32_16x16x32_bf16 a[144:147], a[48:51], v[168:171], a[144:147]\n"
"s_waitcnt lgkmcnt(8)\n"
"ds_read_b64_tr_b16 v[106:107], v193 offset:6368\n"
"v_accvgpr_read_b32 v240, a20\n"
"v_mfma_f32_16x16x32_bf16 a[144:147], a[52:55], v[172:175], a[144:147]\n"
"v_fma_f32 v240, v240, s43, -v220\n"
"v_exp_f32 v240, v240\n"
"v_mfma_f32_16x16x32_bf16 a[148:151], a[56:59], v[168:171], a[148:151]\n"
"v_cvt_pk_bf16_f32 v245, v242, v243\n"
"v_accvgpr_read_b32 v241, a21\n"
"v_mfma_f32_16x16x32_bf16 a[148:151], a[60:63], v[172:175], a[148:151]\n"
"v_fma_f32 v241, v241, s43, -v220\n"
"v_exp_f32 v241, v241\n"
"v_accvgpr_read_b32 v242, a22\n"
"v_mfma_f32_16x16x32_bf16 a[152:155], a[64:67], v[168:171], a[152:155]\n"
"s_add_u32 m0, s40, 0x2940\n"
"v_fma_f32 v242, v242, s43, -v220\n"
"v_cvt_pk_bf16_f32 v246, v240, v241\n"
"v_mfma_f32_16x16x32_bf16 a[152:155], a[68:71], v[172:175], a[152:155]\n"
"s_waitcnt lgkmcnt(6)\n"
"s_mov_b32 s42, 0xa800\n"
"buffer_load_dwordx4 v204, s[32:35], s42 offen lds\n"
"v_exp_f32 v242, v242\n"
"v_mfma_f32_16x16x32_bf16 a[156:159], a[72:75], v[168:171], a[156:159]\n"
"ds_read_b128 v[168:171], v194 offset:45056\n"
"v_accvgpr_read_b32 v243, a23\n"
"v_mfma_f32_16x16x32_bf16 a[156:159], a[76:79], v[172:175], a[156:159]\n"
"s_waitcnt lgkmcnt(5)\n"
"ds_read_b128 v[172:175], v194 offset:46080\n"
"v_fma_f32 v243, v243, s43, -v220\n"
"v_accvgpr_read_b32 v240, a32\n"
"v_mfma_f32_16x16x32_bf16 a[160:163], a[48:51], v[160:163], a[160:163]\n"
"s_waitcnt lgkmcnt(4)\n"
"v_exp_f32 v243, v243\n"
"v_mfma_f32_16x16x32_bf16 a[160:163], a[52:55], v[164:167], a[160:163]\n"
"buffer_load_dwordx4 v205, s[32:35], s42 offen offset:1056 lds\n"
"v_cvt_pk_bf16_f32 v247, v242, v243\n"
"v_fma_f32 v240, v240, s43, -v221\n"
"v_mfma_f32_16x16x32_bf16 a[164:167], a[56:59], v[160:163], a[164:167]\n"
"v_exp_f32 v240, v240\n"
"v_mfma_f32_16x16x32_bf16 a[164:167], a[60:63], v[164:167], a[164:167]\n"
"ds_write_b128 v214, v[244:247] offset:16384\n"
"v_mfma_f32_16x16x32_bf16 v[248:251], v[208:211], v[244:247], 0\n"
"v_accvgpr_read_b32 v241, a33\n"
"v_fma_f32 v241, v241, s43, -v221\n"
"v_mfma_f32_16x16x32_bf16 a[168:171], a[64:67], v[160:163], a[168:171]\n"
"s_add_u32 m0, s40, 0x18c0\n"
"v_exp_f32 v241, v241\n"
"v_mfma_f32_16x16x32_bf16 a[168:171], a[68:71], v[164:167], a[168:171]\n"
"s_waitcnt lgkmcnt(3)\n"
"s_mov_b32 s42, 0x26000\n"
"buffer_load_dwordx4 v238, s[28:31], s42 offen nt lds\n"
"v_accvgpr_read_b32 v242, a34\n"
".Lpad6_%=:\n"
"s_cbranch_vccnz .Lpad10_%=\n"
"v_mfma_f32_16x16x32_bf16 a[172:175], a[72:75], v[160:163], a[172:175]\n"
"ds_read_b128 v[160:163], v194 offset:53248\n"
"ds_read_b64_tr_b16 a[84:85], v192 offset:14784\n"
"v_mfma_f32_16x16x32_bf16 a[172:175], a[76:79], v[164:167], a[172:175]\n"
"s_waitcnt lgkmcnt(4)\n"
"ds_read_b128 v[164:167], v194 offset:54272\n"
"ds_read_b64_tr_b16 a[92:93], v192 offset:14816\n"
"v_fma_f32 v242, v242, s43, -v221\n"
"v_cvt_pk_bf16_f32 v244, v240, v241\n"
"v_mfma_f32_16x16x32_bf16 a[176:179], a[48:51], v[168:171], a[176:179]\n"
"s_waitcnt lgkmcnt(5)\n"
"ds_read_b64_tr_b16 v[100:101], v193 offset:14784\n"
"v_accvgpr_read_b32 v243, a35\n"
"v_mfma_f32_16x16x32_bf16 a[176:179], a[52:55], v[172:175], a[176:179]\n"
"buffer_load_dwordx4 v239, s[28:31], s42 offen offset:1056 nt lds\n"
"v_fma_f32 v243, v243, s43, -v221\n"
"v_mfma_f32_16x16x32_bf16 a[180:183], a[56:59], v[168:171], a[180:183]\n"
"ds_read_b64_tr_b16 v[108:109], v193 offset:14816\n"
"v_add_f32 v230, v230, v248\n"
"v_mfma_f32_16x16x32_bf16 a[180:183], a[60:63], v[172:175], a[180:183]\n"
"v_exp_f32 v242, v242\n"
"v_exp_f32 v243, v243\n"
"v_mfma_f32_16x16x32_bf16 a[184:187], a[64:67], v[168:171], a[184:187]\n"
"v_accvgpr_read_b32 v240, a36\n"
"v_fma_f32 v240, v240, s43, -v221\n"
"v_mfma_f32_16x16x32_bf16 a[184:187], a[68:71], v[172:175], a[184:187]\n"
"v_exp_f32 v240, v240\n"
"v_cvt_pk_bf16_f32 v245, v242, v243\n"
"v_mfma_f32_16x16x32_bf16 a[188:191], a[72:75], v[168:171], a[188:191]\n"
"s_waitcnt vmcnt(14)\n"
"ds_read_b128 v[168:171], v194 offset:61440\n"
"ds_read_b64_tr_b16 v[114:115], v196 offset:16896\n"
"v_mfma_f32_16x16x32_bf16 a[188:191], a[76:79], v[172:175], a[188:191]\n"
"s_waitcnt lgkmcnt(7)\n"
"ds_read_b128 v[172:175], v194 offset:62464\n"
"ds_read_b64_tr_b16 v[118:119], v196 offset:16904\n"
"v_accvgpr_read_b32 v241, a37\n"
"v_fma_f32 v241, v241, s43, -v221\n"
"v_mfma_f32_16x16x32_bf16 a[192:195], a[48:51], v[160:163], a[192:195]\n"
"s_waitcnt lgkmcnt(7)\n"
"ds_read_b64_tr_b16 v[122:123], v197 offset:16896\n"
"v_accvgpr_read_b32 v242, a38\n"
"v_mfma_f32_16x16x32_bf16 a[192:195], a[52:55], v[164:167], a[192:195]\n"
"v_exp_f32 v241, v241\n"
"v_fma_f32 v242, v242, s43, -v221\n"
"v_mfma_f32_16x16x32_bf16 a[196:199], a[56:59], v[160:163], a[196:199]\n"
"ds_read_b64_tr_b16 v[126:127], v197 offset:16904\n"
"v_cvt_pk_bf16_f32 v246, v240, v241\n"
"v_mfma_f32_16x16x32_bf16 a[196:199], a[60:63], v[164:167], a[196:199]\n"
"v_exp_f32 v242, v242\n"
"v_accvgpr_read_b32 v243, a39\n"
"v_fma_f32 v243, v243, s43, -v221\n"
"v_mfma_f32_16x16x32_bf16 a[200:203], a[64:67], v[160:163], a[200:203]\n"
"v_exp_f32 v243, v243\n"
"v_mfma_f32_16x16x32_bf16 a[200:203], a[68:71], v[164:167], a[200:203]\n"
"v_cvt_pk_bf16_f32 v247, v242, v243\n"
"v_fma_f32 v0, v0, s43, -v222\n"
"v_exp_f32 v0, v0\n"
"v_mfma_f32_16x16x32_bf16 v[248:251], v[208:211], v[244:247], 0\n"
"v_fma_f32 v1, v1, s43, -v222\n"
"v_fma_f32 v2, v2, s43, -v222\n"
"v_mfma_f32_16x16x32_bf16 a[204:207], a[72:75], v[160:163], a[204:207]\n"
"ds_read_b128 v[160:163], v195 offset:4096\n"
"v_fma_f32 v3, v3, s43, -v222\n"
"v_mfma_f32_16x16x32_bf16 a[204:207], a[76:79], v[164:167], a[204:207]\n"
"s_waitcnt lgkmcnt(6)\n"
"ds_read_b128 v[164:167], v195 offset:5120\n"
"v_exp_f32 v1, v1\n"
"v_mfma_f32_16x16x32_bf16 a[208:211], a[48:51], v[168:171], a[208:211]\n"
"s_waitcnt vmcnt(12) lgkmcnt(5)\n"
"ds_read_b64_tr_b16 a[86:87], v192 offset:0\n"
"v_cvt_pk_bf16_f32 v0, v0, v1\n"
"v_mfma_f32_16x16x32_bf16 a[208:211], a[52:55], v[172:175], a[208:211]\n"
"ds_write_b128 v214, v[244:247] offset:24576\n"
"v_mfma_f32_16x16x32_bf16 a[212:215], a[56:59], v[168:171], a[212:215]\n"
"ds_read_b64_tr_b16 a[94:95], v192 offset:32\n"
"v_fma_f32 v4, v4, s43, -v222\n"
"v_mfma_f32_16x16x32_bf16 a[212:215], a[60:63], v[172:175], a[212:215]\n"
"ds_read_b64_tr_b16 v[102:103], v193 offset:0\n"
"v_add_f32 v231, v231, v248\n"
"v_mfma_f32_16x16x32_bf16 a[216:219], a[64:67], v[168:171], a[216:219]\n"
"s_add_u32 m0, s40, 0x4200\n"
"ds_read_b64_tr_b16 v[110:111], v193 offset:32\n"
"v_fma_f32 v5, v5, s43, -v222\n"
"v_mfma_f32_16x16x32_bf16 a[216:219], a[68:71], v[172:175], a[216:219]\n"
"s_waitcnt lgkmcnt(7)\n"
"s_mov_b32 s42, 0x28000\n"
"buffer_load_dwordx4 v238, s[28:31], s42 offen nt lds\n"
"v_exp_f32 v2, v2\n"
"v_mfma_f32_16x16x32_bf16 a[220:223], a[72:75], v[168:171], a[220:223]\n"
"ds_read_b128 v[168:171], v195 offset:12288\n"
"v_fma_f32 v6, v6, s43, -v222\n"
"v_mfma_f32_16x16x32_bf16 a[220:223], a[76:79], v[172:175], a[220:223]\n"
"s_waitcnt lgkmcnt(7)\n"
"ds_read_b128 v[172:175], v195 offset:13312\n"
"v_exp_f32 v3, v3\n"
"v_mfma_f32_16x16x32_bf16 a[224:227], a[48:51], v[160:163], a[224:227]\n"
"s_waitcnt lgkmcnt(7)\n"
"v_exp_f32 v4, v4\n"
"v_mfma_f32_16x16x32_bf16 a[224:227], a[52:55], v[164:167], a[224:227]\n"
"buffer_load_dwordx4 v239, s[28:31], s42 offen offset:1056 nt lds\n"
"v_cvt_pk_bf16_f32 v1, v2, v3\n"
"v_fma_f32 v7, v7, s43, -v222\n"
"v_mfma_f32_16x16x32_bf16 a[228:231], a[56:59], v[160:163], a[228:231]\n"
"v_exp_f32 v5, v5\n"
"v_mfma_f32_16x16x32_bf16 a[228:231], a[60:63], v[164:167], a[228:231]\n"
"v_exp_f32 v6, v6\n"
"v_cvt_pk_bf16_f32 v2, v4, v5\n"
"v_mfma_f32_16x16x32_bf16 a[232:235], a[64:67], v[160:163], a[232:235]\n"
"s_add_u32 m0, s40, 0\n"
"v_exp_f32 v7, v7\n"
"v_mfma_f32_16x16x32_bf16 a[232:235], a[68:71], v[164:167], a[232:235]\n"
"s_waitcnt lgkmcnt(2)\n"
"s_mov_b32 s42, 0xe000\n"
"buffer_load_dwordx4 v204, s[32:35], s42 offen lds\n"
"v_cvt_pk_bf16_f32 v3, v6, v7\n"
"v_fma_f32 v16, v16, s43, -v223\n"
"v_mfma_f32_16x16x32_bf16 a[236:239], a[72:75], v[160:163], a[236:239]\n"
"ds_read_b128 v[160:163], v194 offset:6144\n"
"v_fma_f32 v17, v17, s43, -v223\n"
"v_mfma_f32_16x16x32_bf16 a[236:239], a[76:79], v[164:167], a[236:239]\n"
"ds_read_b128 v[164:167], v194 offset:7168\n"
"v_exp_f32 v16, v16\n"
"v_mfma_f32_16x16x32_bf16 v[248:251], v[208:211], v[0:3], 0\n"
"s_waitcnt lgkmcnt(3)\n"
"v_exp_f32 v17, v17\n"
"v_mfma_f32_16x16x32_bf16 a[240:243], a[48:51], v[168:171], a[240:243]\n"
"s_waitcnt lgkmcnt(2)\n"
"v_fma_f32 v18, v18, s43, -v223\n"
"v_cvt_pk_bf16_f32 v16, v16, v17\n"
"v_mfma_f32_16x16x32_bf16 a[240:243], a[52:55], v[172:175], a[240:243]\n"
"buffer_load_dwordx4 v205, s[32:35], s42 offen offset:1056 lds\n"
"v_fma_f32 v19, v19, s43, -v223\n"
"v_mfma_f32_16x16x32_bf16 a[244:247], a[56:59], v[168:171], a[244:247]\n"
"v_exp_f32 v18, v18\n"
"v_mfma_f32_16x16x32_bf16 a[244:247], a[60:63], v[172:175], a[244:247]\n"
"ds_write_b128 v214, v[0:3] offset:32768\n"
"v_mfma_f32_16x16x32_bf16 a[248:251], a[64:67], v[168:171], a[248:251]\n"
"s_add_u32 m0, s40, 0x39c0\n"
"v_exp_f32 v19, v19\n"
"v_mfma_f32_16x16x32_bf16 a[248:251], a[68:71], v[172:175], a[248:251]\n"
"s_mov_b32 s42, 0x2a000\n"
"buffer_load_dwordx4 v238, s[28:31], s42 offen nt lds\n"
"v_mfma_f32_16x16x32_bf16 a[252:255], a[72:75], v[168:171], a[252:255]\n"
"ds_read_b128 v[168:171], v194 offset:14336\n"
"v_fma_f32 v20, v20, s43, -v223\n"
"v_mfma_f32_16x16x32_bf16 a[252:255], a[76:79], v[172:175], a[252:255]\n"
"s_waitcnt lgkmcnt(3)\n"
"ds_read_b128 v[172:175], v194 offset:15360\n"
"v_exp_f32 v20, v20\n"
"v_cvt_pk_bf16_f32 v17, v18, v19\n"
"v_mfma_f32_16x16x32_bf16 a[96:99], a[80:83], v[160:163], a[96:99]\n"
"s_waitcnt lgkmcnt(3)\n"
"v_add_f32 v232, v232, v248\n"
"v_fma_f32 v21, v21, s43, -v223\n"
"v_mfma_f32_16x16x32_bf16 a[96:99], a[84:87], v[164:167], a[96:99]\n"
"buffer_load_dwordx4 v239, s[28:31], s42 offen offset:1056 nt lds\n"
"v_fma_f32 v22, v22, s43, -v223\n"
"v_mfma_f32_16x16x32_bf16 a[100:103], a[88:91], v[160:163], a[100:103]\n"
"s_waitcnt vmcnt(16)\n"
"ds_read_b64_tr_b16 a[48:49], v192 offset:2112\n"
"v_fma_f32 v23, v23, s43, -v223\n"
"v_mfma_f32_16x16x32_bf16 a[100:103], a[92:95], v[164:167], a[100:103]\n"
"ds_read_b64_tr_b16 a[56:57], v192 offset:2144\n"
"v_exp_f32 v21, v21\n"
"s_nop 0\n"
"v_cvt_pk_bf16_f32 v18, v20, v21\n"
"v_mfma_f32_16x16x32_bf16 a[104:107], v[96:99], v[160:163], a[104:107]\n"
"ds_read_b64_tr_b16 a[64:65], v193 offset:2112\n"
"v_fma_f32 v32, v32, s43, -v224\n"
"v_mfma_f32_16x16x32_bf16 a[104:107], v[100:103], v[164:167], a[104:107]\n"
"v_exp_f32 v22, v22\n"
"v_mfma_f32_16x16x32_bf16 a[108:111], v[104:107], v[160:163], a[108:111]\n"
"ds_read_b128 v[160:163], v194 offset:22528\n"
"ds_read_b64_tr_b16 a[72:73], v193 offset:2144\n"
"v_mfma_f32_16x16x32_bf16 a[108:111], v[108:111], v[164:167], a[108:111]\n"
"s_waitcnt lgkmcnt(6)\n"
"ds_read_b128 v[164:167], v194 offset:23552\n"
"v_exp_f32 v23, v23\n"
"s_nop 0\n"
"v_cvt_pk_bf16_f32 v19, v22, v23\n"
"v_fma_f32 v33, v33, s43, -v224\n"
"v_mfma_f32_16x16x32_bf16 a[112:115], a[80:83], v[168:171], a[112:115]\n"
"s_waitcnt lgkmcnt(6)\n"
"v_exp_f32 v32, v32\n"
"v_mfma_f32_16x16x32_bf16 a[112:115], a[84:87], v[172:175], a[112:115]\n"
"ds_write_b128 v214, v[16:19] offset:40960\n"
"v_mfma_f32_16x16x32_bf16 v[248:251], v[208:211], v[16:19], 0\n"
"v_exp_f32 v33, v33\n"
"v_mfma_f32_16x16x32_bf16 a[116:119], a[88:91], v[168:171], a[116:119]\n"
"v_fma_f32 v34, v34, s43, -v224\n"
"v_cvt_pk_bf16_f32 v32, v32, v33\n"
"v_mfma_f32_16x16x32_bf16 a[116:119], a[92:95], v[172:175], a[116:119]\n"
"v_exp_f32 v34, v34\n"
"v_fma_f32 v35, v35, s43, -v224\n"
"v_mfma_f32_16x16x32_bf16 a[120:123], v[96:99], v[168:171], a[120:123]\n"
"v_exp_f32 v35, v35\n"
"v_mfma_f32_16x16x32_bf16 a[120:123], v[100:103], v[172:175], a[120:123]\n"
"v_fma_f32 v36, v36, s43, -v224\n"
"v_exp_f32 v36, v36\n"
"v_cvt_pk_bf16_f32 v33, v34, v35\n"
"v_mfma_f32_16x16x32_bf16 a[124:127], v[104:107], v[168:171], a[124:127]\n"
"s_waitcnt vmcnt(14)\n"
"s_add_u32 m0, s40, 0x840\n"
"ds_read_b128 v[168:171], v194 offset:30720\n"
"ds_read_b64_tr_b16 v[128:129], v196 offset:12672\n"
"v_mfma_f32_16x16x32_bf16 a[124:127], v[108:111], v[172:175], a[124:127]\n"
"s_waitcnt lgkmcnt(5)\n"
"ds_read_b128 v[172:175], v194 offset:31744\n"
"ds_read_b64_tr_b16 v[132:133], v196 offset:12680\n"
"v_add_f32 v233, v233, v248\n"
"v_mfma_f32_16x16x32_bf16 a[128:131], a[80:83], v[160:163], a[128:131]\n"
"s_waitcnt lgkmcnt(5)\n"
"ds_read_b64_tr_b16 v[136:137], v197 offset:12672\n"
"v_fma_f32 v37, v37, s43, -v224\n"
"v_mfma_f32_16x16x32_bf16 a[128:131], a[84:87], v[164:167], a[128:131]\n"
"s_mov_b32 s42, 0x2c000\n"
"buffer_load_dwordx4 v238, s[28:31], s42 offen nt lds\n"
"v_exp_f32 v37, v37\n"
"v_mfma_f32_16x16x32_bf16 a[132:135], a[88:91], v[160:163], a[132:135]\n"
"ds_read_b64_tr_b16 v[140:141], v197 offset:12680\n"
"v_fma_f32 v38, v38, s43, -v224\n"
"v_mfma_f32_16x16x32_bf16 a[132:135], a[92:95], v[164:167], a[132:135]\n"
"v_exp_f32 v38, v38\n"
"v_cvt_pk_bf16_f32 v34, v36, v37\n"
"v_mfma_f32_16x16x32_bf16 a[136:139], v[96:99], v[160:163], a[136:139]\n"
"s_waitcnt vmcnt(13)\n"
"ds_read_b64_tr_b16 a[50:51], v192 offset:8448\n"
"v_fma_f32 v39, v39, s43, -v224\n"
"v_mfma_f32_16x16x32_bf16 a[136:139], v[100:103], v[164:167], a[136:139]\n"
"buffer_load_dwordx4 v239, s[28:31], s42 offen offset:1056 nt lds\n"
"v_exp_f32 v39, v39\n"
"v_mfma_f32_16x16x32_bf16 a[140:143], v[104:107], v[160:163], a[140:143]\n"
"ds_read_b128 v[160:163], v194 offset:38912\n"
"ds_read_b64_tr_b16 a[58:59], v192 offset:8480\n"
"v_mfma_f32_16x16x32_bf16 a[140:143], v[108:111], v[164:167], a[140:143]\n"
"s_waitcnt lgkmcnt(8)\n"
"ds_read_b128 v[164:167], v194 offset:39936\n"
"ds_read_b64_tr_b16 a[66:67], v193 offset:8448\n"
"v_cvt_pk_bf16_f32 v35, v38, v39\n"
"v_mfma_f32_16x16x32_bf16 a[144:147], a[80:83], v[168:171], a[144:147]\n"
"s_waitcnt lgkmcnt(8)\n"
"ds_read_b64_tr_b16 a[74:75], v193 offset:8480\n"
"v_fma_f32 v48, v48, s43, -v225\n"
"v_mfma_f32_16x16x32_bf16 a[144:147], a[84:87], v[172:175], a[144:147]\n"
"ds_write_b128 v214, v[32:35] offset:49152\n"
"v_mfma_f32_16x16x32_bf16 v[248:251], v[208:211], v[32:35], 0\n"
"v_exp_f32 v48, v48\n"
"v_mfma_f32_16x16x32_bf16 a[148:151], a[88:91], v[168:171], a[148:151]\n"
"v_fma_f32 v49, v49, s43, -v225\n"
"v_fma_f32 v50, v50, s43, -v225\n"
"v_mfma_f32_16x16x32_bf16 a[148:151], a[92:95], v[172:175], a[148:151]\n"
"v_exp_f32 v49, v49\n"
"s_nop 0\n"
"v_cvt_pk_bf16_f32 v48, v48, v49\n"
"v_mfma_f32_16x16x32_bf16 a[152:155], v[96:99], v[168:171], a[152:155]\n"
"s_add_u32 m0, s40, 0x3180\n"
"v_exp_f32 v50, v50\n"
"v_mfma_f32_16x16x32_bf16 a[152:155], v[100:103], v[172:175], a[152:155]\n"
"s_waitcnt lgkmcnt(7)\n"
"s_mov_b32 s42, 0x11800\n"
"buffer_load_dwordx4 v204, s[32:35], s42 offen lds\n"
"v_fma_f32 v51, v51, s43, -v225\n"
"v_fma_f32 v52, v52, s43, -v225\n"
"v_mfma_f32_16x16x32_bf16 a[156:159], v[104:107], v[168:171], a[156:159]\n"
"ds_read_b128 v[168:171], v194 offset:47104\n"
"v_add_f32 v234, v234, v248\n"
"v_mfma_f32_16x16x32_bf16 a[156:159], v[108:111], v[172:175], a[156:159]\n"
"s_waitcnt lgkmcnt(6)\n"
"ds_read_b128 v[172:175], v194 offset:48128\n"
"v_exp_f32 v51, v51\n"
"v_mfma_f32_16x16x32_bf16 a[160:163], a[80:83], v[160:163], a[160:163]\n"
"s_waitcnt lgkmcnt(5)\n"
"v_exp_f32 v52, v52\n"
"v_mfma_f32_16x16x32_bf16 a[160:163], a[84:87], v[164:167], a[160:163]\n"
"buffer_load_dwordx4 v205, s[32:35], s42 offen offset:1056 lds\n"
"v_cvt_pk_bf16_f32 v49, v50, v51\n"
"v_fma_f32 v53, v53, s43, -v225\n"
"v_mfma_f32_16x16x32_bf16 a[164:167], a[88:91], v[160:163], a[164:167]\n"
"v_exp_f32 v53, v53\n"
"v_mfma_f32_16x16x32_bf16 a[164:167], a[92:95], v[164:167], a[164:167]\n"
"v_fma_f32 v54, v54, s43, -v225\n"
"v_exp_f32 v54, v54\n"
"v_mfma_f32_16x16x32_bf16 a[168:171], v[96:99], v[160:163], a[168:171]\n"
"s_add_u32 m0, s40, 0x2100\n"
"v_cvt_pk_bf16_f32 v50, v52, v53\n"
"v_fma_f32 v55, v55, s43, -v225\n"
"v_mfma_f32_16x16x32_bf16 a[168:171], v[100:103], v[164:167], a[168:171]\n"
"s_waitcnt lgkmcnt(3)\n"
"s_mov_b32 s42, 0x2e000\n"
"buffer_load_dwordx4 v238, s[28:31], s42 offen nt lds\n"
"v_exp_f32 v55, v55\n"
"v_mfma_f32_16x16x32_bf16 a[172:175], v[104:107], v[160:163], a[172:175]\n"
"s_waitcnt vmcnt(15)\n"
"ds_read_b128 v[160:163], v194 offset:55296\n"
"ds_read_b64_tr_b16 a[52:53], v192 offset:4224\n"
"v_mfma_f32_16x16x32_bf16 a[172:175], v[108:111], v[164:167], a[172:175]\n"
"s_waitcnt lgkmcnt(3)\n"
"ds_read_b128 v[164:167], v194 offset:56320\n"
"ds_read_b64_tr_b16 a[60:61], v192 offset:4256\n"
"v_cvt_pk_bf16_f32 v51, v54, v55\n"
"v_mfma_f32_16x16x32_bf16 a[176:179], a[80:83], v[168:171], a[176:179]\n"
"s_waitcnt lgkmcnt(4)\n"
"ds_read_b64_tr_b16 a[68:69], v193 offset:4224\n"
"v_fma_f32 v64, v64, s43, -v226\n"
"v_mfma_f32_16x16x32_bf16 a[176:179], a[84:87], v[172:175], a[176:179]\n"
"buffer_load_dwordx4 v239, s[28:31], s42 offen offset:1056 nt lds\n"
"v_exp_f32 v64, v64\n"
"v_mfma_f32_16x16x32_bf16 v[248:251], v[208:211], v[48:51], 0\n"
"v_fma_f32 v65, v65, s43, -v226\n"
"v_fma_f32 v66, v66, s43, -v226\n"
"v_mfma_f32_16x16x32_bf16 a[180:183], a[88:91], v[168:171], a[180:183]\n"
"ds_read_b64_tr_b16 a[76:77], v193 offset:4256\n"
"v_fma_f32 v67, v67, s43, -v226\n"
"v_mfma_f32_16x16x32_bf16 a[180:183], a[92:95], v[172:175], a[180:183]\n"
"ds_write_b128 v214, v[48:51] offset:57344\n"
"v_mfma_f32_16x16x32_bf16 a[184:187], v[96:99], v[168:171], a[184:187]\n"
"v_exp_f32 v65, v65\n"
"v_mfma_f32_16x16x32_bf16 a[184:187], v[100:103], v[172:175], a[184:187]\n"
"v_exp_f32 v66, v66\n"
"v_mfma_f32_16x16x32_bf16 a[188:191], v[104:107], v[168:171], a[188:191]\n"
"ds_read_b128 v[168:171], v194 offset:63488\n"
"v_cvt_pk_bf16_f32 v64, v64, v65\n"
"v_mfma_f32_16x16x32_bf16 a[188:191], v[108:111], v[172:175], a[188:191]\n"
"s_waitcnt lgkmcnt(7)\n"
"ds_read_b128 v[172:175], v194 offset:64512\n"
"v_exp_f32 v67, v67\n"
"v_mfma_f32_16x16x32_bf16 a[192:195], a[80:83], v[160:163], a[192:195]\n"
"s_waitcnt lgkmcnt(6)\n"
"v_fma_f32 v68, v68, s43, -v226\n"
"v_cvt_pk_bf16_f32 v65, v66, v67\n"
"v_mfma_f32_16x16x32_bf16 a[192:195], a[84:87], v[164:167], a[192:195]\n"
"v_exp_f32 v68, v68\n"
"v_add_f32 v235, v235, v248\n"
"v_fma_f32 v69, v69, s43, -v226\n"
"v_mfma_f32_16x16x32_bf16 a[196:199], a[88:91], v[160:163], a[196:199]\n"
"v_exp_f32 v69, v69\n"
"v_mfma_f32_16x16x32_bf16 a[196:199], a[92:95], v[164:167], a[196:199]\n"
"v_fma_f32 v70, v70, s43, -v226\n"
"v_exp_f32 v70, v70\n"
"v_mfma_f32_16x16x32_bf16 a[200:203], v[96:99], v[160:163], a[200:203]\n"
"v_cvt_pk_bf16_f32 v66, v68, v69\n"
"v_fma_f32 v71, v71, s43, -v226\n"
"v_mfma_f32_16x16x32_bf16 a[200:203], v[100:103], v[164:167], a[200:203]\n"
"v_exp_f32 v71, v71\n"
"s_nop 0\n"
"v_cvt_pk_bf16_f32 v67, v70, v71\n"
"v_fma_f32 v80, v80, s43, -v227\n"
"v_mfma_f32_16x16x32_bf16 a[204:207], v[104:107], v[160:163], a[204:207]\n"
"s_waitcnt vmcnt(14)\n"
"ds_read_b128 v[160:163], v195 offset:6144\n"
"ds_read_b64_tr_b16 v[130:131], v196 offset:10560\n"
"v_mfma_f32_16x16x32_bf16 a[204:207], v[108:111], v[164:167], a[204:207]\n"
"ds_read_b128 v[164:167], v195 offset:7168\n"
"ds_read_b64_tr_b16 v[134:135], v196 offset:10568\n"
"v_mfma_f32_16x16x32_bf16 v[248:251], v[208:211], v[64:67], 0\n"
"s_waitcnt lgkmcnt(5)\n"
"v_exp_f32 v80, v80\n"
"v_mfma_f32_16x16x32_bf16 a[208:211], a[80:83], v[168:171], a[208:211]\n"
"s_waitcnt vmcnt(12) lgkmcnt(4)\n"
"ds_read_b64_tr_b16 v[138:139], v197 offset:10560\n"
"ds_read_b64_tr_b16 a[54:55], v192 offset:6336\n"
"v_mfma_f32_16x16x32_bf16 a[208:211], a[84:87], v[172:175], a[208:211]\n"
"ds_write_b128 v215, v[64:67] offset:0\n"
"v_mfma_f32_16x16x32_bf16 a[212:215], a[88:91], v[168:171], a[212:215]\n"
"ds_read_b64_tr_b16 v[142:143], v197 offset:10568\n"
"ds_read_b64_tr_b16 a[62:63], v192 offset:6368\n"
"v_mfma_f32_16x16x32_bf16 a[212:215], a[92:95], v[172:175], a[212:215]\n"
"ds_read_b64_tr_b16 a[70:71], v193 offset:6336\n"
"v_fma_f32 v81, v81, s43, -v227\n"
"v_mfma_f32_16x16x32_bf16 a[216:219], v[96:99], v[168:171], a[216:219]\n"
"s_add_u32 m0, s40, 0x1080\n"
"ds_read_b64_tr_b16 a[78:79], v193 offset:6368\n"
"v_fma_f32 v82, v82, s43, -v227\n"
"v_mfma_f32_16x16x32_bf16 a[216:219], v[100:103], v[172:175], a[216:219]\n"
"s_mov_b32 s42, 0x30000\n"
"buffer_load_dwordx4 v238, s[28:31], s42 offen nt lds\n"
"v_fma_f32 v83, v83, s43, -v227\n"
"v_mfma_f32_16x16x32_bf16 a[220:223], v[104:107], v[168:171], a[220:223]\n"
"ds_read_b128 v[168:171], v195 offset:14336\n"
"v_fma_f32 v84, v84, s43, -v227\n"
"v_mfma_f32_16x16x32_bf16 a[220:223], v[108:111], v[172:175], a[220:223]\n"
"s_waitcnt lgkmcnt(11)\n"
"ds_read_b128 v[172:175], v195 offset:15360\n"
"v_exp_f32 v81, v81\n"
"s_nop 0\n"
"v_cvt_pk_bf16_f32 v80, v80, v81\n"
"v_mfma_f32_16x16x32_bf16 a[224:227], a[80:83], v[160:163], a[224:227]\n"
"s_waitcnt lgkmcnt(10)\n"
"v_exp_f32 v82, v82\n"
"v_mfma_f32_16x16x32_bf16 a[224:227], a[84:87], v[164:167], a[224:227]\n"
"buffer_load_dwordx4 v239, s[28:31], s42 offen offset:1056 nt lds\n"
"v_add_f32 v236, v236, v248\n"
"v_mfma_f32_16x16x32_bf16 a[228:231], a[88:91], v[160:163], a[228:231]\n"
"v_exp_f32 v83, v83\n"
"v_mfma_f32_16x16x32_bf16 a[228:231], a[92:95], v[164:167], a[228:231]\n"
"v_exp_f32 v84, v84\n"
"v_cvt_pk_bf16_f32 v81, v82, v83\n"
"v_fma_f32 v85, v85, s43, -v227\n"
"v_mfma_f32_16x16x32_bf16 a[232:235], v[96:99], v[160:163], a[232:235]\n"
"s_add_u32 m0, s40, 0x18c0\n"
"v_exp_f32 v85, v85\n"
"v_mfma_f32_16x16x32_bf16 a[232:235], v[100:103], v[164:167], a[232:235]\n"
"s_waitcnt lgkmcnt(2)\n"
"s_mov_b32 s42, 0x15000\n"
"buffer_load_dwordx4 v204, s[32:35], s42 offen lds\n"
"v_fma_f32 v86, v86, s43, -v227\n"
"v_mfma_f32_16x16x32_bf16 a[236:239], v[104:107], v[160:163], a[236:239]\n"
"s_waitcnt lgkmcnt(0)\n"
"s_barrier\n"
"ds_read_b128 v[160:163], v194 offset:0\n"
"v_mfma_f32_16x16x32_bf16 a[236:239], v[108:111], v[164:167], a[236:239]\n"
"ds_read_b128 v[164:167], v194 offset:1024\n"
"v_exp_f32 v86, v86\n"
"v_cvt_pk_bf16_f32 v82, v84, v85\n"
"v_mfma_f32_16x16x32_bf16 a[240:243], a[80:83], v[168:171], a[240:243]\n"
"v_fma_f32 v87, v87, s43, -v227\n"
"v_mfma_f32_16x16x32_bf16 a[240:243], a[84:87], v[172:175], a[240:243]\n"
"buffer_load_dwordx4 v205, s[32:35], s42 offen offset:1056 lds\n"
"v_exp_f32 v87, v87\n"
"v_mfma_f32_16x16x32_bf16 a[244:247], a[88:91], v[168:171], a[244:247]\n"
"v_cvt_pk_bf16_f32 v83, v86, v87\n"
"v_mfma_f32_16x16x32_bf16 a[244:247], a[92:95], v[172:175], a[244:247]\n"
"ds_write_b128 v215, v[80:83] offset:8192\n"
"v_mfma_f32_16x16x32_bf16 a[248:251], v[96:99], v[168:171], a[248:251]\n"
"s_add_u32 m0, s40, 0x2940\n"
"v_mfma_f32_16x16x32_bf16 a[248:251], v[100:103], v[172:175], a[248:251]\n"
"s_mov_b32 s42, 0x32000\n"
"buffer_load_dwordx4 v238, s[28:31], s42 offen nt lds\n"
"v_mfma_f32_16x16x32_bf16 a[252:255], v[104:107], v[168:171], a[252:255]\n"
"ds_read_b128 v[168:171], v194 offset:8192\n"
"v_mfma_f32_16x16x32_bf16 a[252:255], v[108:111], v[172:175], a[252:255]\n"
"s_waitcnt lgkmcnt(3)\n"
"ds_read_b128 v[172:175], v194 offset:9216\n"
"v_mfma_f32_16x16x32_bf16 a[96:99], a[48:51], v[160:163], a[96:99]\n"
"s_waitcnt lgkmcnt(3)\n"
"v_mfma_f32_16x16x32_bf16 a[96:99], a[52:55], v[164:167], a[96:99]\n"
"buffer_load_dwordx4 v239, s[28:31], s42 offen offset:1056 nt lds\n"
"v_mfma_f32_16x16x32_bf16 a[100:103], a[56:59], v[160:163], a[100:103]\n"
"s_waitcnt vmcnt(16)\n"
"ds_read_b64_tr_b16 a[80:81], v192 offset:16896\n"
"v_mfma_f32_16x16x32_bf16 a[100:103], a[60:63], v[164:167], a[100:103]\n"
"ds_read_b64_tr_b16 a[88:89], v192 offset:16928\n"
"v_fma_f32 v184, v184, s43, -v218\n"
"v_mfma_f32_16x16x32_bf16 v[248:251], v[208:211], v[80:83], 0\n"
"v_exp_f32 v184, v184\n"
"v_mfma_f32_16x16x32_bf16 a[104:107], a[64:67], v[160:163], a[104:107]\n"
"ds_read_b64_tr_b16 v[96:97], v193 offset:16896\n"
"v_fma_f32 v185, v185, s43, -v218\n"
"v_mfma_f32_16x16x32_bf16 a[104:107], a[68:71], v[164:167], a[104:107]\n"
"v_exp_f32 v185, v185\n"
"v_fma_f32 v186, v186, s43, -v218\n"
"v_mfma_f32_16x16x32_bf16 a[108:111], a[72:75], v[160:163], a[108:111]\n"
"ds_read_b128 v[160:163], v194 offset:16384\n"
"ds_read_b64_tr_b16 v[104:105], v193 offset:16928\n"
"v_mfma_f32_16x16x32_bf16 a[108:111], a[76:79], v[164:167], a[108:111]\n"
"s_waitcnt lgkmcnt(6)\n"
"ds_read_b128 v[164:167], v194 offset:17408\n"
"v_exp_f32 v186, v186\n"
"v_cvt_pk_bf16_f32 v184, v184, v185\n"
"v_mfma_f32_16x16x32_bf16 a[112:115], a[48:51], v[168:171], a[112:115]\n"
"s_waitcnt lgkmcnt(6)\n"
"v_fma_f32 v187, v187, s43, -v218\n"
"v_fma_f32 v188, v188, s43, -v218\n"
"v_mfma_f32_16x16x32_bf16 a[112:115], a[52:55], v[172:175], a[112:115]\n"
"v_exp_f32 v187, v187\n"
"v_exp_f32 v188, v188\n"
"v_mfma_f32_16x16x32_bf16 a[116:119], a[56:59], v[168:171], a[116:119]\n"
"v_cvt_pk_bf16_f32 v185, v186, v187\n"
"v_add_f32 v237, v237, v248\n"
"v_mfma_f32_16x16x32_bf16 a[116:119], a[60:63], v[172:175], a[116:119]\n"
"v_fma_f32 v189, v189, s43, -v218\n"
"v_exp_f32 v189, v189\n"
"v_fma_f32 v190, v190, s43, -v218\n"
"v_mfma_f32_16x16x32_bf16 a[120:123], a[64:67], v[168:171], a[120:123]\n"
"v_exp_f32 v190, v190\n"
"v_mfma_f32_16x16x32_bf16 a[120:123], a[68:71], v[172:175], a[120:123]\n"
"v_cvt_pk_bf16_f32 v186, v188, v189\n"
"v_fma_f32 v191, v191, s43, -v218\n"
"v_exp_f32 v191, v191\n"
"v_mfma_f32_16x16x32_bf16 a[124:127], a[72:75], v[168:171], a[124:127]\n"
"s_waitcnt vmcnt(14)\n"
"s_add_u32 m0, s40, 0x4200\n"
"ds_read_b128 v[168:171], v194 offset:24576\n"
"ds_read_b64_tr_b16 v[144:145], v196 offset:0\n"
"v_mfma_f32_16x16x32_bf16 a[124:127], a[76:79], v[172:175], a[124:127]\n"
"s_waitcnt lgkmcnt(4)\n"
"ds_read_b128 v[172:175], v194 offset:25600\n"
"ds_read_b64_tr_b16 v[148:149], v196 offset:8\n"
"v_cvt_pk_bf16_f32 v187, v190, v191\n"
"v_mfma_f32_16x16x32_bf16 a[128:131], a[48:51], v[160:163], a[128:131]\n"
"s_waitcnt lgkmcnt(4)\n"
"ds_read_b64_tr_b16 v[152:153], v197 offset:0\n"
"v_accvgpr_read_b32 v240, a8\n"
"v_mfma_f32_16x16x32_bf16 a[128:131], a[52:55], v[164:167], a[128:131]\n"
"s_mov_b32 s42, 0x34000\n"
"buffer_load_dwordx4 v238, s[28:31], s42 offen nt lds\n"
"v_fma_f32 v240, v240, s43, -v219\n"
"v_mfma_f32_16x16x32_bf16 v[248:251], v[208:211], v[184:187], 0\n"
"v_exp_f32 v240, v240\n"
"v_mfma_f32_16x16x32_bf16 a[132:135], a[56:59], v[160:163], a[132:135]\n"
"ds_read_b64_tr_b16 v[156:157], v197 offset:8\n"
"v_accvgpr_read_b32 v241, a9\n"
"v_mfma_f32_16x16x32_bf16 a[132:135], a[60:63], v[164:167], a[132:135]\n"
"ds_write_b128 v214, v[184:187] offset:4096\n"
"v_mfma_f32_16x16x32_bf16 a[136:139], a[64:67], v[160:163], a[136:139]\n"
"s_waitcnt vmcnt(13)\n"
"ds_read_b64_tr_b16 a[82:83], v192 offset:14784\n"
"v_fma_f32 v241, v241, s43, -v219\n"
"v_mfma_f32_16x16x32_bf16 a[136:139], a[68:71], v[164:167], a[136:139]\n"
"buffer_load_dwordx4 v239, s[28:31], s42 offen offset:1056 nt lds\n"
"v_mfma_f32_16x16x32_bf16 a[140:143], a[72:75], v[160:163], a[140:143]\n"
"ds_read_b128 v[160:163], v194 offset:32768\n"
"ds_read_b64_tr_b16 a[90:91], v192 offset:14816\n"
"v_mfma_f32_16x16x32_bf16 a[140:143], a[76:79], v[164:167], a[140:143]\n"
"s_waitcnt lgkmcnt(9)\n"
"ds_read_b128 v[164:167], v194 offset:33792\n"
"ds_read_b64_tr_b16 v[98:99], v193 offset:14784\n"
"v_exp_f32 v241, v241\n"
"v_mfma_f32_16x16x32_bf16 a[144:147], a[48:51], v[168:171], a[144:147]\n"
"s_waitcnt lgkmcnt(9)\n"
"ds_read_b64_tr_b16 v[106:107], v193 offset:14816\n"
"v_accvgpr_read_b32 v242, a10\n"
"v_mfma_f32_16x16x32_bf16 a[144:147], a[52:55], v[172:175], a[144:147]\n"
"v_fma_f32 v242, v242, s43, -v219\n"
"v_exp_f32 v242, v242\n"
"v_cvt_pk_bf16_f32 v244, v240, v241\n"
"v_mfma_f32_16x16x32_bf16 a[148:151], a[56:59], v[168:171], a[148:151]\n"
"v_accvgpr_read_b32 v243, a11\n"
"v_fma_f32 v243, v243, s43, -v219\n"
"v_mfma_f32_16x16x32_bf16 a[148:151], a[60:63], v[172:175], a[148:151]\n"
"v_add_f32 v228, v228, v248\n"
"v_exp_f32 v243, v243\n"
"v_accvgpr_read_b32 v240, a12\n"
"v_mfma_f32_16x16x32_bf16 a[152:155], a[64:67], v[168:171], a[152:155]\n"
"s_add_u32 m0, s40, 0\n"
"v_fma_f32 v240, v240, s43, -v219\n"
"v_cvt_pk_bf16_f32 v245, v242, v243\n"
"v_mfma_f32_16x16x32_bf16 a[152:155], a[68:71], v[172:175], a[152:155]\n"
"s_waitcnt lgkmcnt(7)\n"
"s_mov_b32 s42, 0x18800\n"
"buffer_load_dwordx4 v204, s[32:35], s42 offen lds\n"
"v_accvgpr_read_b32 v241, a13\n"
"v_mfma_f32_16x16x32_bf16 a[156:159], a[72:75], v[168:171], a[156:159]\n"
"s_barrier\n"
"ds_read_b128 v[168:171], v194 offset:40960\n"
"v_mfma_f32_16x16x32_bf16 a[156:159], a[76:79], v[172:175], a[156:159]\n"
"s_waitcnt lgkmcnt(5)\n"
"ds_read_b128 v[172:175], v194 offset:41984\n"
"v_exp_f32 v240, v240\n"
"v_fma_f32 v241, v241, s43, -v219\n"
"v_mfma_f32_16x16x32_bf16 a[160:163], a[48:51], v[160:163], a[160:163]\n"
"s_waitcnt lgkmcnt(4)\n"
"v_exp_f32 v241, v241\n"
"v_mfma_f32_16x16x32_bf16 a[160:163], a[52:55], v[164:167], a[160:163]\n"
"buffer_load_dwordx4 v205, s[32:35], s42 offen offset:1056 lds\n"
"v_accvgpr_read_b32 v242, a14\n"
"v_fma_f32 v242, v242, s43, -v219\n"
"v_mfma_f32_16x16x32_bf16 a[164:167], a[56:59], v[160:163], a[164:167]\n"
"v_exp_f32 v242, v242\n"
"v_mfma_f32_16x16x32_bf16 a[164:167], a[60:63], v[164:167], a[164:167]\n"
"v_cvt_pk_bf16_f32 v246, v240, v241\n"
"v_accvgpr_read_b32 v243, a15\n"
"v_fma_f32 v243, v243, s43, -v219\n"
"v_accvgpr_read_b32 v240, a24\n"
"v_mfma_f32_16x16x32_bf16 a[168:171], a[64:67], v[160:163], a[168:171]\n"
"s_add_u32 m0, s40, 0x39c0\n"
"v_exp_f32 v243, v243\n"
"v_mfma_f32_16x16x32_bf16 a[168:171], a[68:71], v[164:167], a[168:171]\n"
"s_waitcnt lgkmcnt(2)\n"
"s_mov_b32 s42, 0x36000\n"
"buffer_load_dwordx4 v238, s[28:31], s42 offen nt lds\n"
"v_cvt_pk_bf16_f32 v247, v242, v243\n"
"v_fma_f32 v240, v240, s43, -v220\n"
"v_mfma_f32_16x16x32_bf16 a[172:175], a[72:75], v[160:163], a[172:175]\n"
"s_waitcnt vmcnt(15)\n"
"ds_read_b128 v[160:163], v194 offset:49152\n"
"ds_read_b64_tr_b16 a[84:85], v192 offset:2112\n"
"v_mfma_f32_16x16x32_bf16 a[172:175], a[76:79], v[164:167], a[172:175]\n"
"ds_read_b128 v[164:167], v194 offset:50176\n"
"ds_read_b64_tr_b16 a[92:93], v192 offset:2144\n"
"v_mfma_f32_16x16x32_bf16 v[248:251], v[208:211], v[244:247], 0\n"
"s_waitcnt lgkmcnt(5)\n"
"v_exp_f32 v240, v240\n"
"v_mfma_f32_16x16x32_bf16 a[176:179], a[48:51], v[168:171], a[176:179]\n"
".Lpad7_%=:\n"
"s_cbranch_vccnz .Lpad11_%=\n"
"s_waitcnt lgkmcnt(4)\n"
"ds_read_b64_tr_b16 v[100:101], v193 offset:2112\n"
"v_accvgpr_read_b32 v241, a25\n"
"v_mfma_f32_16x16x32_bf16 a[176:179], a[52:55], v[172:175], a[176:179]\n"
"buffer_load_dwordx4 v239, s[28:31], s42 offen offset:1056 nt lds\n"
"v_fma_f32 v241, v241, s43, -v220\n"
"v_mfma_f32_16x16x32_bf16 a[180:183], a[56:59], v[168:171], a[180:183]\n"
"ds_read_b64_tr_b16 v[108:109], v193 offset:2144\n"
"v_accvgpr_read_b32 v242, a26\n"
"v_mfma_f32_16x16x32_bf16 a[180:183], a[60:63], v[172:175], a[180:183]\n"
"ds_write_b128 v214, v[244:247] offset:12288\n"
"v_mfma_f32_16x16x32_bf16 a[184:187], a[64:67], v[168:171], a[184:187]\n"
"v_exp_f32 v241, v241\n"
"v_mfma_f32_16x16x32_bf16 a[184:187], a[68:71], v[172:175], a[184:187]\n"
"v_fma_f32 v242, v242, s43, -v220\n"
"v_exp_f32 v242, v242\n"
"v_mfma_f32_16x16x32_bf16 a[188:191], a[72:75], v[168:171], a[188:191]\n"
"ds_read_b128 v[168:171], v194 offset:57344\n"
"v_cvt_pk_bf16_f32 v244, v240, v241\n"
"v_mfma_f32_16x16x32_bf16 a[188:191], a[76:79], v[172:175], a[188:191]\n"
"s_waitcnt lgkmcnt(7)\n"
"ds_read_b128 v[172:175], v194 offset:58368\n"
"v_accvgpr_read_b32 v243, a27\n"
"v_fma_f32 v243, v243, s43, -v220\n"
"v_add_f32 v229, v229, v248\n"
"v_mfma_f32_16x16x32_bf16 a[192:195], a[48:51], v[160:163], a[192:195]\n"
"s_waitcnt lgkmcnt(6)\n"
"v_exp_f32 v243, v243\n"
"v_mfma_f32_16x16x32_bf16 a[192:195], a[52:55], v[164:167], a[192:195]\n"
"v_accvgpr_read_b32 v240, a28\n"
"v_fma_f32 v240, v240, s43, -v220\n"
"v_exp_f32 v240, v240\n"
"v_mfma_f32_16x16x32_bf16 a[196:199], a[56:59], v[160:163], a[196:199]\n"
"v_cvt_pk_bf16_f32 v245, v242, v243\n"
"v_accvgpr_read_b32 v241, a29\n"
"v_mfma_f32_16x16x32_bf16 a[196:199], a[60:63], v[164:167], a[196:199]\n"
"v_fma_f32 v241, v241, s43, -v220\n"
"v_exp_f32 v241, v241\n"
"v_mfma_f32_16x16x32_bf16 a[200:203], a[64:67], v[160:163], a[200:203]\n"
"v_accvgpr_read_b32 v242, a30\n"
"v_fma_f32 v242, v242, s43, -v220\n"
"v_mfma_f32_16x16x32_bf16 a[200:203], a[68:71], v[164:167], a[200:203]\n"
"v_exp_f32 v242, v242\n"
"v_cvt_pk_bf16_f32 v246, v240, v241\n"
"v_accvgpr_read_b32 v243, a31\n"
"v_mfma_f32_16x16x32_bf16 a[204:207], a[72:75], v[160:163], a[204:207]\n"
"s_waitcnt vmcnt(14)\n"
"ds_read_b128 v[160:163], v195 offset:0\n"
"ds_read_b64_tr_b16 v[146:147], v196 offset:12672\n"
"v_mfma_f32_16x16x32_bf16 a[204:207], a[76:79], v[164:167], a[204:207]\n"
"s_waitcnt lgkmcnt(3)\n"
"ds_read_b128 v[164:167], v195 offset:1024\n"
"ds_read_b64_tr_b16 v[150:151], v196 offset:12680\n"
"v_fma_f32 v243, v243, s43, -v220\n"
"v_permlane32_swap_b32 v228, v229\n"
"v_mfma_f32_16x16x32_bf16 a[208:211], a[48:51], v[168:171], a[208:211]\n"
"s_waitcnt vmcnt(12) lgkmcnt(4)\n"
"ds_read_b64_tr_b16 v[154:155], v197 offset:12672\n"
"ds_read_b64_tr_b16 a[86:87], v192 offset:8448\n"
"v_mfma_f32_16x16x32_bf16 a[208:211], a[52:55], v[172:175], a[208:211]\n"
"v_exp_f32 v243, v243\n"
"s_nop 0\n"
"v_cvt_pk_bf16_f32 v247, v242, v243\n"
"v_accvgpr_read_b32 v240, a40\n"
"v_mfma_f32_16x16x32_bf16 a[212:215], a[56:59], v[168:171], a[212:215]\n"
"ds_read_b64_tr_b16 v[158:159], v197 offset:12680\n"
"ds_read_b64_tr_b16 a[94:95], v192 offset:8480\n"
"v_mfma_f32_16x16x32_bf16 a[212:215], a[60:63], v[172:175], a[212:215]\n"
"ds_read_b64_tr_b16 v[102:103], v193 offset:8448\n"
"v_fma_f32 v240, v240, s43, -v221\n"
"v_mfma_f32_16x16x32_bf16 v[248:251], v[208:211], v[244:247], 0\n"
"v_exp_f32 v240, v240\n"
"v_mfma_f32_16x16x32_bf16 a[216:219], a[64:67], v[168:171], a[216:219]\n"
"s_add_u32 m0, s40, 0x840\n"
"ds_read_b64_tr_b16 v[110:111], v193 offset:8480\n"
"v_accvgpr_read_b32 v241, a41\n"
"v_mfma_f32_16x16x32_bf16 a[216:219], a[68:71], v[172:175], a[216:219]\n"
"s_mov_b32 s42, 0x38000\n"
"buffer_load_dwordx4 v238, s[28:31], s42 offen nt lds\n"
"v_fma_f32 v241, v241, s43, -v221\n"
"v_mfma_f32_16x16x32_bf16 a[220:223], a[72:75], v[168:171], a[220:223]\n"
"ds_read_b128 v[168:171], v195 offset:8192\n"
"v_accvgpr_read_b32 v242, a42\n"
"v_mfma_f32_16x16x32_bf16 a[220:223], a[76:79], v[172:175], a[220:223]\n"
"s_waitcnt lgkmcnt(10)\n"
"ds_read_b128 v[172:175], v195 offset:9216\n"
"v_exp_f32 v241, v241\n"
"v_fma_f32 v242, v242, s43, -v221\n"
"v_mfma_f32_16x16x32_bf16 a[224:227], a[48:51], v[160:163], a[224:227]\n"
"s_waitcnt lgkmcnt(9)\n"
"v_exp_f32 v242, v242\n"
"v_mfma_f32_16x16x32_bf16 a[224:227], a[52:55], v[164:167], a[224:227]\n"
"buffer_load_dwordx4 v239, s[28:31], s42 offen offset:1056 nt lds\n"
"v_accvgpr_read_b32 v243, a43\n"
"v_fma_f32 v243, v243, s43, -v221\n"
"v_mfma_f32_16x16x32_bf16 a[228:231], a[56:59], v[160:163], a[228:231]\n"
"v_add_f32 v230, v230, v248\n"
"v_mfma_f32_16x16x32_bf16 a[228:231], a[60:63], v[164:167], a[228:231]\n"
"ds_write_b128 v214, v[244:247] offset:20480\n"
"v_mfma_f32_16x16x32_bf16 a[232:235], a[64:67], v[160:163], a[232:235]\n"
"s_add_u32 m0, s40, 0x2100\n"
"ds_write_b32 v216, v228 offset:0\n"
"v_mfma_f32_16x16x32_bf16 a[232:235], a[68:71], v[164:167], a[232:235]\n"
"s_waitcnt lgkmcnt(4)\n"
"s_mov_b32 s42, 0x1c000\n"
"buffer_load_dwordx4 v204, s[32:35], s42 offen lds\n"
"v_cvt_pk_bf16_f32 v244, v240, v241\n"
"v_mfma_f32_16x16x32_bf16 a[236:239], a[72:75], v[160:163], a[236:239]\n"
"ds_read_b128 v[160:163], v194 offset:2048\n"
"v_accvgpr_read_b32 v240, a44\n"
"v_mfma_f32_16x16x32_bf16 a[236:239], a[76:79], v[164:167], a[236:239]\n"
"s_waitcnt lgkmcnt(4)\n"
"ds_read_b128 v[164:167], v194 offset:3072\n"
"v_exp_f32 v243, v243\n"
"v_mfma_f32_16x16x32_bf16 a[240:243], a[48:51], v[168:171], a[240:243]\n"
"s_waitcnt lgkmcnt(4)\n"
"v_fma_f32 v240, v240, s43, -v221\n"
"v_cvt_pk_bf16_f32 v245, v242, v243\n"
"v_mfma_f32_16x16x32_bf16 a[240:243], a[52:55], v[172:175], a[240:243]\n"
"buffer_load_dwordx4 v205, s[32:35], s42 offen offset:1056 lds\n"
"v_exp_f32 v240, v240\n"
"v_mfma_f32_16x16x32_bf16 a[244:247], a[56:59], v[168:171], a[244:247]\n"
"v_accvgpr_read_b32 v241, a45\n"
"v_fma_f32 v241, v241, s43, -v221\n"
"v_mfma_f32_16x16x32_bf16 a[244:247], a[60:63], v[172:175], a[244:247]\n"
"v_exp_f32 v241, v241\n"
"v_accvgpr_read_b32 v242, a46\n"
"v_fma_f32 v242, v242, s43, -v221\n"
"v_mfma_f32_16x16x32_bf16 a[248:251], a[64:67], v[168:171], a[248:251]\n"
"s_add_u32 m0, s40, 0x3180\n"
"v_exp_f32 v242, v242\n"
"v_mfma_f32_16x16x32_bf16 a[248:251], a[68:71], v[172:175], a[248:251]\n"
"s_mov_b32 s42, 0x3a000\n"
"buffer_load_dwordx4 v238, s[28:31], s42 offen nt lds\n"
"v_cvt_pk_bf16_f32 v246, v240, v241\n"
"v_accvgpr_read_b32 v243, a47\n"
"v_mfma_f32_16x16x32_bf16 a[252:255], a[72:75], v[168:171], a[252:255]\n"
"ds_read_b128 v[168:171], v194 offset:10240\n"
"v_fma_f32 v243, v243, s43, -v221\n"
"v_mfma_f32_16x16x32_bf16 a[252:255], a[76:79], v[172:175], a[252:255]\n"
"s_waitcnt lgkmcnt(2)\n"
"ds_read_b128 v[172:175], v194 offset:11264\n"
"v_exp_f32 v243, v243\n"
"s_nop 0\n"
"v_cvt_pk_bf16_f32 v247, v242, v243\n"
"v_mfma_f32_16x16x32_bf16 a[96:99], a[80:83], v[160:163], a[96:99]\n"
"s_waitcnt lgkmcnt(2)\n"
"v_fma_f32 v8, v8, s43, -v222\n"
"v_fma_f32 v9, v9, s43, -v222\n"
"v_mfma_f32_16x16x32_bf16 a[96:99], a[84:87], v[164:167], a[96:99]\n"
"buffer_load_dwordx4 v239, s[28:31], s42 offen offset:1056 nt lds\n"
"v_exp_f32 v8, v8\n"
"v_mfma_f32_16x16x32_bf16 v[248:251], v[208:211], v[244:247], 0\n"
"v_exp_f32 v9, v9\n"
"v_mfma_f32_16x16x32_bf16 a[100:103], a[88:91], v[160:163], a[100:103]\n"
"s_waitcnt vmcnt(16)\n"
"ds_read_b64_tr_b16 a[48:49], v192 offset:4224\n"
"v_fma_f32 v10, v10, s43, -v222\n"
"v_mfma_f32_16x16x32_bf16 a[100:103], a[92:95], v[164:167], a[100:103]\n"
"ds_read_b64_tr_b16 a[56:57], v192 offset:4256\n"
"v_exp_f32 v10, v10\n"
"v_mfma_f32_16x16x32_bf16 a[104:107], v[96:99], v[160:163], a[104:107]\n"
"ds_read_b64_tr_b16 a[64:65], v193 offset:4224\n"
"v_cvt_pk_bf16_f32 v8, v8, v9\n"
"v_mfma_f32_16x16x32_bf16 a[104:107], v[100:103], v[164:167], a[104:107]\n"
"ds_write_b128 v214, v[244:247] offset:28672\n"
"v_mfma_f32_16x16x32_bf16 a[108:111], v[104:107], v[160:163], a[108:111]\n"
"ds_read_b128 v[160:163], v194 offset:18432\n"
"ds_read_b64_tr_b16 a[72:73], v193 offset:4256\n"
"v_mfma_f32_16x16x32_bf16 a[108:111], v[108:111], v[164:167], a[108:111]\n"
"s_waitcnt lgkmcnt(7)\n"
"ds_read_b128 v[164:167], v194 offset:19456\n"
"v_fma_f32 v11, v11, s43, -v222\n"
"v_fma_f32 v12, v12, s43, -v222\n"
"v_mfma_f32_16x16x32_bf16 a[112:115], a[80:83], v[168:171], a[112:115]\n"
"s_waitcnt lgkmcnt(7)\n"
"v_exp_f32 v11, v11\n"
"v_mfma_f32_16x16x32_bf16 a[112:115], a[84:87], v[172:175], a[112:115]\n"
"v_exp_f32 v12, v12\n"
"v_cvt_pk_bf16_f32 v9, v10, v11\n"
"v_add_f32 v231, v231, v248\n"
"v_mfma_f32_16x16x32_bf16 a[116:119], a[88:91], v[168:171], a[116:119]\n"
"v_fma_f32 v13, v13, s43, -v222\n"
"v_fma_f32 v14, v14, s43, -v222\n"
"v_mfma_f32_16x16x32_bf16 a[116:119], a[92:95], v[172:175], a[116:119]\n"
"v_exp_f32 v13, v13\n"
"v_exp_f32 v14, v14\n"
"v_mfma_f32_16x16x32_bf16 a[120:123], v[96:99], v[168:171], a[120:123]\n"
"v_cvt_pk_bf16_f32 v10, v12, v13\n"
"v_fma_f32 v15, v15, s43, -v222\n"
"v_mfma_f32_16x16x32_bf16 a[120:123], v[100:103], v[172:175], a[120:123]\n"
"v_exp_f32 v15, v15\n"
"s_nop 0\n"
"v_cvt_pk_bf16_f32 v11, v14, v15\n"
"v_mfma_f32_16x16x32_bf16 a[124:127], v[104:107], v[168:171], a[124:127]\n"
"s_waitcnt vmcnt(14)\n"
"s_add_u32 m0, s40, 0x1080\n"
"ds_read_b128 v[168:171], v194 offset:26624\n"
"ds_read_b64_tr_b16 v[176:177], v196 offset:6336\n"
"v_mfma_f32_16x16x32_bf16 a[124:127], v[108:111], v[172:175], a[124:127]\n"
"ds_read_b128 v[172:175], v194 offset:27648\n"
"ds_read_b64_tr_b16 v[180:181], v196 offset:6344\n"
"v_permlane32_swap_b32 v230, v231\n"
"v_fma_f32 v24, v24, s43, -v223\n"
"v_mfma_f32_16x16x32_bf16 v[248:251], v[208:211], v[8:11], 0\n"
"s_waitcnt lgkmcnt(6)\n"
"v_exp_f32 v24, v24\n"
"v_mfma_f32_16x16x32_bf16 a[128:131], a[80:83], v[160:163], a[128:131]\n"
"s_waitcnt lgkmcnt(4)\n"
"ds_read_b64_tr_b16 a[0:1], v197 offset:6336\n"
"v_fma_f32 v25, v25, s43, -v223\n"
"v_mfma_f32_16x16x32_bf16 a[128:131], a[84:87], v[164:167], a[128:131]\n"
"s_mov_b32 s42, 0x3c000\n"
"buffer_load_dwordx4 v238, s[28:31], s42 offen nt lds\n"
"v_fma_f32 v26, v26, s43, -v223\n"
"v_mfma_f32_16x16x32_bf16 a[132:135], a[88:91], v[160:163], a[132:135]\n"
"ds_read_b64_tr_b16 a[4:5], v197 offset:6344\n"
"v_mfma_f32_16x16x32_bf16 a[132:135], a[92:95], v[164:167], a[132:135]\n"
"ds_write_b128 v214, v[8:11] offset:36864\n"
"v_mfma_f32_16x16x32_bf16 a[136:139], v[96:99], v[160:163], a[136:139]\n"
"s_waitcnt vmcnt(13)\n"
"ds_read_b64_tr_b16 a[50:51], v192 offset:10560\n"
"v_fma_f32 v27, v27, s43, -v223\n"
"v_mfma_f32_16x16x32_bf16 a[136:139], v[100:103], v[164:167], a[136:139]\n"
"buffer_load_dwordx4 v239, s[28:31], s42 offen offset:1056 nt lds\n"
"v_mfma_f32_16x16x32_bf16 a[140:143], v[104:107], v[160:163], a[140:143]\n"
"ds_read_b128 v[160:163], v194 offset:34816\n"
"ds_read_b64_tr_b16 a[58:59], v192 offset:10592\n"
"v_mfma_f32_16x16x32_bf16 a[140:143], v[108:111], v[164:167], a[140:143]\n"
"s_waitcnt lgkmcnt(9)\n"
"ds_read_b128 v[164:167], v194 offset:35840\n"
"ds_read_b64_tr_b16 a[66:67], v193 offset:10560\n"
"ds_write_b32 v216, v230 offset:512\n"
"v_fma_f32 v28, v28, s43, -v223\n"
"v_mfma_f32_16x16x32_bf16 a[144:147], a[80:83], v[168:171], a[144:147]\n"
"s_waitcnt lgkmcnt(10)\n"
"ds_read_b64_tr_b16 a[74:75], v193 offset:10592\n"
"v_mfma_f32_16x16x32_bf16 a[144:147], a[84:87], v[172:175], a[144:147]\n"
"v_exp_f32 v25, v25\n"
"v_exp_f32 v26, v26\n"
"v_mfma_f32_16x16x32_bf16 a[148:151], a[88:91], v[168:171], a[148:151]\n"
"v_cvt_pk_bf16_f32 v24, v24, v25\n"
"v_add_f32 v232, v232, v248\n"
"v_mfma_f32_16x16x32_bf16 a[148:151], a[92:95], v[172:175], a[148:151]\n"
"v_exp_f32 v27, v27\n"
"s_nop 0\n"
"v_cvt_pk_bf16_f32 v25, v26, v27\n"
"v_mfma_f32_16x16x32_bf16 a[152:155], v[96:99], v[168:171], a[152:155]\n"
"v_exp_f32 v28, v28\n"
"v_mfma_f32_16x16x32_bf16 a[152:155], v[100:103], v[172:175], a[152:155]\n"
"v_fma_f32 v29, v29, s43, -v223\n"
"v_exp_f32 v29, v29\n"
"v_fma_f32 v30, v30, s43, -v223\n"
"v_mfma_f32_16x16x32_bf16 a[156:159], v[104:107], v[168:171], a[156:159]\n"
"s_add_u32 m0, s40, 0x2940\n"
"ds_read_b128 v[168:171], v194 offset:43008\n"
"v_cvt_pk_bf16_f32 v26, v28, v29\n"
"v_mfma_f32_16x16x32_bf16 a[156:159], v[108:111], v[172:175], a[156:159]\n"
"s_waitcnt lgkmcnt(6)\n"
"ds_read_b128 v[172:175], v194 offset:44032\n"
"v_exp_f32 v30, v30\n"
"v_fma_f32 v31, v31, s43, -v223\n"
"v_mfma_f32_16x16x32_bf16 a[160:163], a[80:83], v[160:163], a[160:163]\n"
"s_waitcnt lgkmcnt(5)\n"
"v_exp_f32 v31, v31\n"
"v_mfma_f32_16x16x32_bf16 a[160:163], a[84:87], v[164:167], a[160:163]\n"
"s_waitcnt lgkmcnt(2)\n"
"s_mov_b32 s42, 0x3e000\n"
"buffer_load_dwordx4 v238, s[28:31], s42 offen nt lds\n"
"v_cvt_pk_bf16_f32 v27, v30, v31\n"
"v_fma_f32 v40, v40, s43, -v224\n"
"v_mfma_f32_16x16x32_bf16 a[164:167], a[88:91], v[160:163], a[164:167]\n"
"v_exp_f32 v40, v40\n"
"v_mfma_f32_16x16x32_bf16 a[164:167], a[92:95], v[164:167], a[164:167]\n"
"ds_write_b128 v214, v[24:27] offset:45056\n"
"v_mfma_f32_16x16x32_bf16 v[248:251], v[208:211], v[24:27], 0\n"
"v_fma_f32 v41, v41, s43, -v224\n"
"v_fma_f32 v42, v42, s43, -v224\n"
"v_mfma_f32_16x16x32_bf16 a[168:171], v[96:99], v[160:163], a[168:171]\n"
"v_exp_f32 v41, v41\n"
"v_mfma_f32_16x16x32_bf16 a[168:171], v[100:103], v[164:167], a[168:171]\n"
"buffer_load_dwordx4 v239, s[28:31], s42 offen offset:1056 nt lds\n"
"v_cvt_pk_bf16_f32 v40, v40, v41\n"
"v_mfma_f32_16x16x32_bf16 a[172:175], v[104:107], v[160:163], a[172:175]\n"
"s_waitcnt vmcnt(14)\n"
"s_add_u32 m0, s40, 0x18c0\n"
"ds_read_b128 v[160:163], v194 offset:51200\n"
"ds_read_b64_tr_b16 a[52:53], v192 offset:16896\n"
"v_mfma_f32_16x16x32_bf16 a[172:175], v[108:111], v[164:167], a[172:175]\n"
"s_waitcnt lgkmcnt(4)\n"
"ds_read_b128 v[164:167], v194 offset:52224\n"
"ds_read_b64_tr_b16 a[60:61], v192 offset:16928\n"
"v_exp_f32 v42, v42\n"
"v_mfma_f32_16x16x32_bf16 a[176:179], a[80:83], v[168:171], a[176:179]\n"
"s_waitcnt lgkmcnt(5)\n"
"ds_read_b64_tr_b16 a[68:69], v193 offset:16896\n"
"v_fma_f32 v43, v43, s43, -v224\n"
"v_mfma_f32_16x16x32_bf16 a[176:179], a[84:87], v[172:175], a[176:179]\n"
"s_mov_b32 s42, 0x1f800\n"
"buffer_load_dwordx4 v204, s[32:35], s42 offen lds\n"
"v_exp_f32 v43, v43\n"
"v_mfma_f32_16x16x32_bf16 a[180:183], a[88:91], v[168:171], a[180:183]\n"
"ds_read_b64_tr_b16 a[76:77], v193 offset:16928\n"
"v_fma_f32 v44, v44, s43, -v224\n"
"v_mfma_f32_16x16x32_bf16 a[180:183], a[92:95], v[172:175], a[180:183]\n"
"v_exp_f32 v44, v44\n"
"v_cvt_pk_bf16_f32 v41, v42, v43\n"
"v_add_f32 v233, v233, v248\n"
"v_mfma_f32_16x16x32_bf16 a[184:187], v[96:99], v[168:171], a[184:187]\n"
"v_fma_f32 v45, v45, s43, -v224\n"
"v_fma_f32 v46, v46, s43, -v224\n"
"v_mfma_f32_16x16x32_bf16 a[184:187], v[100:103], v[172:175], a[184:187]\n"
"buffer_load_dwordx4 v205, s[32:35], s42 offen offset:1056 lds\n"
"v_exp_f32 v45, v45\n"
"v_mfma_f32_16x16x32_bf16 a[188:191], v[104:107], v[168:171], a[188:191]\n"
"ds_read_b128 v[168:171], v194 offset:59392\n"
"v_cvt_pk_bf16_f32 v42, v44, v45\n"
"v_mfma_f32_16x16x32_bf16 a[188:191], v[108:111], v[172:175], a[188:191]\n"
"s_waitcnt lgkmcnt(6)\n"
"ds_read_b128 v[172:175], v194 offset:60416\n"
"v_exp_f32 v46, v46\n"
"v_mfma_f32_16x16x32_bf16 a[192:195], a[80:83], v[160:163], a[192:195]\n"
"s_waitcnt lgkmcnt(5)\n"
"v_fma_f32 v47, v47, s43, -v224\n"
"v_permlane32_swap_b32 v232, v233\n"
"v_mfma_f32_16x16x32_bf16 a[192:195], a[84:87], v[164:167], a[192:195]\n"
"v_exp_f32 v47, v47\n"
"s_nop 0\n"
"v_cvt_pk_bf16_f32 v43, v46, v47\n"
"v_fma_f32 v56, v56, s43, -v225\n"
"v_mfma_f32_16x16x32_bf16 a[196:199], a[88:91], v[160:163], a[196:199]\n"
"v_exp_f32 v56, v56\n"
"v_mfma_f32_16x16x32_bf16 a[196:199], a[92:95], v[164:167], a[196:199]\n"
"ds_write_b128 v214, v[40:43] offset:53248\n"
"ds_write_b32 v216, v232 offset:1024\n"
"v_mfma_f32_16x16x32_bf16 v[248:251], v[208:211], v[40:43], 0\n"
"v_mfma_f32_16x16x32_bf16 a[200:203], v[96:99], v[160:163], a[200:203]\n"
"v_fma_f32 v57, v57, s43, -v225\n"
"v_fma_f32 v58, v58, s43, -v225\n"
"v_mfma_f32_16x16x32_bf16 a[200:203], v[100:103], v[164:167], a[200:203]\n"
"v_exp_f32 v57, v57\n"
"s_nop 0\n"
"v_cvt_pk_bf16_f32 v56, v56, v57\n"
"v_mfma_f32_16x16x32_bf16 a[204:207], v[104:107], v[160:163], a[204:207]\n"
"s_waitcnt vmcnt(14)\n"
"s_add_u32 m0, s40, 0x4200\n"
"ds_read_b128 v[160:163], v195 offset:2048\n"
"ds_read_b64_tr_b16 v[178:179], v196 offset:0\n"
"v_mfma_f32_16x16x32_bf16 a[204:207], v[108:111], v[164:167], a[204:207]\n"
"s_waitcnt lgkmcnt(5)\n"
"ds_read_b128 v[164:167], v195 offset:3072\n"
"ds_read_b64_tr_b16 v[182:183], v196 offset:8\n"
"v_exp_f32 v58, v58\n"
"v_mfma_f32_16x16x32_bf16 a[208:211], a[80:83], v[168:171], a[208:211]\n"
"s_waitcnt vmcnt(12) lgkmcnt(6)\n"
"ds_read_b64_tr_b16 a[2:3], v197 offset:0\n"
"ds_read_b64_tr_b16 a[54:55], v192 offset:14784\n"
"v_mfma_f32_16x16x32_bf16 a[208:211], a[84:87], v[172:175], a[208:211]\n"
"s_mov_b32 s42, 0x23000\n"
"buffer_load_dwordx4 v204, s[32:35], s42 offen lds\n"
"v_fma_f32 v59, v59, s43, -v225\n"
"v_fma_f32 v60, v60, s43, -v225\n"
"v_mfma_f32_16x16x32_bf16 a[212:215], a[88:91], v[168:171], a[212:215]\n"
"ds_read_b64_tr_b16 a[6:7], v197 offset:8\n"
"ds_read_b64_tr_b16 a[62:63], v192 offset:14816\n"
"v_mfma_f32_16x16x32_bf16 a[212:215], a[92:95], v[172:175], a[212:215]\n"
"ds_read_b64_tr_b16 a[70:71], v193 offset:14784\n"
"v_exp_f32 v59, v59\n"
"s_nop 0\n"
"v_cvt_pk_bf16_f32 v57, v58, v59\n"
"v_mfma_f32_16x16x32_bf16 a[216:219], v[96:99], v[168:171], a[216:219]\n"
"ds_read_b64_tr_b16 a[78:79], v193 offset:14816\n"
"v_add_f32 v234, v234, v248\n"
"v_mfma_f32_16x16x32_bf16 a[216:219], v[100:103], v[172:175], a[216:219]\n"
"buffer_load_dwordx4 v205, s[32:35], s42 offen offset:1056 lds\n"
"v_exp_f32 v60, v60\n"
"v_mfma_f32_16x16x32_bf16 a[220:223], v[104:107], v[168:171], a[220:223]\n"
"ds_read_b128 v[168:171], v195 offset:10240\n"
"v_fma_f32 v61, v61, s43, -v225\n"
"v_mfma_f32_16x16x32_bf16 a[220:223], v[108:111], v[172:175], a[220:223]\n"
"s_waitcnt lgkmcnt(10)\n"
"ds_read_b128 v[172:175], v195 offset:11264\n"
"v_exp_f32 v61, v61\n"
"v_mfma_f32_16x16x32_bf16 a[224:227], a[80:83], v[160:163], a[224:227]\n"
"s_waitcnt lgkmcnt(9)\n"
"v_fma_f32 v62, v62, s43, -v225\n"
"v_cvt_pk_bf16_f32 v58, v60, v61\n"
"v_mfma_f32_16x16x32_bf16 a[224:227], a[84:87], v[164:167], a[224:227]\n"
"v_exp_f32 v62, v62\n"
"v_fma_f32 v63, v63, s43, -v225\n"
"v_fma_f32 v72, v72, s43, -v226\n"
"v_mfma_f32_16x16x32_bf16 a[228:231], a[88:91], v[160:163], a[228:231]\n"
"v_exp_f32 v63, v63\n"
"v_mfma_f32_16x16x32_bf16 a[228:231], a[92:95], v[164:167], a[228:231]\n"
"v_cvt_pk_bf16_f32 v59, v62, v63\n"
"v_exp_f32 v72, v72\n"
"v_fma_f32 v73, v73, s43, -v226\n"
"v_mfma_f32_16x16x32_bf16 v[248:251], v[208:211], v[56:59], 0\n"
"v_exp_f32 v73, v73\n"
"v_mfma_f32_16x16x32_bf16 a[232:235], v[96:99], v[160:163], a[232:235]\n"
"v_fma_f32 v74, v74, s43, -v226\n"
"v_cvt_pk_bf16_f32 v72, v72, v73\n"
"v_mfma_f32_16x16x32_bf16 a[232:235], v[100:103], v[164:167], a[232:235]\n"
"ds_write_b128 v214, v[56:59] offset:61440\n"
"v_mfma_f32_16x16x32_bf16 a[236:239], v[104:107], v[160:163], a[236:239]\n"
"s_waitcnt lgkmcnt(1)\n"
"s_add_u32 m0, s40, 0x39c0\n"
"s_barrier\n"
"ds_read_b128 v[160:163], v194 offset:4096\n"
"v_mfma_f32_16x16x32_bf16 a[236:239], v[108:111], v[164:167], a[236:239]\n"
"ds_read_b128 v[164:167], v194 offset:5120\n"
"v_fma_f32 v75, v75, s43, -v226\n"
"v_mfma_f32_16x16x32_bf16 a[240:243], a[80:83], v[168:171], a[240:243]\n"
"v_exp_f32 v74, v74\n"
"v_mfma_f32_16x16x32_bf16 a[240:243], a[84:87], v[172:175], a[240:243]\n"
"s_mov_b32 s42, 0x26800\n"
"buffer_load_dwordx4 v204, s[32:35], s42 offen lds\n"
"v_exp_f32 v75, v75\n"
"v_mfma_f32_16x16x32_bf16 a[244:247], a[88:91], v[168:171], a[244:247]\n"
"v_fma_f32 v76, v76, s43, -v226\n"
"v_cvt_pk_bf16_f32 v73, v74, v75\n"
"v_mfma_f32_16x16x32_bf16 a[244:247], a[92:95], v[172:175], a[244:247]\n"
"v_exp_f32 v76, v76\n"
"v_add_f32 v235, v235, v248\n"
"v_fma_f32 v77, v77, s43, -v226\n"
"v_mfma_f32_16x16x32_bf16 a[248:251], v[96:99], v[168:171], a[248:251]\n"
"v_exp_f32 v77, v77\n"
"v_mfma_f32_16x16x32_bf16 a[248:251], v[100:103], v[172:175], a[248:251]\n"
"buffer_load_dwordx4 v205, s[32:35], s42 offen offset:1056 lds\n"
"v_fma_f32 v78, v78, s43, -v226\n"
"v_cvt_pk_bf16_f32 v74, v76, v77\n"
"v_mfma_f32_16x16x32_bf16 a[252:255], v[104:107], v[168:171], a[252:255]\n"
"ds_read_b128 v[168:171], v194 offset:12288\n"
"v_fma_f32 v79, v79, s43, -v226\n"
"v_mfma_f32_16x16x32_bf16 a[252:255], v[108:111], v[172:175], a[252:255]\n"
"s_waitcnt lgkmcnt(2)\n"
"ds_read_b128 v[172:175], v194 offset:13312\n"
"v_exp_f32 v78, v78\n"
"v_mfma_f32_16x16x32_bf16 a[96:99], a[48:51], v[160:163], a[96:99]\n"
"s_waitcnt lgkmcnt(2)\n"
"v_exp_f32 v79, v79\n"
"v_mfma_f32_16x16x32_bf16 a[96:99], a[52:55], v[164:167], a[96:99]\n"
"v_cvt_pk_bf16_f32 v75, v78, v79\n"
"v_permlane32_swap_b32 v234, v235\n"
"v_fma_f32 v88, v88, s43, -v227\n"
"v_mfma_f32_16x16x32_bf16 v[248:251], v[208:211], v[72:75], 0\n"
"v_exp_f32 v88, v88\n"
"v_mfma_f32_16x16x32_bf16 a[100:103], a[56:59], v[160:163], a[100:103]\n"
"s_waitcnt vmcnt(14)\n"
"ds_read_b64_tr_b16 a[80:81], v192 offset:2112\n"
"v_fma_f32 v89, v89, s43, -v227\n"
"v_mfma_f32_16x16x32_bf16 a[100:103], a[60:63], v[164:167], a[100:103]\n"
"ds_read_b64_tr_b16 a[88:89], v192 offset:2144\n"
"v_exp_f32 v89, v89\n"
"v_mfma_f32_16x16x32_bf16 a[104:107], a[64:67], v[160:163], a[104:107]\n"
"ds_read_b64_tr_b16 v[96:97], v193 offset:2112\n"
"v_fma_f32 v90, v90, s43, -v227\n"
"v_mfma_f32_16x16x32_bf16 a[104:107], a[68:71], v[164:167], a[104:107]\n"
"ds_write_b128 v215, v[72:75] offset:4096\n"
"v_mfma_f32_16x16x32_bf16 a[108:111], a[72:75], v[160:163], a[108:111]\n"
"s_add_u32 m0, s40, 0\n"
"ds_read_b128 v[160:163], v194 offset:20480\n"
"ds_read_b64_tr_b16 v[104:105], v193 offset:2144\n"
"v_mfma_f32_16x16x32_bf16 a[108:111], a[76:79], v[164:167], a[108:111]\n"
"s_waitcnt lgkmcnt(7)\n"
"ds_read_b128 v[164:167], v194 offset:21504\n"
"ds_write_b32 v216, v234 offset:1536\n"
"v_mfma_f32_16x16x32_bf16 a[112:115], a[48:51], v[168:171], a[112:115]\n"
"s_waitcnt lgkmcnt(8)\n"
"v_exp_f32 v90, v90\n"
"v_mfma_f32_16x16x32_bf16 a[112:115], a[52:55], v[172:175], a[112:115]\n"
"s_mov_b32 s42, 0x2a000\n"
"buffer_load_dwordx4 v204, s[32:35], s42 offen lds\n"
"v_cvt_pk_bf16_f32 v88, v88, v89\n"
"v_fma_f32 v91, v91, s43, -v227\n"
"v_mfma_f32_16x16x32_bf16 a[116:119], a[56:59], v[168:171], a[116:119]\n"
"v_exp_f32 v91, v91\n"
"v_mfma_f32_16x16x32_bf16 a[116:119], a[60:63], v[172:175], a[116:119]\n"
"v_fma_f32 v92, v92, s43, -v227\n"
"v_exp_f32 v92, v92\n"
"v_cvt_pk_bf16_f32 v89, v90, v91\n"
"v_mfma_f32_16x16x32_bf16 a[120:123], a[64:67], v[168:171], a[120:123]\n"
"v_add_f32 v236, v236, v248\n"
"v_fma_f32 v93, v93, s43, -v227\n"
"v_mfma_f32_16x16x32_bf16 a[120:123], a[68:71], v[172:175], a[120:123]\n"
"buffer_load_dwordx4 v205, s[32:35], s42 offen offset:1056 lds\n"
"v_fma_f32 v94, v94, s43, -v227\n"
"v_mfma_f32_16x16x32_bf16 a[124:127], a[72:75], v[168:171], a[124:127]\n"
"s_waitcnt vmcnt(14)\n"
"ds_read_b128 v[168:171], v194 offset:28672\n"
"ds_read_b64_tr_b16 a[16:17], v196 offset:8448\n"
"v_mfma_f32_16x16x32_bf16 a[124:127], a[76:79], v[172:175], a[124:127]\n"
"s_waitcnt lgkmcnt(5)\n"
"ds_read_b128 v[172:175], v194 offset:29696\n"
"ds_read_b64_tr_b16 a[20:21], v196 offset:8456\n"
"v_exp_f32 v93, v93\n"
"v_mfma_f32_16x16x32_bf16 a[128:131], a[48:51], v[160:163], a[128:131]\n"
"s_waitcnt vmcnt(6) lgkmcnt(5)\n"
"ds_read_b64_tr_b16 a[32:33], v197 offset:8448\n"
"ds_read_b64_tr_b16 a[18:19], v196 offset:6336\n"
"v_mfma_f32_16x16x32_bf16 a[128:131], a[52:55], v[164:167], a[128:131]\n"
"v_exp_f32 v94, v94\n"
"v_cvt_pk_bf16_f32 v90, v92, v93\n"
"v_fma_f32 v95, v95, s43, -v227\n"
"v_mfma_f32_16x16x32_bf16 a[132:135], a[56:59], v[160:163], a[132:135]\n"
"ds_read_b64_tr_b16 a[36:37], v197 offset:8456\n"
"ds_read_b64_tr_b16 a[22:23], v196 offset:6344\n"
"v_mfma_f32_16x16x32_bf16 a[132:135], a[60:63], v[164:167], a[132:135]\n"
"ds_read_b64_tr_b16 a[34:35], v197 offset:6336\n"
"v_exp_f32 v95, v95\n"
"s_nop 0\n"
"v_cvt_pk_bf16_f32 v91, v94, v95\n"
"v_mfma_f32_16x16x32_bf16 a[136:139], a[64:67], v[160:163], a[136:139]\n"
"ds_read_b64_tr_b16 a[82:83], v192 offset:12672\n"
"ds_read_b64_tr_b16 a[38:39], v197 offset:6344\n"
"v_mfma_f32_16x16x32_bf16 a[136:139], a[68:71], v[164:167], a[136:139]\n"
"ds_write_b128 v215, v[88:91] offset:12288\n"
"v_mfma_f32_16x16x32_bf16 a[140:143], a[72:75], v[160:163], a[140:143]\n"
"s_add_u32 m0, s40, 0x840\n"
"ds_read_b128 v[160:163], v194 offset:36864\n"
"ds_read_b64_tr_b16 a[90:91], v192 offset:12704\n"
"v_mfma_f32_16x16x32_bf16 a[140:143], a[76:79], v[164:167], a[140:143]\n"
"s_waitcnt lgkmcnt(13)\n"
"ds_read_b128 v[164:167], v194 offset:37888\n"
"ds_read_b64_tr_b16 v[98:99], v193 offset:12672\n"
"v_mfma_f32_16x16x32_bf16 a[144:147], a[48:51], v[168:171], a[144:147]\n"
"s_waitcnt lgkmcnt(13)\n"
"ds_read_b64_tr_b16 v[106:107], v193 offset:12704\n"
"v_mfma_f32_16x16x32_bf16 a[144:147], a[52:55], v[172:175], a[144:147]\n"
"s_mov_b32 s42, 0x2d800\n"
"buffer_load_dwordx4 v204, s[32:35], s42 offen lds\n"
"v_mfma_f32_16x16x32_bf16 v[248:251], v[208:211], v[88:91], 0\n"
"v_mfma_f32_16x16x32_bf16 a[148:151], a[56:59], v[168:171], a[148:151]\n"
"v_mfma_f32_16x16x32_bf16 a[148:151], a[60:63], v[172:175], a[148:151]\n"
"v_mfma_f32_16x16x32_bf16 a[152:155], a[64:67], v[168:171], a[152:155]\n"
"v_mfma_f32_16x16x32_bf16 a[152:155], a[68:71], v[172:175], a[152:155]\n"
"buffer_load_dwordx4 v205, s[32:35], s42 offen offset:1056 lds\n"
"v_mfma_f32_16x16x32_bf16 a[156:159], a[72:75], v[168:171], a[156:159]\n"
"s_waitcnt vmcnt(6) lgkmcnt(5)\n"
"s_barrier\n"
"ds_read_b128 v[168:171], v194 offset:45056\n"
"ds_read_b64_tr_b16 v[0:1], v196 offset:16896\n"
"v_mfma_f32_16x16x32_bf16 a[156:159], a[76:79], v[172:175], a[156:159]\n"
"s_waitcnt lgkmcnt(6)\n"
"ds_read_b128 v[172:175], v194 offset:46080\n"
"ds_read_b64_tr_b16 v[4:5], v196 offset:16904\n"
"v_mfma_f32_16x16x32_bf16 a[160:163], a[48:51], v[160:163], a[160:163]\n"
"s_waitcnt lgkmcnt(6)\n"
"ds_read_b64_tr_b16 v[16:17], v197 offset:16896\n"
"ds_read_b128 v[252:255], v217 offset:0\n"
"v_mfma_f32_16x16x32_bf16 a[160:163], a[52:55], v[164:167], a[160:163]\n"
"v_mfma_f32_16x16x32_bf16 a[164:167], a[56:59], v[160:163], a[164:167]\n"
"ds_read_b64_tr_b16 v[20:21], v197 offset:16904\n"
"ds_read_b128 v[200:203], v217 offset:256\n"
"v_mfma_f32_16x16x32_bf16 a[164:167], a[60:63], v[164:167], a[164:167]\n"
"v_mfma_f32_16x16x32_bf16 a[168:171], a[64:67], v[160:163], a[168:171]\n"
"v_add_f32 v237, v237, v248\n"
"v_mfma_f32_16x16x32_bf16 a[168:171], a[68:71], v[164:167], a[168:171]\n"
"s_nop 0\n"
"v_permlane32_swap_b32 v236, v237\n"
"v_mfma_f32_16x16x32_bf16 a[172:175], a[72:75], v[160:163], a[172:175]\n"
"s_add_u32 m0, s40, 0x3180\n"
"ds_read_b128 v[160:163], v194 offset:53248\n"
"ds_read_b64_tr_b16 a[84:85], v192 offset:4224\n"
"v_mfma_f32_16x16x32_bf16 a[172:175], a[76:79], v[164:167], a[172:175]\n"
"s_waitcnt lgkmcnt(9)\n"
"ds_read_b128 v[164:167], v194 offset:54272\n"
"ds_read_b64_tr_b16 a[92:93], v192 offset:4256\n"
"v_mfma_f32_16x16x32_bf16 a[176:179], a[48:51], v[168:171], a[176:179]\n"
"s_waitcnt lgkmcnt(6)\n"
"ds_read_b64_tr_b16 v[100:101], v193 offset:4224\n"
"v_add_f32 v252, v252, v253\n"
"v_mfma_f32_16x16x32_bf16 a[176:179], a[52:55], v[172:175], a[176:179]\n"
"s_mov_b32 s42, 0x31000\n"
"buffer_load_dwordx4 v204, s[32:35], s42 offen lds\n"
"v_add_f32 v254, v254, v255\n"
"v_mfma_f32_16x16x32_bf16 a[180:183], a[56:59], v[168:171], a[180:183]\n"
"ds_read_b64_tr_b16 v[108:109], v193 offset:4256\n"
"v_add_f32 v252, v252, v254\n"
"v_mfma_f32_16x16x32_bf16 a[180:183], a[60:63], v[172:175], a[180:183]\n"
"s_waitcnt lgkmcnt(6)\n"
"v_rcp_f32 v228, v252\n"
"v_add_f32 v200, v200, v201\n"
"v_add_f32 v202, v202, v203\n"
"v_mfma_f32_16x16x32_bf16 a[184:187], a[64:67], v[168:171], a[184:187]\n"
".Lpad8_%=:\n"
"s_cbranch_vccnz .Lpad12_%=\n"
"v_add_f32 v200, v200, v202\n"
"v_rcp_f32 v229, v200\n"
"v_mfma_f32_16x16x32_bf16 a[184:187], a[68:71], v[172:175], a[184:187]\n"
"buffer_load_dwordx4 v205, s[32:35], s42 offen offset:1056 lds\n"
"v_mfma_f32_16x16x32_bf16 a[188:191], a[72:75], v[168:171], a[188:191]\n"
"s_waitcnt vmcnt(6)\n"
"ds_read_b128 v[168:171], v194 offset:61440\n"
"ds_read_b64_tr_b16 v[2:3], v196 offset:14784\n"
"v_mfma_f32_16x16x32_bf16 a[188:191], a[76:79], v[172:175], a[188:191]\n"
"s_waitcnt lgkmcnt(7)\n"
"ds_read_b128 v[172:175], v194 offset:62464\n"
"ds_read_b64_tr_b16 v[6:7], v196 offset:14792\n"
"v_mfma_f32_16x16x32_bf16 a[192:195], a[48:51], v[160:163], a[192:195]\n"
"s_waitcnt lgkmcnt(7)\n"
"ds_read_b64_tr_b16 v[18:19], v197 offset:14784\n"
"ds_read_b128 v[252:255], v217 offset:512\n"
"v_mfma_f32_16x16x32_bf16 a[192:195], a[52:55], v[164:167], a[192:195]\n"
"v_mfma_f32_16x16x32_bf16 a[196:199], a[56:59], v[160:163], a[196:199]\n"
"ds_read_b64_tr_b16 v[22:23], v197 offset:14792\n"
"ds_read_b128 v[200:203], v217 offset:768\n"
"v_mfma_f32_16x16x32_bf16 a[196:199], a[60:63], v[164:167], a[196:199]\n"
"v_mfma_f32_16x16x32_bf16 a[200:203], a[64:67], v[160:163], a[200:203]\n"
"v_mfma_f32_16x16x32_bf16 a[200:203], a[68:71], v[164:167], a[200:203]\n"
"v_mfma_f32_16x16x32_bf16 a[204:207], a[72:75], v[160:163], a[204:207]\n"
"s_add_u32 m0, s40, 0x1080\n"
"ds_read_b128 v[160:163], v195 offset:4096\n"
"v_mfma_f32_16x16x32_bf16 a[204:207], a[76:79], v[164:167], a[204:207]\n"
"s_waitcnt lgkmcnt(8)\n"
"ds_read_b128 v[164:167], v195 offset:5120\n"
"v_mfma_f32_16x16x32_bf16 a[208:211], a[48:51], v[168:171], a[208:211]\n"
"s_waitcnt lgkmcnt(4)\n"
"ds_read_b64_tr_b16 a[86:87], v192 offset:10560\n"
"v_add_f32 v252, v252, v253\n"
"v_mfma_f32_16x16x32_bf16 a[208:211], a[52:55], v[172:175], a[208:211]\n"
"s_waitcnt lgkmcnt(3)\n"
"s_mov_b32 s42, 0x34800\n"
"buffer_load_dwordx4 v204, s[32:35], s42 offen lds\n"
"v_add_f32 v254, v254, v255\n"
"v_add_f32 v252, v252, v254\n"
"v_add_f32 v200, v200, v201\n"
"v_mfma_f32_16x16x32_bf16 a[212:215], a[56:59], v[168:171], a[212:215]\n"
"ds_read_b64_tr_b16 a[94:95], v192 offset:10592\n"
"v_add_f32 v202, v202, v203\n"
"v_mfma_f32_16x16x32_bf16 a[212:215], a[60:63], v[172:175], a[212:215]\n"
"ds_read_b64_tr_b16 v[102:103], v193 offset:10560\n"
"v_rcp_f32 v230, v252\n"
"v_add_f32 v200, v200, v202\n"
"v_rcp_f32 v231, v200\n"
"v_mfma_f32_16x16x32_bf16 a[216:219], a[64:67], v[168:171], a[216:219]\n"
"ds_read_b64_tr_b16 v[110:111], v193 offset:10592\n"
"ds_write_b32 v216, v236 offset:2048\n"
"v_mfma_f32_16x16x32_bf16 a[216:219], a[68:71], v[172:175], a[216:219]\n"
"buffer_load_dwordx4 v205, s[32:35], s42 offen offset:1056 lds\n"
"v_mfma_f32_16x16x32_bf16 a[220:223], a[72:75], v[168:171], a[220:223]\n"
"s_waitcnt vmcnt(6)\n"
"ds_read_b128 v[168:171], v195 offset:12288\n"
"ds_read_b64_tr_b16 v[32:33], v196 offset:0\n"
"v_mfma_f32_16x16x32_bf16 a[220:223], a[76:79], v[172:175], a[220:223]\n"
"s_waitcnt lgkmcnt(8)\n"
"ds_read_b128 v[172:175], v195 offset:13312\n"
"ds_read_b64_tr_b16 v[36:37], v196 offset:8\n"
"v_mfma_f32_16x16x32_bf16 a[224:227], a[48:51], v[160:163], a[224:227]\n"
"s_waitcnt lgkmcnt(9)\n"
"ds_read_b64_tr_b16 v[48:49], v197 offset:0\n"
"ds_read_b128 v[252:255], v217 offset:1024\n"
"v_mfma_f32_16x16x32_bf16 a[224:227], a[52:55], v[164:167], a[224:227]\n"
"v_mfma_f32_16x16x32_bf16 a[228:231], a[56:59], v[160:163], a[228:231]\n"
"ds_read_b64_tr_b16 v[52:53], v197 offset:8\n"
"ds_read_b128 v[200:203], v217 offset:1280\n"
"v_mfma_f32_16x16x32_bf16 a[228:231], a[60:63], v[164:167], a[228:231]\n"
"v_mfma_f32_16x16x32_bf16 a[232:235], a[64:67], v[160:163], a[232:235]\n"
"v_mfma_f32_16x16x32_bf16 a[232:235], a[68:71], v[164:167], a[232:235]\n"
"v_mfma_f32_16x16x32_bf16 a[236:239], a[72:75], v[160:163], a[236:239]\n"
"ds_read_b128 v[160:163], v194 offset:6144\n"
"v_mfma_f32_16x16x32_bf16 a[236:239], a[76:79], v[164:167], a[236:239]\n"
"s_waitcnt lgkmcnt(8)\n"
"ds_read_b128 v[164:167], v194 offset:7168\n"
"v_mfma_f32_16x16x32_bf16 a[240:243], a[48:51], v[168:171], a[240:243]\n"
"s_waitcnt lgkmcnt(4)\n"
"v_add_f32 v252, v252, v253\n"
"v_mfma_f32_16x16x32_bf16 a[240:243], a[52:55], v[172:175], a[240:243]\n"
"v_add_f32 v254, v254, v255\n"
"v_add_f32 v252, v252, v254\n"
"v_mfma_f32_16x16x32_bf16 a[244:247], a[56:59], v[168:171], a[244:247]\n"
"v_rcp_f32 v232, v252\n"
"v_mfma_f32_16x16x32_bf16 a[244:247], a[60:63], v[172:175], a[244:247]\n"
"s_waitcnt lgkmcnt(2)\n"
"v_add_f32 v200, v200, v201\n"
"v_add_f32 v202, v202, v203\n"
"v_mfma_f32_16x16x32_bf16 a[248:251], a[64:67], v[168:171], a[248:251]\n"
"s_add_u32 m0, s40, 0\n"
"v_add_f32 v200, v200, v202\n"
"v_rcp_f32 v233, v200\n"
"v_mfma_f32_16x16x32_bf16 a[248:251], a[68:71], v[172:175], a[248:251]\n"
"s_mov_b32 s42, 0\n"
"buffer_load_dwordx4 v206, s[32:35], s42 offen lds\n"
"v_mfma_f32_16x16x32_bf16 a[252:255], a[72:75], v[168:171], a[252:255]\n"
"s_waitcnt vmcnt(5)\n"
"ds_read_b128 v[168:171], v194 offset:14336\n"
"ds_read_b64_tr_b16 v[34:35], v196 offset:2112\n"
"v_mfma_f32_16x16x32_bf16 a[252:255], a[76:79], v[172:175], a[252:255]\n"
"s_waitcnt lgkmcnt(3)\n"
"ds_read_b128 v[172:175], v194 offset:15360\n"
"ds_read_b64_tr_b16 v[38:39], v196 offset:2120\n"
"v_mfma_f32_16x16x32_bf16 a[96:99], a[80:83], v[160:163], a[96:99]\n"
"s_waitcnt lgkmcnt(4)\n"
"ds_read_b64_tr_b16 v[50:51], v197 offset:2112\n"
"ds_read_b128 v[252:255], v217 offset:1536\n"
"v_mfma_f32_16x16x32_bf16 a[96:99], a[84:87], v[164:167], a[96:99]\n"
"buffer_load_dwordx4 v207, s[32:35], s42 offen offset:1056 lds\n"
"v_mfma_f32_16x16x32_bf16 a[100:103], a[88:91], v[160:163], a[100:103]\n"
"ds_read_b64_tr_b16 v[54:55], v197 offset:2120\n"
"ds_read_b128 v[200:203], v217 offset:1792\n"
"v_mfma_f32_16x16x32_bf16 a[100:103], a[92:95], v[164:167], a[100:103]\n"
"v_mfma_f32_16x16x32_bf16 a[104:107], v[96:99], v[160:163], a[104:107]\n"
"v_mfma_f32_16x16x32_bf16 a[104:107], v[100:103], v[164:167], a[104:107]\n"
"v_mfma_f32_16x16x32_bf16 a[108:111], v[104:107], v[160:163], a[108:111]\n"
"s_add_u32 m0, s40, 0x2940\n"
"ds_read_b128 v[160:163], v194 offset:22528\n"
"v_mfma_f32_16x16x32_bf16 a[108:111], v[108:111], v[164:167], a[108:111]\n"
"s_waitcnt lgkmcnt(8)\n"
"ds_read_b128 v[164:167], v194 offset:23552\n"
"v_mfma_f32_16x16x32_bf16 a[112:115], a[80:83], v[168:171], a[112:115]\n"
"s_waitcnt lgkmcnt(4)\n"
"v_add_f32 v252, v252, v253\n"
"v_add_f32 v254, v254, v255\n"
"v_mfma_f32_16x16x32_bf16 a[112:115], a[84:87], v[172:175], a[112:115]\n"
"s_mov_b32 s42, 0x3800\n"
"buffer_load_dwordx4 v206, s[32:35], s42 offen lds\n"
"v_mfma_f32_16x16x32_bf16 a[116:119], a[88:91], v[168:171], a[116:119]\n"
"s_waitcnt lgkmcnt(2)\n"
"v_add_f32 v252, v252, v254\n"
"v_add_f32 v200, v200, v201\n"
"v_mfma_f32_16x16x32_bf16 a[116:119], a[92:95], v[172:175], a[116:119]\n"
"v_rcp_f32 v234, v252\n"
"v_add_f32 v202, v202, v203\n"
"v_mfma_f32_16x16x32_bf16 a[120:123], v[96:99], v[168:171], a[120:123]\n"
"v_add_f32 v200, v200, v202\n"
"v_rcp_f32 v235, v200\n"
"v_mfma_f32_16x16x32_bf16 a[120:123], v[100:103], v[172:175], a[120:123]\n"
"buffer_load_dwordx4 v207, s[32:35], s42 offen offset:1056 lds\n"
"v_mfma_f32_16x16x32_bf16 a[124:127], v[104:107], v[168:171], a[124:127]\n"
"s_waitcnt vmcnt(6)\n"
"ds_read_b128 v[168:171], v194 offset:30720\n"
"ds_read_b64_tr_b16 v[64:65], v196 offset:12672\n"
"v_mfma_f32_16x16x32_bf16 a[124:127], v[108:111], v[172:175], a[124:127]\n"
"s_waitcnt lgkmcnt(3)\n"
"ds_read_b128 v[172:175], v194 offset:31744\n"
"ds_read_b64_tr_b16 v[68:69], v196 offset:12680\n"
"v_mfma_f32_16x16x32_bf16 a[128:131], a[80:83], v[160:163], a[128:131]\n"
"s_waitcnt lgkmcnt(4)\n"
"ds_read_b64_tr_b16 v[80:81], v197 offset:12672\n"
"v_mfma_f32_16x16x32_bf16 a[128:131], a[84:87], v[164:167], a[128:131]\n"
"v_mfma_f32_16x16x32_bf16 a[132:135], a[88:91], v[160:163], a[132:135]\n"
"s_barrier\n"
"ds_read_b64_tr_b16 v[84:85], v197 offset:12680\n"
"v_mfma_f32_16x16x32_bf16 a[132:135], a[92:95], v[164:167], a[132:135]\n"
"ds_read_b128 v[252:255], v217 offset:2048\n"
"ds_read_b128 v[200:203], v217 offset:2304\n"
"v_accvgpr_read_b32 v240, a96\n"
"v_accvgpr_read_b32 v241, a97\n"
"v_mfma_f32_16x16x32_bf16 a[136:139], v[96:99], v[160:163], a[136:139]\n"
"s_add_u32 m0, s40, 0x840\n"
"v_mul_f32 v240, v240, v228\n"
"v_mul_f32 v241, v241, v228\n"
"v_mfma_f32_16x16x32_bf16 a[136:139], v[100:103], v[164:167], a[136:139]\n"
"s_mov_b32 s42, 0x7000\n"
"buffer_load_dwordx4 v206, s[32:35], s42 offen lds\n"
"v_cvt_pk_bf16_f32 v244, v240, v241\n"
"v_accvgpr_read_b32 v242, a98\n"
"v_accvgpr_read_b32 v243, a99\n"
"v_mfma_f32_16x16x32_bf16 a[140:143], v[104:107], v[160:163], a[140:143]\n"
"ds_read_b128 v[160:163], v194 offset:38912\n"
"v_mul_f32 v242, v242, v228\n"
"v_mfma_f32_16x16x32_bf16 a[140:143], v[108:111], v[164:167], a[140:143]\n"
"s_waitcnt lgkmcnt(8)\n"
"ds_read_b128 v[164:167], v194 offset:39936\n"
"v_mul_f32 v243, v243, v228\n"
"v_cvt_pk_bf16_f32 v245, v242, v243\n"
"v_accvgpr_read_b32 v240, a100\n"
"v_mfma_f32_16x16x32_bf16 a[144:147], a[80:83], v[168:171], a[144:147]\n"
"s_waitcnt lgkmcnt(7)\n"
"v_accvgpr_read_b32 v241, a101\n"
"v_mul_f32 v240, v240, v228\n"
"v_mfma_f32_16x16x32_bf16 a[144:147], a[84:87], v[172:175], a[144:147]\n"
"buffer_load_dwordx4 v207, s[32:35], s42 offen offset:1056 lds\n"
"v_mul_f32 v241, v241, v228\n"
"v_cvt_pk_bf16_f32 v246, v240, v241\n"
"v_accvgpr_read_b32 v242, a102\n"
"v_mfma_f32_16x16x32_bf16 a[148:151], a[88:91], v[168:171], a[148:151]\n"
"s_waitcnt lgkmcnt(3)\n"
"v_add_f32 v252, v252, v253\n"
"v_add_f32 v254, v254, v255\n"
"v_mfma_f32_16x16x32_bf16 a[148:151], a[92:95], v[172:175], a[148:151]\n"
"s_waitcnt lgkmcnt(2)\n"
"v_add_f32 v252, v252, v254\n"
"v_add_f32 v200, v200, v201\n"
"v_accvgpr_read_b32 v243, a103\n"
"v_mul_f32 v242, v242, v228\n"
"v_mfma_f32_16x16x32_bf16 a[152:155], v[96:99], v[168:171], a[152:155]\n"
"v_rcp_f32 v236, v252\n"
"v_mfma_f32_16x16x32_bf16 a[152:155], v[100:103], v[172:175], a[152:155]\n"
"v_add_f32 v202, v202, v203\n"
"v_add_f32 v200, v200, v202\n"
"v_rcp_f32 v237, v200\n"
"v_mul_f32 v243, v243, v228\n"
"v_mfma_f32_16x16x32_bf16 a[156:159], v[104:107], v[168:171], a[156:159]\n"
"s_waitcnt vmcnt(6)\n"
"s_add_u32 m0, s40, 0x3180\n"
"ds_read_b128 v[168:171], v194 offset:47104\n"
"ds_read_b64_tr_b16 v[66:67], v196 offset:4224\n"
"v_mfma_f32_16x16x32_bf16 a[156:159], v[108:111], v[172:175], a[156:159]\n"
"s_waitcnt lgkmcnt(3)\n"
"ds_read_b128 v[172:175], v194 offset:48128\n"
"ds_read_b64_tr_b16 v[70:71], v196 offset:4232\n"
"v_cvt_pk_bf16_f32 v247, v242, v243\n"
"v_accvgpr_read_b32 v240, a104\n"
"v_mfma_f32_16x16x32_bf16 a[160:163], a[80:83], v[160:163], a[160:163]\n"
"s_waitcnt lgkmcnt(4)\n"
"ds_read_b64_tr_b16 v[82:83], v197 offset:4224\n"
"v_accvgpr_read_b32 v241, a105\n"
"v_mfma_f32_16x16x32_bf16 a[160:163], a[84:87], v[164:167], a[160:163]\n"
"s_mov_b32 s42, 0xa800\n"
"buffer_load_dwordx4 v206, s[32:35], s42 offen lds\n"
"v_mul_f32 v240, v240, v228\n"
"v_mul_f32 v241, v241, v228\n"
"v_cvt_pk_bf16_f32 v248, v240, v241\n"
"v_mfma_f32_16x16x32_bf16 a[164:167], a[88:91], v[160:163], a[164:167]\n"
"ds_read_b64_tr_b16 v[86:87], v197 offset:4232\n"
"v_accvgpr_read_b32 v242, a106\n"
"v_mfma_f32_16x16x32_bf16 a[164:167], a[92:95], v[164:167], a[164:167]\n"
"ds_write_b128 v212, v[244:247] offset:0\n"
"v_mfma_f32_16x16x32_bf16 a[168:171], v[96:99], v[160:163], a[168:171]\n"
"v_accvgpr_read_b32 v243, a107\n"
"v_mul_f32 v242, v242, v228\n"
"v_mfma_f32_16x16x32_bf16 a[168:171], v[100:103], v[164:167], a[168:171]\n"
"buffer_load_dwordx4 v207, s[32:35], s42 offen offset:1056 lds\n"
"v_mul_f32 v243, v243, v228\n"
"v_cvt_pk_bf16_f32 v249, v242, v243\n"
"v_mfma_f32_16x16x32_bf16 a[172:175], v[104:107], v[160:163], a[172:175]\n"
"ds_read_b128 v[160:163], v194 offset:55296\n"
"v_accvgpr_read_b32 v240, a108\n"
"v_mfma_f32_16x16x32_bf16 a[172:175], v[108:111], v[164:167], a[172:175]\n"
"s_waitcnt lgkmcnt(7)\n"
"ds_read_b128 v[164:167], v194 offset:56320\n"
"v_accvgpr_read_b32 v241, a109\n"
"v_mul_f32 v240, v240, v228\n"
"v_mul_f32 v241, v241, v228\n"
"v_mfma_f32_16x16x32_bf16 a[176:179], a[80:83], v[168:171], a[176:179]\n"
"s_waitcnt lgkmcnt(6)\n"
"v_cvt_pk_bf16_f32 v250, v240, v241\n"
"v_accvgpr_read_b32 v242, a110\n"
"v_mfma_f32_16x16x32_bf16 a[176:179], a[84:87], v[172:175], a[176:179]\n"
"v_accvgpr_read_b32 v243, a111\n"
"v_mul_f32 v242, v242, v228\n"
"v_mul_f32 v243, v243, v228\n"
"v_cvt_pk_bf16_f32 v251, v242, v243\n"
"v_accvgpr_read_b32 v240, a112\n"
"v_mfma_f32_16x16x32_bf16 a[180:183], a[88:91], v[168:171], a[180:183]\n"
"v_accvgpr_read_b32 v241, a113\n"
"v_mul_f32 v240, v240, v229\n"
"v_mfma_f32_16x16x32_bf16 a[180:183], a[92:95], v[172:175], a[180:183]\n"
"ds_write_b128 v212, v[248:251] offset:1024\n"
"v_mfma_f32_16x16x32_bf16 a[184:187], v[96:99], v[168:171], a[184:187]\n"
"s_add_u32 m0, s40, 0x1080\n"
"v_mul_f32 v241, v241, v229\n"
"v_cvt_pk_bf16_f32 v244, v240, v241\n"
"v_mfma_f32_16x16x32_bf16 a[184:187], v[100:103], v[172:175], a[184:187]\n"
"s_waitcnt lgkmcnt(4)\n"
"s_mov_b32 s42, 0xe000\n"
"buffer_load_dwordx4 v206, s[32:35], s42 offen lds\n"
"v_accvgpr_read_b32 v242, a114\n"
"v_accvgpr_read_b32 v243, a115\n"
"v_mfma_f32_16x16x32_bf16 a[188:191], v[104:107], v[168:171], a[188:191]\n"
"ds_read_b128 v[168:171], v194 offset:63488\n"
"v_mul_f32 v242, v242, v229\n"
"v_mfma_f32_16x16x32_bf16 a[188:191], v[108:111], v[172:175], a[188:191]\n"
"s_waitcnt lgkmcnt(3)\n"
"ds_read_b128 v[172:175], v194 offset:64512\n"
"v_mul_f32 v243, v243, v229\n"
"v_cvt_pk_bf16_f32 v245, v242, v243\n"
"v_accvgpr_read_b32 v240, a116\n"
"v_mfma_f32_16x16x32_bf16 a[192:195], a[80:83], v[160:163], a[192:195]\n"
"s_waitcnt lgkmcnt(3)\n"
"v_accvgpr_read_b32 v241, a117\n"
"v_mul_f32 v240, v240, v229\n"
"v_mfma_f32_16x16x32_bf16 a[192:195], a[84:87], v[164:167], a[192:195]\n"
"buffer_load_dwordx4 v207, s[32:35], s42 offen offset:1056 lds\n"
"v_mul_f32 v241, v241, v229\n"
"v_cvt_pk_bf16_f32 v246, v240, v241\n"
"v_accvgpr_read_b32 v242, a118\n"
"v_mfma_f32_16x16x32_bf16 a[196:199], a[88:91], v[160:163], a[196:199]\n"
"s_waitcnt vmcnt(8)\n"
"ds_read_b64_tr_b16 a[8:9], v198 offset:0\n"
"v_accvgpr_read_b32 v243, a119\n"
"v_mfma_f32_16x16x32_bf16 a[196:199], a[92:95], v[164:167], a[196:199]\n"
"ds_read_b64_tr_b16 a[12:13], v198 offset:8\n"
"v_mul_f32 v242, v242, v229\n"
"v_mul_f32 v243, v243, v229\n"
"v_cvt_pk_bf16_f32 v247, v242, v243\n"
"v_mfma_f32_16x16x32_bf16 a[200:203], v[96:99], v[160:163], a[200:203]\n"
"ds_read_b64_tr_b16 a[24:25], v199 offset:0\n"
"v_accvgpr_read_b32 v240, a120\n"
"v_mfma_f32_16x16x32_bf16 a[200:203], v[100:103], v[164:167], a[200:203]\n"
"ds_write_b128 v212, v[244:247] offset:8192\n"
"v_mfma_f32_16x16x32_bf16 a[204:207], v[104:107], v[160:163], a[204:207]\n"
"s_add_u32 m0, s40, 0x39c0\n"
"ds_read_b128 v[160:163], v195 offset:6144\n"
"v_accvgpr_read_b32 v241, a121\n"
"v_mfma_f32_16x16x32_bf16 a[204:207], v[108:111], v[164:167], a[204:207]\n"
"s_waitcnt lgkmcnt(6)\n"
"ds_read_b128 v[164:167], v195 offset:7168\n"
"v_mul_f32 v240, v240, v229\n"
"v_mul_f32 v241, v241, v229\n"
"v_cvt_pk_bf16_f32 v248, v240, v241\n"
"v_mfma_f32_16x16x32_bf16 a[208:211], a[80:83], v[168:171], a[208:211]\n"
"s_waitcnt lgkmcnt(6)\n"
"v_accvgpr_read_b32 v242, a122\n"
"v_accvgpr_read_b32 v243, a123\n"
"v_mfma_f32_16x16x32_bf16 a[208:211], a[84:87], v[172:175], a[208:211]\n"
"s_mov_b32 s42, 0x11800\n"
"buffer_load_dwordx4 v206, s[32:35], s42 offen lds\n"
"v_mul_f32 v242, v242, v229\n"
"v_mul_f32 v243, v243, v229\n"
"v_cvt_pk_bf16_f32 v249, v242, v243\n"
"v_mfma_f32_16x16x32_bf16 a[212:215], a[88:91], v[168:171], a[212:215]\n"
"v_accvgpr_read_b32 v240, a124\n"
"v_accvgpr_read_b32 v241, a125\n"
"v_mfma_f32_16x16x32_bf16 a[212:215], a[92:95], v[172:175], a[212:215]\n"
"v_mul_f32 v240, v240, v229\n"
"v_mul_f32 v241, v241, v229\n"
"v_cvt_pk_bf16_f32 v250, v240, v241\n"
"v_accvgpr_read_b32 v242, a126\n"
"v_mfma_f32_16x16x32_bf16 a[216:219], v[96:99], v[168:171], a[216:219]\n"
"v_accvgpr_read_b32 v243, a127\n"
"v_mul_f32 v242, v242, v229\n"
"v_mfma_f32_16x16x32_bf16 a[216:219], v[100:103], v[172:175], a[216:219]\n"
"buffer_load_dwordx4 v207, s[32:35], s42 offen offset:1056 lds\n"
"v_mul_f32 v243, v243, v229\n"
"v_cvt_pk_bf16_f32 v251, v242, v243\n"
"v_mfma_f32_16x16x32_bf16 a[220:223], v[104:107], v[168:171], a[220:223]\n"
"s_waitcnt vmcnt(8)\n"
"ds_read_b128 v[168:171], v195 offset:14336\n"
"ds_read_b64_tr_b16 a[10:11], v198 offset:10560\n"
"v_mfma_f32_16x16x32_bf16 a[220:223], v[108:111], v[172:175], a[220:223]\n"
"s_waitcnt lgkmcnt(3)\n"
"ds_read_b128 v[172:175], v195 offset:15360\n"
"ds_read_b64_tr_b16 a[14:15], v198 offset:10568\n"
"ds_write_b128 v212, v[248:251] offset:9216\n"
"v_mfma_f32_16x16x32_bf16 a[224:227], a[80:83], v[160:163], a[224:227]\n"
"s_waitcnt lgkmcnt(5)\n"
"ds_read_b64_tr_b16 a[26:27], v199 offset:10560\n"
"v_mfma_f32_16x16x32_bf16 a[224:227], a[84:87], v[164:167], a[224:227]\n"
"v_mfma_f32_16x16x32_bf16 a[228:231], a[88:91], v[160:163], a[228:231]\n"
"v_mfma_f32_16x16x32_bf16 a[228:231], a[92:95], v[164:167], a[228:231]\n"
"v_mfma_f32_16x16x32_bf16 a[232:235], v[96:99], v[160:163], a[232:235]\n"
"s_add_u32 m0, s40, 0\n"
"v_mfma_f32_16x16x32_bf16 a[232:235], v[100:103], v[164:167], a[232:235]\n"
"s_mov_b32 s42, 0x15000\n"
"buffer_load_dwordx4 v206, s[32:35], s42 offen lds\n"
"v_mfma_f32_16x16x32_bf16 a[236:239], v[104:107], v[160:163], a[236:239]\n"
"s_waitcnt lgkmcnt(1)\n"
"s_barrier\n"
"ds_read_b128 v[160:163], v194 offset:0\n"
"v_mfma_f32_16x16x32_bf16 a[236:239], v[108:111], v[164:167], a[236:239]\n"
"ds_read_b128 v[164:167], v194 offset:1024\n"
"v_accvgpr_read_b32 v240, a128\n"
"v_accvgpr_read_b32 v241, a129\n"
"v_mfma_f32_16x16x32_bf16 a[240:243], a[80:83], v[168:171], a[240:243]\n"
"v_mul_f32 v240, v240, v230\n"
"v_mul_f32 v241, v241, v230\n"
"v_mfma_f32_16x16x32_bf16 a[240:243], a[84:87], v[172:175], a[240:243]\n"
"buffer_load_dwordx4 v207, s[32:35], s42 offen offset:1056 lds\n"
"v_cvt_pk_bf16_f32 v244, v240, v241\n"
"v_mfma_f32_16x16x32_bf16 a[244:247], a[88:91], v[168:171], a[244:247]\n"
"s_waitcnt vmcnt(8)\n"
"ds_read_b64_tr_b16 a[28:29], v198 offset:2112\n"
"v_accvgpr_read_b32 v242, a130\n"
"v_mfma_f32_16x16x32_bf16 a[244:247], a[92:95], v[172:175], a[244:247]\n"
"ds_read_b64_tr_b16 a[40:41], v198 offset:2120\n"
"v_accvgpr_read_b32 v243, a131\n"
"v_mul_f32 v242, v242, v230\n"
"v_mul_f32 v243, v243, v230\n"
"v_mfma_f32_16x16x32_bf16 a[248:251], v[96:99], v[168:171], a[248:251]\n"
"ds_read_b64_tr_b16 a[44:45], v199 offset:2112\n"
"v_cvt_pk_bf16_f32 v245, v242, v243\n"
"v_mfma_f32_16x16x32_bf16 a[248:251], v[100:103], v[172:175], a[248:251]\n"
"v_accvgpr_read_b32 v240, a132\n"
"v_accvgpr_read_b32 v241, a133\n"
"v_mul_f32 v240, v240, v230\n"
"v_mfma_f32_16x16x32_bf16 a[252:255], v[104:107], v[168:171], a[252:255]\n"
"s_add_u32 m0, s40, 0x2940\n"
"ds_read_b128 v[168:171], v194 offset:2048\n"
"v_mul_f32 v241, v241, v230\n"
"v_mfma_f32_16x16x32_bf16 a[252:255], v[108:111], v[172:175], a[252:255]\n"
"s_waitcnt lgkmcnt(5)\n"
"ds_read_b128 v[172:175], v194 offset:3072\n"
"v_cvt_pk_bf16_f32 v246, v240, v241\n"
"v_accvgpr_read_b32 v242, a134\n"
"v_mfma_f32_16x16x32_bf16 v[184:187], v[112:115], v[160:163], 0\n"
"s_waitcnt lgkmcnt(5)\n"
"v_accvgpr_read_b32 v243, a135\n"
"v_mul_f32 v242, v242, v230\n"
"v_mfma_f32_16x16x32_bf16 v[184:187], v[128:131], v[164:167], v[184:187]\n"
"s_mov_b32 s42, 0x18800\n"
"buffer_load_dwordx4 v206, s[32:35], s42 offen lds\n"
"v_mul_f32 v243, v243, v230\n"
"v_cvt_pk_bf16_f32 v247, v242, v243\n"
"v_mfma_f32_16x16x32_bf16 v[188:191], v[116:119], v[160:163], 0\n"
"v_accvgpr_read_b32 v240, a136\n"
"v_accvgpr_read_b32 v241, a137\n"
"v_mfma_f32_16x16x32_bf16 v[188:191], v[132:135], v[164:167], v[188:191]\n"
"ds_write_b128 v212, v[244:247] offset:16384\n"
"v_mfma_f32_16x16x32_bf16 v[8:11], v[120:123], v[160:163], 0\n"
"v_mul_f32 v240, v240, v230\n"
"v_mul_f32 v241, v241, v230\n"
"v_mfma_f32_16x16x32_bf16 v[8:11], v[136:139], v[164:167], v[8:11]\n"
"buffer_load_dwordx4 v207, s[32:35], s42 offen offset:1056 lds\n"
"v_mfma_f32_16x16x32_bf16 v[12:15], v[124:127], v[160:163], 0\n"
"s_waitcnt vmcnt(8)\n"
"ds_read_b128 v[160:163], v194 offset:4096\n"
"ds_read_b64_tr_b16 a[30:31], v198 offset:12672\n"
"v_mfma_f32_16x16x32_bf16 v[12:15], v[140:143], v[164:167], v[12:15]\n"
"s_waitcnt lgkmcnt(4)\n"
"ds_read_b128 v[164:167], v194 offset:5120\n"
"ds_read_b64_tr_b16 a[42:43], v198 offset:12680\n"
"v_cvt_pk_bf16_f32 v248, v240, v241\n"
"v_mfma_f32_16x16x32_bf16 v[184:187], v[144:147], v[168:171], v[184:187]\n"
"s_waitcnt lgkmcnt(5)\n"
"ds_read_b64_tr_b16 a[46:47], v199 offset:12672\n"
"v_accvgpr_read_b32 v242, a138\n"
"v_mfma_f32_16x16x32_bf16 v[184:187], v[176:179], v[172:175], v[184:187]\n"
"v_accvgpr_read_b32 v243, a139\n"
"v_mul_f32 v242, v242, v230\n"
"v_mul_f32 v243, v243, v230\n"
"v_mfma_f32_16x16x32_bf16 v[188:191], v[148:151], v[168:171], v[188:191]\n"
"v_cvt_pk_bf16_f32 v249, v242, v243\n"
"v_accvgpr_read_b32 v240, a140\n"
"v_mfma_f32_16x16x32_bf16 v[188:191], v[180:183], v[172:175], v[188:191]\n"
"v_accvgpr_read_b32 v241, a141\n"
"v_mul_f32 v240, v240, v230\n"
"v_mul_f32 v241, v241, v230\n"
"v_cvt_pk_bf16_f32 v250, v240, v241\n"
"v_mfma_f32_16x16x32_bf16 v[8:11], v[152:155], v[168:171], v[8:11]\n"
"s_add_u32 m0, s40, 0x840\n"
"v_accvgpr_read_b32 v242, a142\n"
"v_accvgpr_read_b32 v243, a143\n"
"v_mfma_f32_16x16x32_bf16 v[8:11], a[0:3], v[172:175], v[8:11]\n"
"s_mov_b32 s42, 0x1c000\n"
"buffer_load_dwordx4 v206, s[32:35], s42 offen lds\n"
"v_mul_f32 v242, v242, v230\n"
"v_mfma_f32_16x16x32_bf16 v[12:15], v[156:159], v[168:171], v[12:15]\n"
"ds_read_b128 v[168:171], v194 offset:6144\n"
"v_mul_f32 v243, v243, v230\n"
"v_mfma_f32_16x16x32_bf16 v[12:15], a[4:7], v[172:175], v[12:15]\n"
"s_waitcnt lgkmcnt(5)\n"
"ds_read_b128 v[172:175], v194 offset:7168\n"
"v_cvt_pk_bf16_f32 v251, v242, v243\n"
"v_accvgpr_read_b32 v240, a144\n"
"v_accvgpr_read_b32 v241, a145\n"
"v_mfma_f32_16x16x32_bf16 v[184:187], a[16:19], v[160:163], v[184:187]\n"
"s_waitcnt lgkmcnt(4)\n"
"v_mul_f32 v240, v240, v231\n"
"v_mul_f32 v241, v241, v231\n"
"v_mfma_f32_16x16x32_bf16 v[184:187], v[0:3], v[164:167], v[184:187]\n"
"buffer_load_dwordx4 v207, s[32:35], s42 offen offset:1056 lds\n"
"v_cvt_pk_bf16_f32 v244, v240, v241\n"
"v_mfma_f32_16x16x32_bf16 v[188:191], a[20:23], v[160:163], v[188:191]\n"
"s_waitcnt vmcnt(8)\n"
"ds_read_b64_tr_b16 a[48:49], v198 offset:4224\n"
"v_accvgpr_read_b32 v242, a146\n"
"v_mfma_f32_16x16x32_bf16 v[188:191], v[4:7], v[164:167], v[188:191]\n"
"ds_read_b64_tr_b16 a[52:53], v198 offset:4232\n"
"v_accvgpr_read_b32 v243, a147\n"
"v_mul_f32 v242, v242, v231\n"
"v_mfma_f32_16x16x32_bf16 v[8:11], a[32:35], v[160:163], v[8:11]\n"
"ds_read_b64_tr_b16 a[56:57], v199 offset:4224\n"
"v_mul_f32 v243, v243, v231\n"
"v_mfma_f32_16x16x32_bf16 v[8:11], v[16:19], v[164:167], v[8:11]\n"
"ds_write_b128 v212, v[248:251] offset:17408\n"
"v_mfma_f32_16x16x32_bf16 v[12:15], a[36:39], v[160:163], v[12:15]\n"
"s_add_u32 m0, s40, 0x3180\n"
"ds_read_b128 v[160:163], v194 offset:8192\n"
"v_cvt_pk_bf16_f32 v245, v242, v243\n"
"v_mfma_f32_16x16x32_bf16 v[12:15], v[20:23], v[164:167], v[12:15]\n"
"s_waitcnt lgkmcnt(6)\n"
"ds_read_b128 v[164:167], v194 offset:9216\n"
"v_accvgpr_read_b32 v240, a148\n"
"v_mfma_f32_16x16x32_bf16 v[184:187], v[32:35], v[168:171], v[184:187]\n"
"s_waitcnt lgkmcnt(6)\n"
"v_accvgpr_read_b32 v241, a149\n"
"v_mul_f32 v240, v240, v231\n"
"v_mfma_f32_16x16x32_bf16 v[184:187], v[64:67], v[172:175], v[184:187]\n"
"s_mov_b32 s42, 0x1f800\n"
"buffer_load_dwordx4 v206, s[32:35], s42 offen lds\n"
"v_mul_f32 v241, v241, v231\n"
"v_cvt_pk_bf16_f32 v246, v240, v241\n"
"v_mfma_f32_16x16x32_bf16 v[188:191], v[36:39], v[168:171], v[188:191]\n"
"v_accvgpr_read_b32 v242, a150\n"
"v_accvgpr_read_b32 v243, a151\n"
"v_mfma_f32_16x16x32_bf16 v[188:191], v[68:71], v[172:175], v[188:191]\n"
"v_mul_f32 v242, v242, v231\n"
"v_mul_f32 v243, v243, v231\n"
"v_cvt_pk_bf16_f32 v247, v242, v243\n"
"v_mfma_f32_16x16x32_bf16 v[8:11], v[48:51], v[168:171], v[8:11]\n"
"v_accvgpr_read_b32 v240, a152\n"
"v_accvgpr_read_b32 v241, a153\n"
"v_mfma_f32_16x16x32_bf16 v[8:11], v[80:83], v[172:175], v[8:11]\n"
"buffer_load_dwordx4 v207, s[32:35], s42 offen offset:1056 lds\n"
"v_mul_f32 v240, v240, v231\n"
"v_mfma_f32_16x16x32_bf16 v[12:15], v[52:55], v[168:171], v[12:15]\n"
"s_waitcnt vmcnt(8)\n"
"ds_read_b128 v[168:171], v194 offset:10240\n"
"ds_read_b64_tr_b16 a[50:51], v198 offset:14784\n"
"v_mfma_f32_16x16x32_bf16 v[12:15], v[84:87], v[172:175], v[12:15]\n"
"s_waitcnt lgkmcnt(3)\n"
"ds_read_b128 v[172:175], v194 offset:11264\n"
"ds_read_b64_tr_b16 a[54:55], v198 offset:14792\n"
"v_mul_f32 v241, v241, v231\n"
"v_cvt_pk_bf16_f32 v248, v240, v241\n"
"v_mfma_f32_16x16x32_bf16 v[44:47], v[112:115], v[160:163], 0\n"
"s_waitcnt lgkmcnt(4)\n"
"ds_read_b64_tr_b16 a[58:59], v199 offset:14784\n"
"v_accvgpr_read_b32 v242, a154\n"
"v_mfma_f32_16x16x32_bf16 v[44:47], v[128:131], v[164:167], v[44:47]\n"
"ds_write_b128 v212, v[244:247] offset:24576\n"
"v_mfma_f32_16x16x32_bf16 v[56:59], v[116:119], v[160:163], 0\n"
"v_accvgpr_read_b32 v243, a155\n"
"v_mul_f32 v242, v242, v231\n"
"v_mfma_f32_16x16x32_bf16 v[56:59], v[132:135], v[164:167], v[56:59]\n"
"v_mul_f32 v243, v243, v231\n"
"v_cvt_pk_bf16_f32 v249, v242, v243\n"
"v_mfma_f32_16x16x32_bf16 v[60:63], v[120:123], v[160:163], 0\n"
"s_add_u32 m0, s40, 0x1080\n"
"s_mov_b32 s47, 0\n"
"v_accvgpr_read_b32 v240, a156\n"
"v_mfma_f32_16x16x32_bf16 v[60:63], v[136:139], v[164:167], v[60:63]\n"
"s_mov_b32 s42, 0x23000\n"
"buffer_load_dwordx4 v206, s[32:35], s42 offen lds\n"
"v_accvgpr_read_b32 v241, a157\n"
"v_mfma_f32_16x16x32_bf16 v[72:75], v[124:127], v[160:163], 0\n"
"ds_read_b128 v[160:163], v194 offset:12288\n"
"v_mul_f32 v240, v240, v231\n"
"v_mfma_f32_16x16x32_bf16 v[72:75], v[140:143], v[164:167], v[72:75]\n"
"s_waitcnt lgkmcnt(6)\n"
"ds_read_b128 v[164:167], v194 offset:13312\n"
"v_cvt_pk_bf16_f32 v100, v184, v185\n"
"v_mul_f32 v241, v241, v231\n"
"v_mfma_f32_16x16x32_bf16 v[44:47], v[144:147], v[168:171], v[44:47]\n"
"s_waitcnt lgkmcnt(5)\n"
"v_cvt_pk_bf16_f32 v101, v186, v187\n"
"v_cvt_pk_bf16_f32 v250, v240, v241\n"
"v_mfma_f32_16x16x32_bf16 v[44:47], v[176:179], v[172:175], v[44:47]\n"
"buffer_load_dwordx4 v207, s[32:35], s42 offen offset:1056 lds\n"
"v_accvgpr_read_b32 v242, a158\n"
"v_accvgpr_read_b32 v243, a159\n"
"v_mfma_f32_16x16x32_bf16 v[56:59], v[148:151], v[168:171], v[56:59]\n"
"s_waitcnt vmcnt(8)\n"
"ds_read_b64_tr_b16 a[60:61], v198 offset:0\n"
"v_mul_f32 v242, v242, v231\n"
"v_mfma_f32_16x16x32_bf16 v[56:59], v[180:183], v[172:175], v[56:59]\n"
"ds_read_b64_tr_b16 a[64:65], v198 offset:8\n"
"v_cvt_pk_bf16_f32 v102, v188, v189\n"
"v_mul_f32 v243, v243, v231\n"
"v_mfma_f32_16x16x32_bf16 v[60:63], v[152:155], v[168:171], v[60:63]\n"
"ds_read_b64_tr_b16 a[68:69], v199 offset:0\n"
"v_cvt_pk_bf16_f32 v251, v242, v243\n"
"v_mfma_f32_16x16x32_bf16 v[60:63], a[0:3], v[172:175], v[60:63]\n"
"ds_write_b128 v212, v[248:251] offset:25600\n"
"v_cvt_pk_bf16_f32 v103, v190, v191\n"
"v_cvt_pk_bf16_f32 v104, v8, v9\n"
"v_mfma_f32_16x16x32_bf16 v[72:75], v[156:159], v[168:171], v[72:75]\n"
"s_add_u32 m0, s40, 0x39c0\n"
"ds_read_b128 v[168:171], v194 offset:14336\n"
"v_mfma_f32_16x16x32_bf16 v[72:75], a[4:7], v[172:175], v[72:75]\n"
"s_waitcnt lgkmcnt(6)\n"
"ds_read_b128 v[172:175], v194 offset:15360\n"
"v_cvt_pk_bf16_f32 v105, v10, v11\n"
"v_mfma_f32_16x16x32_bf16 v[44:47], a[16:19], v[160:163], v[44:47]\n"
"s_waitcnt lgkmcnt(6)\n"
"v_cvt_pk_bf16_f32 v106, v12, v13\n"
"v_mfma_f32_16x16x32_bf16 v[44:47], v[0:3], v[164:167], v[44:47]\n"
"s_mov_b32 s42, 0x26800\n"
"buffer_load_dwordx4 v206, s[32:35], s42 offen lds\n"
"v_mfma_f32_16x16x32_bf16 v[56:59], a[20:23], v[160:163], v[56:59]\n"
"v_cvt_pk_bf16_f32 v107, v14, v15\n"
"v_mfma_f32_16x16x32_bf16 v[56:59], v[4:7], v[164:167], v[56:59]\n"
"buffer_store_dwordx4 v[100:103], v212, s[36:39], s47 offen sc1\n"
"v_mfma_f32_16x16x32_bf16 v[60:63], a[32:35], v[160:163], v[60:63]\n"
"v_mfma_f32_16x16x32_bf16 v[60:63], v[16:19], v[164:167], v[60:63]\n"
"buffer_load_dwordx4 v207, s[32:35], s42 offen offset:1056 lds\n"
"v_mfma_f32_16x16x32_bf16 v[72:75], a[36:39], v[160:163], v[72:75]\n"
"s_waitcnt vmcnt(9) lgkmcnt(2)\n"
".Lpad9_%=:\n"
"s_cbranch_vccnz .Lpad12_%=\n"
"s_barrier\n"
"ds_read_b128 v[160:163], v194 offset:16384\n"
"ds_read_b64_tr_b16 a[62:63], v198 offset:10560\n"
"v_mfma_f32_16x16x32_bf16 v[72:75], v[20:23], v[164:167], v[72:75]\n"
"s_waitcnt lgkmcnt(3)\n"
"ds_read_b128 v[164:167], v194 offset:17408\n"
"ds_read_b64_tr_b16 a[66:67], v198 offset:10568\n"
"v_accvgpr_read_b32 v240, a160\n"
"v_mfma_f32_16x16x32_bf16 v[44:47], v[32:35], v[168:171], v[44:47]\n"
"s_waitcnt lgkmcnt(4)\n"
"ds_read_b64_tr_b16 a[70:71], v199 offset:10560\n"
"v_accvgpr_read_b32 v241, a161\n"
"v_mfma_f32_16x16x32_bf16 v[44:47], v[64:67], v[172:175], v[44:47]\n"
"v_mul_f32 v240, v240, v232\n"
"v_mul_f32 v241, v241, v232\n"
"v_cvt_pk_bf16_f32 v244, v240, v241\n"
"v_accvgpr_read_b32 v242, a162\n"
"v_mfma_f32_16x16x32_bf16 v[56:59], v[36:39], v[168:171], v[56:59]\n"
"v_accvgpr_read_b32 v243, a163\n"
"v_mul_f32 v242, v242, v232\n"
"v_mfma_f32_16x16x32_bf16 v[56:59], v[68:71], v[172:175], v[56:59]\n"
"buffer_store_dwordx4 v[104:107], v212, s[36:39], s47 offen offset:1024 sc1\n"
"v_mfma_f32_16x16x32_bf16 v[60:63], v[48:51], v[168:171], v[60:63]\n"
"s_add_u32 m0, s40, 0\n"
"v_mul_f32 v243, v243, v232\n"
"v_cvt_pk_bf16_f32 v245, v242, v243\n"
"v_mfma_f32_16x16x32_bf16 v[60:63], v[80:83], v[172:175], v[60:63]\n"
"s_mov_b32 s42, 0x2a000\n"
"buffer_load_dwordx4 v206, s[32:35], s42 offen lds\n"
"v_accvgpr_read_b32 v240, a164\n"
"v_mfma_f32_16x16x32_bf16 v[72:75], v[52:55], v[168:171], v[72:75]\n"
"ds_read_b128 v[168:171], v194 offset:18432\n"
"v_accvgpr_read_b32 v241, a165\n"
"v_mfma_f32_16x16x32_bf16 v[72:75], v[84:87], v[172:175], v[72:75]\n"
"s_waitcnt lgkmcnt(5)\n"
"ds_read_b128 v[172:175], v194 offset:19456\n"
"v_mul_f32 v240, v240, v232\n"
"v_mul_f32 v241, v241, v232\n"
"v_cvt_pk_bf16_f32 v246, v240, v241\n"
"v_mfma_f32_16x16x32_bf16 v[184:187], v[112:115], v[160:163], 0\n"
"s_waitcnt lgkmcnt(4)\n"
"v_accvgpr_read_b32 v242, a166\n"
"v_accvgpr_read_b32 v243, a167\n"
"v_mfma_f32_16x16x32_bf16 v[184:187], v[128:131], v[164:167], v[184:187]\n"
"buffer_load_dwordx4 v207, s[32:35], s42 offen offset:1056 lds\n"
"v_mul_f32 v242, v242, v232\n"
"v_mul_f32 v243, v243, v232\n"
"v_mfma_f32_16x16x32_bf16 v[188:191], v[116:119], v[160:163], 0\n"
"v_cvt_pk_bf16_f32 v247, v242, v243\n"
"v_accvgpr_read_b32 v240, a168\n"
"v_mfma_f32_16x16x32_bf16 v[188:191], v[132:135], v[164:167], v[188:191]\n"
"ds_write_b128 v212, v[244:247] offset:32768\n"
"v_mfma_f32_16x16x32_bf16 v[8:11], v[120:123], v[160:163], 0\n"
"s_mov_b32 s47, 0x2000\n"
"v_accvgpr_read_b32 v241, a169\n"
"v_mfma_f32_16x16x32_bf16 v[8:11], v[136:139], v[164:167], v[8:11]\n"
"v_cvt_pk_bf16_f32 v96, v44, v45\n"
"v_mul_f32 v240, v240, v232\n"
"v_mul_f32 v241, v241, v232\n"
"v_mfma_f32_16x16x32_bf16 v[12:15], v[124:127], v[160:163], 0\n"
"s_add_u32 m0, s40, 0x2940\n"
"ds_read_b128 v[160:163], v194 offset:20480\n"
"v_cvt_pk_bf16_f32 v248, v240, v241\n"
"v_mfma_f32_16x16x32_bf16 v[12:15], v[140:143], v[164:167], v[12:15]\n"
"s_waitcnt lgkmcnt(3)\n"
"ds_read_b128 v[164:167], v194 offset:21504\n"
"v_accvgpr_read_b32 v242, a170\n"
"v_accvgpr_read_b32 v243, a171\n"
"v_mfma_f32_16x16x32_bf16 v[184:187], v[144:147], v[168:171], v[184:187]\n"
"s_waitcnt lgkmcnt(3)\n"
"v_cvt_pk_bf16_f32 v97, v46, v47\n"
"v_cvt_pk_bf16_f32 v98, v56, v57\n"
"v_mfma_f32_16x16x32_bf16 v[184:187], v[176:179], v[172:175], v[184:187]\n"
"s_mov_b32 s42, 0x2d800\n"
"buffer_load_dwordx4 v206, s[32:35], s42 offen lds\n"
"v_mul_f32 v242, v242, v232\n"
"v_mul_f32 v243, v243, v232\n"
"v_mfma_f32_16x16x32_bf16 v[188:191], v[148:151], v[168:171], v[188:191]\n"
"v_cvt_pk_bf16_f32 v249, v242, v243\n"
"v_accvgpr_read_b32 v240, a172\n"
"v_mfma_f32_16x16x32_bf16 v[188:191], v[180:183], v[172:175], v[188:191]\n"
"v_cvt_pk_bf16_f32 v99, v58, v59\n"
"v_accvgpr_read_b32 v241, a173\n"
"v_mul_f32 v240, v240, v232\n"
"v_mul_f32 v241, v241, v232\n"
"v_mfma_f32_16x16x32_bf16 v[8:11], v[152:155], v[168:171], v[8:11]\n"
"v_cvt_pk_bf16_f32 v100, v60, v61\n"
"v_cvt_pk_bf16_f32 v250, v240, v241\n"
"v_mfma_f32_16x16x32_bf16 v[8:11], a[0:3], v[172:175], v[8:11]\n"
"buffer_load_dwordx4 v207, s[32:35], s42 offen offset:1056 lds\n"
"v_accvgpr_read_b32 v242, a174\n"
"v_accvgpr_read_b32 v243, a175\n"
"v_mfma_f32_16x16x32_bf16 v[12:15], v[156:159], v[168:171], v[12:15]\n"
"ds_read_b128 v[168:171], v194 offset:22528\n"
"v_mul_f32 v242, v242, v232\n"
"v_mfma_f32_16x16x32_bf16 v[12:15], a[4:7], v[172:175], v[12:15]\n"
"s_waitcnt lgkmcnt(2)\n"
"ds_read_b128 v[172:175], v194 offset:23552\n"
"v_mul_f32 v243, v243, v232\n"
"v_cvt_pk_bf16_f32 v251, v242, v243\n"
"v_accvgpr_read_b32 v240, a176\n"
"v_mfma_f32_16x16x32_bf16 v[184:187], a[16:19], v[160:163], v[184:187]\n"
"s_waitcnt lgkmcnt(2)\n"
"v_cvt_pk_bf16_f32 v101, v62, v63\n"
"v_accvgpr_read_b32 v241, a177\n"
"v_mfma_f32_16x16x32_bf16 v[184:187], v[0:3], v[164:167], v[184:187]\n"
"buffer_store_dwordx4 v[96:99], v212, s[36:39], s47 offen sc1\n"
"v_mfma_f32_16x16x32_bf16 v[188:191], a[20:23], v[160:163], v[188:191]\n"
"v_mul_f32 v240, v240, v233\n"
"v_mul_f32 v241, v241, v233\n"
"v_mfma_f32_16x16x32_bf16 v[188:191], v[4:7], v[164:167], v[188:191]\n"
"ds_write_b128 v212, v[248:251] offset:33792\n"
"v_mfma_f32_16x16x32_bf16 v[8:11], a[32:35], v[160:163], v[8:11]\n"
"s_add_u32 m0, s40, 0x4200\n"
"v_cvt_pk_bf16_f32 v244, v240, v241\n"
"v_accvgpr_read_b32 v242, a178\n"
"v_mfma_f32_16x16x32_bf16 v[8:11], v[16:19], v[164:167], v[8:11]\n"
"s_mov_b32 s42, 0x31000\n"
"buffer_load_dwordx4 v206, s[32:35], s42 offen lds\n"
"v_mfma_f32_16x16x32_bf16 v[12:15], a[36:39], v[160:163], v[12:15]\n"
"ds_read_b128 v[160:163], v194 offset:24576\n"
"v_accvgpr_read_b32 v243, a179\n"
"v_mfma_f32_16x16x32_bf16 v[12:15], v[20:23], v[164:167], v[12:15]\n"
"s_waitcnt lgkmcnt(3)\n"
"ds_read_b128 v[164:167], v194 offset:25600\n"
"v_mul_f32 v242, v242, v233\n"
"v_mul_f32 v243, v243, v233\n"
"v_cvt_pk_bf16_f32 v245, v242, v243\n"
"v_mfma_f32_16x16x32_bf16 v[184:187], v[32:35], v[168:171], v[184:187]\n"
"s_waitcnt lgkmcnt(3)\n"
"v_cvt_pk_bf16_f32 v102, v72, v73\n"
"v_accvgpr_read_b32 v240, a180\n"
"v_mfma_f32_16x16x32_bf16 v[184:187], v[64:67], v[172:175], v[184:187]\n"
"buffer_load_dwordx4 v207, s[32:35], s42 offen offset:1056 lds\n"
"v_accvgpr_read_b32 v241, a181\n"
"v_mfma_f32_16x16x32_bf16 v[188:191], v[36:39], v[168:171], v[188:191]\n"
"v_cvt_pk_bf16_f32 v103, v74, v75\n"
"v_mul_f32 v240, v240, v233\n"
"v_mfma_f32_16x16x32_bf16 v[188:191], v[68:71], v[172:175], v[188:191]\n"
"buffer_store_dwordx4 v[100:103], v212, s[36:39], s47 offen offset:1024 sc1\n"
"v_mfma_f32_16x16x32_bf16 v[8:11], v[48:51], v[168:171], v[8:11]\n"
"v_mul_f32 v241, v241, v233\n"
"v_cvt_pk_bf16_f32 v246, v240, v241\n"
"v_mfma_f32_16x16x32_bf16 v[8:11], v[80:83], v[172:175], v[8:11]\n"
"v_accvgpr_read_b32 v242, a182\n"
"v_accvgpr_read_b32 v243, a183\n"
"v_mul_f32 v242, v242, v233\n"
"v_mfma_f32_16x16x32_bf16 v[12:15], v[52:55], v[168:171], v[12:15]\n"
"s_add_u32 m0, s40, 0x18c0\n"
"ds_read_b128 v[168:171], v194 offset:26624\n"
"v_mul_f32 v243, v243, v233\n"
"v_mfma_f32_16x16x32_bf16 v[12:15], v[84:87], v[172:175], v[12:15]\n"
"s_waitcnt lgkmcnt(2)\n"
"ds_read_b128 v[172:175], v194 offset:27648\n"
"v_cvt_pk_bf16_f32 v247, v242, v243\n"
"v_accvgpr_read_b32 v240, a184\n"
"v_accvgpr_read_b32 v241, a185\n"
"v_mfma_f32_16x16x32_bf16 v[44:47], v[112:115], v[160:163], 0\n"
"s_waitcnt lgkmcnt(2)\n"
"v_mul_f32 v240, v240, v233\n"
"v_mul_f32 v241, v241, v233\n"
"v_mfma_f32_16x16x32_bf16 v[44:47], v[128:131], v[164:167], v[44:47]\n"
"s_mov_b32 s42, 0x34800\n"
"buffer_load_dwordx4 v206, s[32:35], s42 offen lds\n"
"v_cvt_pk_bf16_f32 v248, v240, v241\n"
"v_accvgpr_read_b32 v242, a186\n"
"v_mfma_f32_16x16x32_bf16 v[56:59], v[116:119], v[160:163], 0\n"
"v_accvgpr_read_b32 v243, a187\n"
"v_mul_f32 v242, v242, v233\n"
"v_mfma_f32_16x16x32_bf16 v[56:59], v[132:135], v[164:167], v[56:59]\n"
"ds_write_b128 v212, v[244:247] offset:40960\n"
"v_mfma_f32_16x16x32_bf16 v[60:63], v[120:123], v[160:163], 0\n"
"s_mov_b32 s47, 0x4000\n"
"v_mul_f32 v243, v243, v233\n"
"v_mfma_f32_16x16x32_bf16 v[60:63], v[136:139], v[164:167], v[60:63]\n"
"buffer_load_dwordx4 v207, s[32:35], s42 offen offset:1056 lds\n"
"v_cvt_pk_bf16_f32 v249, v242, v243\n"
"v_mfma_f32_16x16x32_bf16 v[72:75], v[124:127], v[160:163], 0\n"
"ds_read_b128 v[160:163], v194 offset:28672\n"
"v_accvgpr_read_b32 v240, a188\n"
"v_mfma_f32_16x16x32_bf16 v[72:75], v[140:143], v[164:167], v[72:75]\n"
"s_waitcnt lgkmcnt(3)\n"
"ds_read_b128 v[164:167], v194 offset:29696\n"
"v_accvgpr_read_b32 v241, a189\n"
"v_mul_f32 v240, v240, v233\n"
"v_mul_f32 v241, v241, v233\n"
"v_mfma_f32_16x16x32_bf16 v[44:47], v[144:147], v[168:171], v[44:47]\n"
"s_waitcnt lgkmcnt(3)\n"
"v_cvt_pk_bf16_f32 v250, v240, v241\n"
"v_accvgpr_read_b32 v242, a190\n"
"v_mfma_f32_16x16x32_bf16 v[44:47], v[176:179], v[172:175], v[44:47]\n"
"v_cvt_pk_bf16_f32 v104, v184, v185\n"
"v_accvgpr_read_b32 v243, a191\n"
"v_mul_f32 v242, v242, v233\n"
"v_mul_f32 v243, v243, v233\n"
"v_mfma_f32_16x16x32_bf16 v[56:59], v[148:151], v[168:171], v[56:59]\n"
"v_cvt_pk_bf16_f32 v105, v186, v187\n"
"v_cvt_pk_bf16_f32 v251, v242, v243\n"
"v_mfma_f32_16x16x32_bf16 v[56:59], v[180:183], v[172:175], v[56:59]\n"
"ds_write_b128 v212, v[248:251] offset:41984\n"
"v_cvt_pk_bf16_f32 v106, v188, v189\n"
"v_mfma_f32_16x16x32_bf16 v[60:63], v[152:155], v[168:171], v[60:63]\n"
"v_cvt_pk_bf16_f32 v107, v190, v191\n"
"v_mfma_f32_16x16x32_bf16 v[60:63], a[0:3], v[172:175], v[60:63]\n"
"buffer_store_dwordx4 v[104:107], v212, s[36:39], s47 offen sc1\n"
"v_mfma_f32_16x16x32_bf16 v[72:75], v[156:159], v[168:171], v[72:75]\n"
"ds_read_b128 v[168:171], v194 offset:30720\n"
"v_mfma_f32_16x16x32_bf16 v[72:75], a[4:7], v[172:175], v[72:75]\n"
"s_waitcnt lgkmcnt(3)\n"
"ds_read_b128 v[172:175], v194 offset:31744\n"
"v_mfma_f32_16x16x32_bf16 v[44:47], a[16:19], v[160:163], v[44:47]\n"
"s_waitcnt lgkmcnt(3)\n"
"v_mfma_f32_16x16x32_bf16 v[44:47], v[0:3], v[164:167], v[44:47]\n"
"v_mfma_f32_16x16x32_bf16 v[56:59], a[20:23], v[160:163], v[56:59]\n"
"v_mfma_f32_16x16x32_bf16 v[56:59], v[4:7], v[164:167], v[56:59]\n"
"v_mfma_f32_16x16x32_bf16 v[60:63], a[32:35], v[160:163], v[60:63]\n"
"v_cvt_pk_bf16_f32 v96, v8, v9\n"
"v_mfma_f32_16x16x32_bf16 v[60:63], v[16:19], v[164:167], v[60:63]\n"
"v_cvt_pk_bf16_f32 v97, v10, v11\n"
"v_mfma_f32_16x16x32_bf16 v[72:75], a[36:39], v[160:163], v[72:75]\n"
"s_waitcnt lgkmcnt(2)\n"
"s_barrier\n"
"ds_read_b128 v[160:163], v194 offset:32768\n"
"v_mfma_f32_16x16x32_bf16 v[72:75], v[20:23], v[164:167], v[72:75]\n"
"s_waitcnt lgkmcnt(2)\n"
"ds_read_b128 v[164:167], v194 offset:33792\n"
"v_accvgpr_read_b32 v240, a192\n"
"v_accvgpr_read_b32 v241, a193\n"
"v_mfma_f32_16x16x32_bf16 v[44:47], v[32:35], v[168:171], v[44:47]\n"
"s_waitcnt lgkmcnt(2)\n"
"v_mul_f32 v240, v240, v234\n"
"v_mul_f32 v241, v241, v234\n"
"v_mfma_f32_16x16x32_bf16 v[44:47], v[64:67], v[172:175], v[44:47]\n"
"v_cvt_pk_bf16_f32 v98, v12, v13\n"
"v_cvt_pk_bf16_f32 v244, v240, v241\n"
"v_accvgpr_read_b32 v242, a194\n"
"v_accvgpr_read_b32 v243, a195\n"
"v_mfma_f32_16x16x32_bf16 v[56:59], v[36:39], v[168:171], v[56:59]\n"
"v_cvt_pk_bf16_f32 v99, v14, v15\n"
"v_mul_f32 v242, v242, v234\n"
"v_mfma_f32_16x16x32_bf16 v[56:59], v[68:71], v[172:175], v[56:59]\n"
"buffer_store_dwordx4 v[96:99], v212, s[36:39], s47 offen offset:1024 sc1\n"
"v_mfma_f32_16x16x32_bf16 v[60:63], v[48:51], v[168:171], v[60:63]\n"
"v_mul_f32 v243, v243, v234\n"
"v_cvt_pk_bf16_f32 v245, v242, v243\n"
"v_mfma_f32_16x16x32_bf16 v[60:63], v[80:83], v[172:175], v[60:63]\n"
"v_accvgpr_read_b32 v240, a196\n"
"v_accvgpr_read_b32 v241, a197\n"
"v_mul_f32 v240, v240, v234\n"
"v_mfma_f32_16x16x32_bf16 v[72:75], v[52:55], v[168:171], v[72:75]\n"
"ds_read_b128 v[168:171], v194 offset:34816\n"
"v_mul_f32 v241, v241, v234\n"
"v_mfma_f32_16x16x32_bf16 v[72:75], v[84:87], v[172:175], v[72:75]\n"
"s_waitcnt lgkmcnt(2)\n"
"ds_read_b128 v[172:175], v194 offset:35840\n"
"v_cvt_pk_bf16_f32 v246, v240, v241\n"
"v_accvgpr_read_b32 v242, a198\n"
"v_accvgpr_read_b32 v243, a199\n"
"v_mfma_f32_16x16x32_bf16 v[184:187], v[112:115], v[160:163], 0\n"
"s_waitcnt lgkmcnt(2)\n"
"v_mul_f32 v242, v242, v234\n"
"v_mul_f32 v243, v243, v234\n"
"v_mfma_f32_16x16x32_bf16 v[184:187], v[128:131], v[164:167], v[184:187]\n"
"v_cvt_pk_bf16_f32 v247, v242, v243\n"
"v_accvgpr_read_b32 v240, a200\n"
"v_accvgpr_read_b32 v241, a201\n"
"v_mfma_f32_16x16x32_bf16 v[188:191], v[116:119], v[160:163], 0\n"
"v_mul_f32 v240, v240, v234\n"
"v_mul_f32 v241, v241, v234\n"
"v_mfma_f32_16x16x32_bf16 v[188:191], v[132:135], v[164:167], v[188:191]\n"
"ds_write_b128 v212, v[244:247] offset:49152\n"
"v_mfma_f32_16x16x32_bf16 v[8:11], v[120:123], v[160:163], 0\n"
"s_mov_b32 s47, 0x6000\n"
"v_cvt_pk_bf16_f32 v248, v240, v241\n"
"v_mfma_f32_16x16x32_bf16 v[8:11], v[136:139], v[164:167], v[8:11]\n"
"v_cvt_pk_bf16_f32 v100, v44, v45\n"
"v_accvgpr_read_b32 v242, a202\n"
"v_accvgpr_read_b32 v243, a203\n"
"v_mfma_f32_16x16x32_bf16 v[12:15], v[124:127], v[160:163], 0\n"
"ds_read_b128 v[160:163], v194 offset:36864\n"
"v_mul_f32 v242, v242, v234\n"
"v_mfma_f32_16x16x32_bf16 v[12:15], v[140:143], v[164:167], v[12:15]\n"
"s_waitcnt lgkmcnt(3)\n"
"ds_read_b128 v[164:167], v194 offset:37888\n"
"v_mul_f32 v243, v243, v234\n"
"v_cvt_pk_bf16_f32 v249, v242, v243\n"
"v_accvgpr_read_b32 v240, a204\n"
"v_mfma_f32_16x16x32_bf16 v[184:187], v[144:147], v[168:171], v[184:187]\n"
"s_waitcnt lgkmcnt(3)\n"
"v_cvt_pk_bf16_f32 v101, v46, v47\n"
"v_accvgpr_read_b32 v241, a205\n"
"v_mfma_f32_16x16x32_bf16 v[184:187], v[176:179], v[172:175], v[184:187]\n"
"v_cvt_pk_bf16_f32 v102, v56, v57\n"
"v_cvt_pk_bf16_f32 v103, v58, v59\n"
"v_mul_f32 v240, v240, v234\n"
"v_mul_f32 v241, v241, v234\n"
"v_mfma_f32_16x16x32_bf16 v[188:191], v[148:151], v[168:171], v[188:191]\n"
"v_cvt_pk_bf16_f32 v104, v60, v61\n"
"v_cvt_pk_bf16_f32 v250, v240, v241\n"
"v_mfma_f32_16x16x32_bf16 v[188:191], v[180:183], v[172:175], v[188:191]\n"
"buffer_store_dwordx4 v[100:103], v212, s[36:39], s47 offen sc1\n"
"v_mfma_f32_16x16x32_bf16 v[8:11], v[152:155], v[168:171], v[8:11]\n"
"v_accvgpr_read_b32 v242, a206\n"
"v_accvgpr_read_b32 v243, a207\n"
"v_mfma_f32_16x16x32_bf16 v[8:11], a[0:3], v[172:175], v[8:11]\n"
"v_mul_f32 v242, v242, v234\n"
"v_mul_f32 v243, v243, v234\n"
"v_mfma_f32_16x16x32_bf16 v[12:15], v[156:159], v[168:171], v[12:15]\n"
"ds_read_b128 v[168:171], v194 offset:38912\n"
"v_cvt_pk_bf16_f32 v251, v242, v243\n"
"v_mfma_f32_16x16x32_bf16 v[12:15], a[4:7], v[172:175], v[12:15]\n"
"s_waitcnt lgkmcnt(2)\n"
"ds_read_b128 v[172:175], v194 offset:39936\n"
"v_accvgpr_read_b32 v240, a208\n"
"v_accvgpr_read_b32 v241, a209\n"
"v_mul_f32 v240, v240, v235\n"
"v_mfma_f32_16x16x32_bf16 v[184:187], a[16:19], v[160:163], v[184:187]\n"
"s_waitcnt lgkmcnt(2)\n"
"v_mul_f32 v241, v241, v235\n"
"v_cvt_pk_bf16_f32 v244, v240, v241\n"
"v_mfma_f32_16x16x32_bf16 v[184:187], v[0:3], v[164:167], v[184:187]\n"
"ds_write_b128 v212, v[248:251] offset:50176\n"
"v_cvt_pk_bf16_f32 v105, v62, v63\n"
"v_mfma_f32_16x16x32_bf16 v[188:191], a[20:23], v[160:163], v[188:191]\n"
"v_cvt_pk_bf16_f32 v106, v72, v73\n"
"v_accvgpr_read_b32 v242, a210\n"
"v_mfma_f32_16x16x32_bf16 v[188:191], v[4:7], v[164:167], v[188:191]\n"
"s_waitcnt vmcnt(21)\n"
"ds_read_b64_tr_b16 a[72:73], v198 offset:2112\n"
"v_accvgpr_read_b32 v243, a211\n"
"v_mfma_f32_16x16x32_bf16 v[8:11], a[32:35], v[160:163], v[8:11]\n"
"ds_read_b64_tr_b16 a[76:77], v198 offset:2120\n"
"v_cvt_pk_bf16_f32 v107, v74, v75\n"
"v_mfma_f32_16x16x32_bf16 v[8:11], v[16:19], v[164:167], v[8:11]\n"
"buffer_store_dwordx4 v[104:107], v212, s[36:39], s47 offen offset:1024 sc1\n"
"v_mfma_f32_16x16x32_bf16 v[12:15], a[36:39], v[160:163], v[12:15]\n"
"ds_read_b128 v[160:163], v194 offset:40960\n"
"ds_read_b64_tr_b16 a[96:97], v199 offset:2112\n"
"v_mfma_f32_16x16x32_bf16 v[12:15], v[20:23], v[164:167], v[12:15]\n"
"s_waitcnt vmcnt(20) lgkmcnt(6)\n"
"ds_read_b128 v[164:167], v194 offset:41984\n"
"ds_read_b64_tr_b16 a[74:75], v198 offset:12672\n"
"v_mfma_f32_16x16x32_bf16 v[184:187], v[32:35], v[168:171], v[184:187]\n"
"s_waitcnt lgkmcnt(7)\n"
"ds_read_b64_tr_b16 a[78:79], v198 offset:12680\n"
"v_mul_f32 v242, v242, v235\n"
"v_mfma_f32_16x16x32_bf16 v[184:187], v[64:67], v[172:175], v[184:187]\n"
"v_mul_f32 v243, v243, v235\n"
"v_cvt_pk_bf16_f32 v245, v242, v243\n"
"v_accvgpr_read_b32 v240, a212\n"
"v_accvgpr_read_b32 v241, a213\n"
"v_mfma_f32_16x16x32_bf16 v[188:191], v[36:39], v[168:171], v[188:191]\n"
"ds_read_b64_tr_b16 a[98:99], v199 offset:12672\n"
"v_mul_f32 v240, v240, v235\n"
"v_mfma_f32_16x16x32_bf16 v[188:191], v[68:71], v[172:175], v[188:191]\n"
"s_waitcnt vmcnt(18)\n"
"ds_read_b64_tr_b16 a[100:101], v198 offset:4224\n"
"v_mul_f32 v241, v241, v235\n"
"v_cvt_pk_bf16_f32 v246, v240, v241\n"
"v_accvgpr_read_b32 v242, a214\n"
"v_mfma_f32_16x16x32_bf16 v[8:11], v[48:51], v[168:171], v[8:11]\n"
"ds_read_b64_tr_b16 a[104:105], v198 offset:4232\n"
"v_accvgpr_read_b32 v243, a215\n"
"v_mfma_f32_16x16x32_bf16 v[8:11], v[80:83], v[172:175], v[8:11]\n"
"v_mul_f32 v242, v242, v235\n"
"v_mul_f32 v243, v243, v235\n"
"v_cvt_pk_bf16_f32 v247, v242, v243\n"
"v_accvgpr_read_b32 v240, a216\n"
"v_mfma_f32_16x16x32_bf16 v[12:15], v[52:55], v[168:171], v[12:15]\n"
"ds_read_b128 v[168:171], v194 offset:43008\n"
"ds_read_b64_tr_b16 a[108:109], v199 offset:4224\n"
"v_mfma_f32_16x16x32_bf16 v[12:15], v[84:87], v[172:175], v[12:15]\n"
"s_waitcnt vmcnt(15) lgkmcnt(9)\n"
"ds_read_b128 v[172:175], v194 offset:44032\n"
"ds_read_b64_tr_b16 a[102:103], v198 offset:14784\n"
"v_accvgpr_read_b32 v241, a217\n"
"v_mul_f32 v240, v240, v235\n"
"v_mfma_f32_16x16x32_bf16 v[44:47], v[112:115], v[160:163], 0\n"
"s_waitcnt lgkmcnt(9)\n"
"ds_read_b64_tr_b16 a[106:107], v198 offset:14792\n"
"v_mul_f32 v241, v241, v235\n"
"v_mfma_f32_16x16x32_bf16 v[44:47], v[128:131], v[164:167], v[44:47]\n"
"ds_write_b128 v212, v[244:247] offset:57344\n"
"v_mfma_f32_16x16x32_bf16 v[56:59], v[116:119], v[160:163], 0\n"
"ds_read_b64_tr_b16 a[110:111], v199 offset:14784\n"
"v_cvt_pk_bf16_f32 v248, v240, v241\n"
"v_mfma_f32_16x16x32_bf16 v[56:59], v[132:135], v[164:167], v[56:59]\n"
"s_waitcnt vmcnt(12)\n"
"ds_read_b64_tr_b16 a[112:113], v198 offset:0\n"
"v_accvgpr_read_b32 v242, a218\n"
"v_mfma_f32_16x16x32_bf16 v[60:63], v[120:123], v[160:163], 0\n"
"ds_read_b64_tr_b16 a[116:117], v198 offset:8\n"
"v_accvgpr_read_b32 v243, a219\n"
"v_mfma_f32_16x16x32_bf16 v[60:63], v[136:139], v[164:167], v[60:63]\n"
"s_mov_b32 s47, 0x8000\n"
"v_mul_f32 v242, v242, v235\n"
"v_mul_f32 v243, v243, v235\n"
"v_cvt_pk_bf16_f32 v249, v242, v243\n"
"v_mfma_f32_16x16x32_bf16 v[72:75], v[124:127], v[160:163], 0\n"
"ds_read_b128 v[160:163], v194 offset:45056\n"
"ds_read_b64_tr_b16 a[120:121], v199 offset:0\n"
"v_mfma_f32_16x16x32_bf16 v[72:75], v[140:143], v[164:167], v[72:75]\n"
"s_waitcnt vmcnt(10) lgkmcnt(10)\n"
"ds_read_b128 v[164:167], v194 offset:46080\n"
"ds_read_b64_tr_b16 a[114:115], v198 offset:10560\n"
"v_accvgpr_read_b32 v240, a220\n"
"v_accvgpr_read_b32 v241, a221\n"
"v_mfma_f32_16x16x32_bf16 v[44:47], v[144:147], v[168:171], v[44:47]\n"
"s_waitcnt lgkmcnt(10)\n"
"ds_read_b64_tr_b16 a[118:119], v198 offset:10568\n"
"v_mul_f32 v240, v240, v235\n"
"v_mfma_f32_16x16x32_bf16 v[44:47], v[176:179], v[172:175], v[44:47]\n"
"v_cvt_pk_bf16_f32 v96, v184, v185\n"
"v_mul_f32 v241, v241, v235\n"
"v_cvt_pk_bf16_f32 v250, v240, v241\n"
"v_accvgpr_read_b32 v242, a222\n"
"v_mfma_f32_16x16x32_bf16 v[56:59], v[148:151], v[168:171], v[56:59]\n"
"ds_read_b64_tr_b16 a[122:123], v199 offset:10560\n"
"v_accvgpr_read_b32 v243, a223\n"
"v_mfma_f32_16x16x32_bf16 v[56:59], v[180:183], v[172:175], v[56:59]\n"
"s_waitcnt vmcnt(7)\n"
"ds_read_b64_tr_b16 a[124:125], v198 offset:16896\n"
"v_cvt_pk_bf16_f32 v97, v186, v187\n"
"v_mul_f32 v242, v242, v235\n"
"v_mul_f32 v243, v243, v235\n"
"v_mfma_f32_16x16x32_bf16 v[60:63], v[152:155], v[168:171], v[60:63]\n"
"ds_read_b64_tr_b16 a[80:81], v198 offset:16904\n"
"v_cvt_pk_bf16_f32 v251, v242, v243\n"
"v_mfma_f32_16x16x32_bf16 v[60:63], a[0:3], v[172:175], v[60:63]\n"
"ds_write_b128 v212, v[248:251] offset:58368\n"
"v_cvt_pk_bf16_f32 v98, v188, v189\n"
"v_mfma_f32_16x16x32_bf16 v[72:75], v[156:159], v[168:171], v[72:75]\n"
"ds_read_b128 v[168:171], v194 offset:47104\n"
"ds_read_b64_tr_b16 a[84:85], v199 offset:16896\n"
"v_mfma_f32_16x16x32_bf16 v[72:75], a[4:7], v[172:175], v[72:75]\n"
"s_waitcnt vmcnt(4) lgkmcnt(10)\n"
"ds_read_b128 v[172:175], v194 offset:48128\n"
"ds_read_b64_tr_b16 a[126:127], v198 offset:6336\n"
"v_mfma_f32_16x16x32_bf16 v[44:47], a[16:19], v[160:163], v[44:47]\n"
"s_waitcnt lgkmcnt(10)\n"
"ds_read_b64_tr_b16 a[82:83], v198 offset:6344\n"
"v_mfma_f32_16x16x32_bf16 v[44:47], v[0:3], v[164:167], v[44:47]\n"
"v_cvt_pk_bf16_f32 v99, v190, v191\n"
"v_mfma_f32_16x16x32_bf16 v[56:59], a[20:23], v[160:163], v[56:59]\n"
"ds_read_b64_tr_b16 a[86:87], v199 offset:6336\n"
"v_mfma_f32_16x16x32_bf16 v[56:59], v[4:7], v[164:167], v[56:59]\n"
"buffer_store_dwordx4 v[96:99], v212, s[36:39], s47 offen sc1\n"
"v_mfma_f32_16x16x32_bf16 v[60:63], a[32:35], v[160:163], v[60:63]\n"
"v_mfma_f32_16x16x32_bf16 v[60:63], v[16:19], v[164:167], v[60:63]\n"
"v_mfma_f32_16x16x32_bf16 v[72:75], a[36:39], v[160:163], v[72:75]\n"
"s_waitcnt lgkmcnt(6)\n"
"s_barrier\n"
"ds_read_b128 v[160:163], v194 offset:49152\n"
"v_mfma_f32_16x16x32_bf16 v[72:75], v[20:23], v[164:167], v[72:75]\n"
"s_waitcnt lgkmcnt(6)\n"
"ds_read_b128 v[164:167], v194 offset:50176\n"
"v_mfma_f32_16x16x32_bf16 v[44:47], v[32:35], v[168:171], v[44:47]\n"
"s_waitcnt lgkmcnt(5)\n"
"v_cvt_pk_bf16_f32 v100, v8, v9\n"
"v_accvgpr_read_b32 v240, a224\n"
"v_mfma_f32_16x16x32_bf16 v[44:47], v[64:67], v[172:175], v[44:47]\n"
"v_cvt_pk_bf16_f32 v101, v10, v11\n"
"v_mfma_f32_16x16x32_bf16 v[56:59], v[36:39], v[168:171], v[56:59]\n"
"v_cvt_pk_bf16_f32 v102, v12, v13\n"
"v_cvt_pk_bf16_f32 v103, v14, v15\n"
"v_mfma_f32_16x16x32_bf16 v[56:59], v[68:71], v[172:175], v[56:59]\n"
"buffer_store_dwordx4 v[100:103], v212, s[36:39], s47 offen offset:1024 sc1\n"
"v_mfma_f32_16x16x32_bf16 v[60:63], v[48:51], v[168:171], v[60:63]\n"
"v_mfma_f32_16x16x32_bf16 v[60:63], v[80:83], v[172:175], v[60:63]\n"
"v_mfma_f32_16x16x32_bf16 v[72:75], v[52:55], v[168:171], v[72:75]\n"
"ds_read_b128 v[168:171], v194 offset:51200\n"
"v_accvgpr_read_b32 v241, a225\n"
"v_mfma_f32_16x16x32_bf16 v[72:75], v[84:87], v[172:175], v[72:75]\n"
"s_waitcnt lgkmcnt(2)\n"
"ds_read_b128 v[172:175], v194 offset:52224\n"
"v_mfma_f32_16x16x32_bf16 v[184:187], v[112:115], v[160:163], 0\n"
"s_waitcnt lgkmcnt(2)\n"
"v_mul_f32 v240, v240, v236\n"
"v_mul_f32 v241, v241, v236\n"
"v_mfma_f32_16x16x32_bf16 v[184:187], v[128:131], v[164:167], v[184:187]\n"
"v_cvt_pk_bf16_f32 v244, v240, v241\n"
"v_mfma_f32_16x16x32_bf16 v[188:191], v[116:119], v[160:163], 0\n"
"v_accvgpr_read_b32 v242, a226\n"
"v_accvgpr_read_b32 v243, a227\n"
"v_mfma_f32_16x16x32_bf16 v[188:191], v[132:135], v[164:167], v[188:191]\n"
"v_mul_f32 v242, v242, v236\n"
"v_mul_f32 v243, v243, v236\n"
"v_mfma_f32_16x16x32_bf16 v[8:11], v[120:123], v[160:163], 0\n"
"v_cvt_pk_bf16_f32 v245, v242, v243\n"
"v_mfma_f32_16x16x32_bf16 v[8:11], v[136:139], v[164:167], v[8:11]\n"
"v_accvgpr_read_b32 v240, a228\n"
"v_accvgpr_read_b32 v241, a229\n"
"v_mfma_f32_16x16x32_bf16 v[12:15], v[124:127], v[160:163], 0\n"
"s_mov_b32 s47, 0xa000\n"
"v_mul_f32 v240, v240, v236\n"
"v_mfma_f32_16x16x32_bf16 v[12:15], v[140:143], v[164:167], v[12:15]\n"
"v_mul_f32 v241, v241, v236\n"
"v_mfma_f32_16x16x32_bf16 v[24:27], a[8:11], v[160:163], 0\n"
"v_cvt_pk_bf16_f32 v104, v44, v45\n"
"v_cvt_pk_bf16_f32 v246, v240, v241\n"
"v_mfma_f32_16x16x32_bf16 v[24:27], a[28:31], v[164:167], v[24:27]\n"
"v_accvgpr_read_b32 v242, a230\n"
"v_accvgpr_read_b32 v243, a231\n"
"v_mfma_f32_16x16x32_bf16 v[28:31], a[12:15], v[160:163], 0\n"
"v_mul_f32 v242, v242, v236\n"
"v_mfma_f32_16x16x32_bf16 v[28:31], a[40:43], v[164:167], v[28:31]\n"
"v_cvt_pk_bf16_f32 v105, v46, v47\n"
"v_mul_f32 v243, v243, v236\n"
"v_mfma_f32_16x16x32_bf16 v[40:43], a[24:27], v[160:163], 0\n"
"ds_read_b128 v[160:163], v194 offset:53248\n"
"v_mfma_f32_16x16x32_bf16 v[40:43], a[44:47], v[164:167], v[40:43]\n"
"s_waitcnt lgkmcnt(2)\n"
"ds_read_b128 v[164:167], v194 offset:54272\n"
"v_cvt_pk_bf16_f32 v247, v242, v243\n"
"v_mfma_f32_16x16x32_bf16 v[184:187], v[144:147], v[168:171], v[184:187]\n"
"s_waitcnt lgkmcnt(2)\n"
"v_accvgpr_read_b32 v240, a232\n"
"v_accvgpr_read_b32 v241, a233\n"
"v_mfma_f32_16x16x32_bf16 v[184:187], v[176:179], v[172:175], v[184:187]\n"
"ds_write_b128 v213, v[244:247] offset:0\n"
"v_mfma_f32_16x16x32_bf16 v[188:191], v[148:151], v[168:171], v[188:191]\n"
"v_mfma_f32_16x16x32_bf16 v[188:191], v[180:183], v[172:175], v[188:191]\n"
"v_mfma_f32_16x16x32_bf16 v[8:11], v[152:155], v[168:171], v[8:11]\n"
"v_cvt_pk_bf16_f32 v106, v56, v57\n"
"v_mfma_f32_16x16x32_bf16 v[8:11], a[0:3], v[172:175], v[8:11]\n"
"v_mul_f32 v240, v240, v236\n"
"v_mul_f32 v241, v241, v236\n"
"v_mfma_f32_16x16x32_bf16 v[12:15], v[156:159], v[168:171], v[12:15]\n"
"v_cvt_pk_bf16_f32 v107, v58, v59\n"
"v_cvt_pk_bf16_f32 v248, v240, v241\n"
"v_mfma_f32_16x16x32_bf16 v[12:15], a[4:7], v[172:175], v[12:15]\n"
"buffer_store_dwordx4 v[104:107], v212, s[36:39], s47 offen sc1\n"
"v_mfma_f32_16x16x32_bf16 v[24:27], a[48:51], v[168:171], v[24:27]\n"
"v_mfma_f32_16x16x32_bf16 v[24:27], a[60:63], v[172:175], v[24:27]\n"
"v_mfma_f32_16x16x32_bf16 v[28:31], a[52:55], v[168:171], v[28:31]\n"
"v_accvgpr_read_b32 v242, a234\n"
"v_mfma_f32_16x16x32_bf16 v[28:31], a[64:67], v[172:175], v[28:31]\n"
"v_accvgpr_read_b32 v243, a235\n"
"v_mul_f32 v242, v242, v236\n"
"v_mfma_f32_16x16x32_bf16 v[40:43], a[56:59], v[168:171], v[40:43]\n"
"ds_read_b128 v[168:171], v194 offset:55296\n"
"v_mfma_f32_16x16x32_bf16 v[40:43], a[68:71], v[172:175], v[40:43]\n"
"s_waitcnt lgkmcnt(3)\n"
"ds_read_b128 v[172:175], v194 offset:56320\n"
"v_mul_f32 v243, v243, v236\n"
"v_mfma_f32_16x16x32_bf16 v[184:187], a[16:19], v[160:163], v[184:187]\n"
"s_waitcnt lgkmcnt(3)\n"
"v_cvt_pk_bf16_f32 v249, v242, v243\n"
"v_accvgpr_read_b32 v240, a236\n"
"v_mfma_f32_16x16x32_bf16 v[184:187], v[0:3], v[164:167], v[184:187]\n"
"v_accvgpr_read_b32 v241, a237\n"
"v_mfma_f32_16x16x32_bf16 v[188:191], a[20:23], v[160:163], v[188:191]\n"
"v_mul_f32 v240, v240, v236\n"
"v_mul_f32 v241, v241, v236\n"
"v_mfma_f32_16x16x32_bf16 v[188:191], v[4:7], v[164:167], v[188:191]\n"
"v_cvt_pk_bf16_f32 v250, v240, v241\n"
"v_accvgpr_read_b32 v242, a238\n"
"v_mfma_f32_16x16x32_bf16 v[8:11], a[32:35], v[160:163], v[8:11]\n"
"v_accvgpr_read_b32 v243, a239\n"
"v_mfma_f32_16x16x32_bf16 v[8:11], v[16:19], v[164:167], v[8:11]\n"
"v_mul_f32 v242, v242, v236\n"
"v_mul_f32 v243, v243, v236\n"
"v_mfma_f32_16x16x32_bf16 v[12:15], a[36:39], v[160:163], v[12:15]\n"
"v_cvt_pk_bf16_f32 v251, v242, v243\n"
"v_mfma_f32_16x16x32_bf16 v[12:15], v[20:23], v[164:167], v[12:15]\n"
"ds_write_b128 v213, v[248:251] offset:1024\n"
"v_mfma_f32_16x16x32_bf16 v[24:27], a[72:75], v[160:163], v[24:27]\n"
"v_mfma_f32_16x16x32_bf16 v[24:27], a[100:103], v[164:167], v[24:27]\n"
"v_mfma_f32_16x16x32_bf16 v[28:31], a[76:79], v[160:163], v[28:31]\n"
"v_cvt_pk_bf16_f32 v96, v60, v61\n"
"v_accvgpr_read_b32 v240, a240\n"
"v_mfma_f32_16x16x32_bf16 v[28:31], a[104:107], v[164:167], v[28:31]\n"
"v_accvgpr_read_b32 v241, a241\n"
"v_mul_f32 v240, v240, v237\n"
"v_mfma_f32_16x16x32_bf16 v[40:43], a[96:99], v[160:163], v[40:43]\n"
"ds_read_b128 v[160:163], v194 offset:57344\n"
"v_mfma_f32_16x16x32_bf16 v[40:43], a[108:111], v[164:167], v[40:43]\n"
".Lpad10_%=:\n"
"s_cbranch_vccnz .Lpad12_%=\n"
"s_waitcnt lgkmcnt(3)\n"
"ds_read_b128 v[164:167], v194 offset:58368\n"
"v_mul_f32 v241, v241, v237\n"
"v_mfma_f32_16x16x32_bf16 v[184:187], v[32:35], v[168:171], v[184:187]\n"
"s_waitcnt lgkmcnt(3)\n"
"v_cvt_pk_bf16_f32 v244, v240, v241\n"
"v_accvgpr_read_b32 v242, a242\n"
"v_mfma_f32_16x16x32_bf16 v[184:187], v[64:67], v[172:175], v[184:187]\n"
"v_accvgpr_read_b32 v243, a243\n"
"v_mfma_f32_16x16x32_bf16 v[188:191], v[36:39], v[168:171], v[188:191]\n"
"v_mul_f32 v242, v242, v237\n"
"v_mul_f32 v243, v243, v237\n"
"v_mfma_f32_16x16x32_bf16 v[188:191], v[68:71], v[172:175], v[188:191]\n"
"v_cvt_pk_bf16_f32 v245, v242, v243\n"
"v_mfma_f32_16x16x32_bf16 v[8:11], v[48:51], v[168:171], v[8:11]\n"
"v_cvt_pk_bf16_f32 v97, v62, v63\n"
"v_accvgpr_read_b32 v240, a244\n"
"v_mfma_f32_16x16x32_bf16 v[8:11], v[80:83], v[172:175], v[8:11]\n"
"v_accvgpr_read_b32 v241, a245\n"
"v_mul_f32 v240, v240, v237\n"
"v_mfma_f32_16x16x32_bf16 v[12:15], v[52:55], v[168:171], v[12:15]\n"
"v_cvt_pk_bf16_f32 v98, v72, v73\n"
"v_mfma_f32_16x16x32_bf16 v[12:15], v[84:87], v[172:175], v[12:15]\n"
"v_mul_f32 v241, v241, v237\n"
"v_cvt_pk_bf16_f32 v246, v240, v241\n"
"v_mfma_f32_16x16x32_bf16 v[24:27], a[112:115], v[168:171], v[24:27]\n"
"v_cvt_pk_bf16_f32 v99, v74, v75\n"
"v_accvgpr_read_b32 v242, a246\n"
"v_mfma_f32_16x16x32_bf16 v[24:27], a[124:127], v[172:175], v[24:27]\n"
"buffer_store_dwordx4 v[96:99], v212, s[36:39], s47 offen offset:1024 sc1\n"
"v_mfma_f32_16x16x32_bf16 v[28:31], a[116:119], v[168:171], v[28:31]\n"
"v_mfma_f32_16x16x32_bf16 v[28:31], a[80:83], v[172:175], v[28:31]\n"
"v_mfma_f32_16x16x32_bf16 v[40:43], a[120:123], v[168:171], v[40:43]\n"
"ds_read_b128 v[168:171], v194 offset:59392\n"
"v_mfma_f32_16x16x32_bf16 v[40:43], a[84:87], v[172:175], v[40:43]\n"
"s_waitcnt lgkmcnt(2)\n"
"ds_read_b128 v[172:175], v194 offset:60416\n"
"v_accvgpr_read_b32 v243, a247\n"
"v_mfma_f32_16x16x32_bf16 v[44:47], v[112:115], v[160:163], 0\n"
"s_waitcnt lgkmcnt(2)\n"
"v_mul_f32 v242, v242, v237\n"
"v_mfma_f32_16x16x32_bf16 v[44:47], v[128:131], v[164:167], v[44:47]\n"
"v_mul_f32 v243, v243, v237\n"
"v_cvt_pk_bf16_f32 v247, v242, v243\n"
"v_mfma_f32_16x16x32_bf16 v[56:59], v[116:119], v[160:163], 0\n"
"v_accvgpr_read_b32 v240, a248\n"
"v_accvgpr_read_b32 v241, a249\n"
"v_mfma_f32_16x16x32_bf16 v[56:59], v[132:135], v[164:167], v[56:59]\n"
"ds_write_b128 v213, v[244:247] offset:8192\n"
"v_mfma_f32_16x16x32_bf16 v[60:63], v[120:123], v[160:163], 0\n"
"v_mfma_f32_16x16x32_bf16 v[60:63], v[136:139], v[164:167], v[60:63]\n"
"s_mov_b32 s47, 0xc000\n"
"v_mfma_f32_16x16x32_bf16 v[72:75], v[124:127], v[160:163], 0\n"
"v_cvt_pk_bf16_f32 v100, v184, v185\n"
"v_mfma_f32_16x16x32_bf16 v[72:75], v[140:143], v[164:167], v[72:75]\n"
"v_mul_f32 v240, v240, v237\n"
"v_mfma_f32_16x16x32_bf16 v[76:79], a[8:11], v[160:163], 0\n"
"v_cvt_pk_bf16_f32 v101, v186, v187\n"
"v_mul_f32 v241, v241, v237\n"
"v_mfma_f32_16x16x32_bf16 v[76:79], a[28:31], v[164:167], v[76:79]\n"
"v_cvt_pk_bf16_f32 v102, v188, v189\n"
"v_mfma_f32_16x16x32_bf16 v[88:91], a[12:15], v[160:163], 0\n"
"v_cvt_pk_bf16_f32 v103, v190, v191\n"
"v_cvt_pk_bf16_f32 v248, v240, v241\n"
"v_mfma_f32_16x16x32_bf16 v[88:91], a[40:43], v[164:167], v[88:91]\n"
"buffer_store_dwordx4 v[100:103], v212, s[36:39], s47 offen sc1\n"
"v_mfma_f32_16x16x32_bf16 v[92:95], a[24:27], v[160:163], 0\n"
"ds_read_b128 v[160:163], v194 offset:61440\n"
"v_mfma_f32_16x16x32_bf16 v[92:95], a[44:47], v[164:167], v[92:95]\n"
"s_waitcnt lgkmcnt(3)\n"
"ds_read_b128 v[164:167], v194 offset:62464\n"
"v_mfma_f32_16x16x32_bf16 v[44:47], v[144:147], v[168:171], v[44:47]\n"
"s_waitcnt lgkmcnt(3)\n"
"v_mfma_f32_16x16x32_bf16 v[44:47], v[176:179], v[172:175], v[44:47]\n"
"v_accvgpr_read_b32 v242, a250\n"
"v_mfma_f32_16x16x32_bf16 v[56:59], v[148:151], v[168:171], v[56:59]\n"
"v_accvgpr_read_b32 v243, a251\n"
"v_mul_f32 v242, v242, v237\n"
"v_mfma_f32_16x16x32_bf16 v[56:59], v[180:183], v[172:175], v[56:59]\n"
"v_mul_f32 v243, v243, v237\n"
"v_mfma_f32_16x16x32_bf16 v[60:63], v[152:155], v[168:171], v[60:63]\n"
"v_cvt_pk_bf16_f32 v249, v242, v243\n"
"v_accvgpr_read_b32 v240, a252\n"
"v_mfma_f32_16x16x32_bf16 v[60:63], a[0:3], v[172:175], v[60:63]\n"
"v_accvgpr_read_b32 v241, a253\n"
"v_mul_f32 v240, v240, v237\n"
"v_mfma_f32_16x16x32_bf16 v[72:75], v[156:159], v[168:171], v[72:75]\n"
"v_cvt_pk_bf16_f32 v104, v8, v9\n"
"v_mfma_f32_16x16x32_bf16 v[72:75], a[4:7], v[172:175], v[72:75]\n"
"v_mul_f32 v241, v241, v237\n"
"v_cvt_pk_bf16_f32 v250, v240, v241\n"
"v_mfma_f32_16x16x32_bf16 v[76:79], a[48:51], v[168:171], v[76:79]\n"
"v_cvt_pk_bf16_f32 v105, v10, v11\n"
"v_mfma_f32_16x16x32_bf16 v[76:79], a[60:63], v[172:175], v[76:79]\n"
"v_cvt_pk_bf16_f32 v106, v12, v13\n"
"v_accvgpr_read_b32 v242, a254\n"
"v_mfma_f32_16x16x32_bf16 v[88:91], a[52:55], v[168:171], v[88:91]\n"
"v_accvgpr_read_b32 v243, a255\n"
"v_mul_f32 v242, v242, v237\n"
"v_mfma_f32_16x16x32_bf16 v[88:91], a[64:67], v[172:175], v[88:91]\n"
"v_cvt_pk_bf16_f32 v107, v14, v15\n"
"v_mfma_f32_16x16x32_bf16 v[92:95], a[56:59], v[168:171], v[92:95]\n"
"ds_read_b128 v[168:171], v194 offset:63488\n"
"v_mul_f32 v243, v243, v237\n"
"v_mfma_f32_16x16x32_bf16 v[92:95], a[68:71], v[172:175], v[92:95]\n"
"s_waitcnt lgkmcnt(2)\n"
"ds_read_b128 v[172:175], v194 offset:64512\n"
"v_cvt_pk_bf16_f32 v251, v242, v243\n"
"v_mfma_f32_16x16x32_bf16 v[44:47], a[16:19], v[160:163], v[44:47]\n"
"s_waitcnt lgkmcnt(2)\n"
"v_mfma_f32_16x16x32_bf16 v[44:47], v[0:3], v[164:167], v[44:47]\n"
"buffer_store_dwordx4 v[104:107], v212, s[36:39], s47 offen offset:1024 sc1\n"
"v_mfma_f32_16x16x32_bf16 v[56:59], a[20:23], v[160:163], v[56:59]\n"
"v_mfma_f32_16x16x32_bf16 v[56:59], v[4:7], v[164:167], v[56:59]\n"
"ds_write_b128 v213, v[248:251] offset:9216\n"
"v_mfma_f32_16x16x32_bf16 v[60:63], a[32:35], v[160:163], v[60:63]\n"
"v_mfma_f32_16x16x32_bf16 v[60:63], v[16:19], v[164:167], v[60:63]\n"
"v_mfma_f32_16x16x32_bf16 v[72:75], a[36:39], v[160:163], v[72:75]\n"
"v_mfma_f32_16x16x32_bf16 v[72:75], v[20:23], v[164:167], v[72:75]\n"
"v_mfma_f32_16x16x32_bf16 v[76:79], a[72:75], v[160:163], v[76:79]\n"
"s_mov_b32 s47, 0x1a000\n"
"v_mfma_f32_16x16x32_bf16 v[76:79], a[100:103], v[164:167], v[76:79]\n"
"v_cvt_pk_bf16_f32 v96, v24, v25\n"
"v_mfma_f32_16x16x32_bf16 v[88:91], a[76:79], v[160:163], v[88:91]\n"
"v_cvt_pk_bf16_f32 v97, v26, v27\n"
"v_mfma_f32_16x16x32_bf16 v[88:91], a[104:107], v[164:167], v[88:91]\n"
"v_mfma_f32_16x16x32_bf16 v[92:95], a[96:99], v[160:163], v[92:95]\n"
"s_waitcnt lgkmcnt(0)\n"
"s_barrier\n"
"ds_read_b128 v[160:163], v195 offset:0\n"
"v_mfma_f32_16x16x32_bf16 v[92:95], a[108:111], v[164:167], v[92:95]\n"
"ds_read_b128 v[164:167], v195 offset:1024\n"
"v_mfma_f32_16x16x32_bf16 v[44:47], v[32:35], v[168:171], v[44:47]\n"
"v_mfma_f32_16x16x32_bf16 v[44:47], v[64:67], v[172:175], v[44:47]\n"
"v_mfma_f32_16x16x32_bf16 v[56:59], v[36:39], v[168:171], v[56:59]\n"
"v_cvt_pk_bf16_f32 v98, v28, v29\n"
"v_mfma_f32_16x16x32_bf16 v[56:59], v[68:71], v[172:175], v[56:59]\n"
"v_cvt_pk_bf16_f32 v99, v30, v31\n"
"v_mfma_f32_16x16x32_bf16 v[60:63], v[48:51], v[168:171], v[60:63]\n"
"v_mfma_f32_16x16x32_bf16 v[60:63], v[80:83], v[172:175], v[60:63]\n"
"buffer_store_dwordx4 v[96:99], v214, s[36:39], s47 offen sc1\n"
"v_mfma_f32_16x16x32_bf16 v[72:75], v[52:55], v[168:171], v[72:75]\n"
"v_mfma_f32_16x16x32_bf16 v[72:75], v[84:87], v[172:175], v[72:75]\n"
"v_mfma_f32_16x16x32_bf16 v[76:79], a[112:115], v[168:171], v[76:79]\n"
"v_mfma_f32_16x16x32_bf16 v[76:79], a[124:127], v[172:175], v[76:79]\n"
"v_mfma_f32_16x16x32_bf16 v[88:91], a[116:119], v[168:171], v[88:91]\n"
"v_mfma_f32_16x16x32_bf16 v[88:91], a[80:83], v[172:175], v[88:91]\n"
"v_cvt_pk_bf16_f32 v108, v40, v41\n"
"v_cvt_pk_bf16_f32 v109, v42, v43\n"
"v_mfma_f32_16x16x32_bf16 v[92:95], a[120:123], v[168:171], v[92:95]\n"
"ds_read_b128 v[168:171], v195 offset:2048\n"
"v_mfma_f32_16x16x32_bf16 v[92:95], a[84:87], v[172:175], v[92:95]\n"
"s_waitcnt lgkmcnt(2)\n"
"ds_read_b128 v[172:175], v195 offset:3072\n"
"v_mfma_f32_16x16x32_bf16 v[184:187], v[112:115], v[160:163], 0\n"
"s_waitcnt lgkmcnt(2)\n"
"v_mfma_f32_16x16x32_bf16 v[184:187], v[128:131], v[164:167], v[184:187]\n"
"v_mfma_f32_16x16x32_bf16 v[188:191], v[116:119], v[160:163], 0\n"
"v_mfma_f32_16x16x32_bf16 v[188:191], v[132:135], v[164:167], v[188:191]\n"
"v_mfma_f32_16x16x32_bf16 v[8:11], v[120:123], v[160:163], 0\n"
"v_mfma_f32_16x16x32_bf16 v[8:11], v[136:139], v[164:167], v[8:11]\n"
"s_mov_b32 s47, 0xe000\n"
"v_mfma_f32_16x16x32_bf16 v[12:15], v[124:127], v[160:163], 0\n"
"v_cvt_pk_bf16_f32 v100, v44, v45\n"
"v_mfma_f32_16x16x32_bf16 v[12:15], v[140:143], v[164:167], v[12:15]\n"
"v_cvt_pk_bf16_f32 v101, v46, v47\n"
"v_mfma_f32_16x16x32_bf16 v[24:27], a[8:11], v[160:163], 0\n"
"v_cvt_pk_bf16_f32 v102, v56, v57\n"
"v_mfma_f32_16x16x32_bf16 v[24:27], a[28:31], v[164:167], v[24:27]\n"
"v_mfma_f32_16x16x32_bf16 v[28:31], a[12:15], v[160:163], 0\n"
"v_cvt_pk_bf16_f32 v103, v58, v59\n"
"v_mfma_f32_16x16x32_bf16 v[28:31], a[40:43], v[164:167], v[28:31]\n"
"buffer_store_dwordx4 v[100:103], v212, s[36:39], s47 offen sc1\n"
"v_mfma_f32_16x16x32_bf16 v[40:43], a[24:27], v[160:163], 0\n"
"ds_read_b128 v[160:163], v195 offset:4096\n"
"v_mfma_f32_16x16x32_bf16 v[40:43], a[44:47], v[164:167], v[40:43]\n"
"s_waitcnt lgkmcnt(2)\n"
"ds_read_b128 v[164:167], v195 offset:5120\n"
"v_mfma_f32_16x16x32_bf16 v[184:187], v[144:147], v[168:171], v[184:187]\n"
"s_waitcnt lgkmcnt(2)\n"
"v_mfma_f32_16x16x32_bf16 v[184:187], v[176:179], v[172:175], v[184:187]\n"
"v_mfma_f32_16x16x32_bf16 v[188:191], v[148:151], v[168:171], v[188:191]\n"
"v_mfma_f32_16x16x32_bf16 v[188:191], v[180:183], v[172:175], v[188:191]\n"
"v_mfma_f32_16x16x32_bf16 v[8:11], v[152:155], v[168:171], v[8:11]\n"
"v_mfma_f32_16x16x32_bf16 v[8:11], a[0:3], v[172:175], v[8:11]\n"
"v_mfma_f32_16x16x32_bf16 v[12:15], v[156:159], v[168:171], v[12:15]\n"
"v_cvt_pk_bf16_f32 v104, v60, v61\n"
"v_mfma_f32_16x16x32_bf16 v[12:15], a[4:7], v[172:175], v[12:15]\n"
"v_cvt_pk_bf16_f32 v105, v62, v63\n"
"v_mfma_f32_16x16x32_bf16 v[24:27], a[48:51], v[168:171], v[24:27]\n"
"v_cvt_pk_bf16_f32 v106, v72, v73\n"
"v_mfma_f32_16x16x32_bf16 v[24:27], a[60:63], v[172:175], v[24:27]\n"
"v_cvt_pk_bf16_f32 v107, v74, v75\n"
"v_mfma_f32_16x16x32_bf16 v[28:31], a[52:55], v[168:171], v[28:31]\n"
"v_mfma_f32_16x16x32_bf16 v[28:31], a[64:67], v[172:175], v[28:31]\n"
"buffer_store_dwordx4 v[104:107], v212, s[36:39], s47 offen offset:1024 sc1\n"
"v_mfma_f32_16x16x32_bf16 v[40:43], a[56:59], v[168:171], v[40:43]\n"
"ds_read_b128 v[168:171], v195 offset:6144\n"
"v_mfma_f32_16x16x32_bf16 v[40:43], a[68:71], v[172:175], v[40:43]\n"
"s_waitcnt lgkmcnt(2)\n"
"ds_read_b128 v[172:175], v195 offset:7168\n"
"v_mfma_f32_16x16x32_bf16 v[184:187], a[16:19], v[160:163], v[184:187]\n"
"s_waitcnt lgkmcnt(2)\n"
"v_mfma_f32_16x16x32_bf16 v[184:187], v[0:3], v[164:167], v[184:187]\n"
"v_mfma_f32_16x16x32_bf16 v[188:191], a[20:23], v[160:163], v[188:191]\n"
"v_mfma_f32_16x16x32_bf16 v[188:191], v[4:7], v[164:167], v[188:191]\n"
"v_mfma_f32_16x16x32_bf16 v[8:11], a[32:35], v[160:163], v[8:11]\n"
"v_mfma_f32_16x16x32_bf16 v[8:11], v[16:19], v[164:167], v[8:11]\n"
"s_mov_b32 s47, 0x1b000\n"
"v_mfma_f32_16x16x32_bf16 v[12:15], a[36:39], v[160:163], v[12:15]\n"
"v_mfma_f32_16x16x32_bf16 v[12:15], v[20:23], v[164:167], v[12:15]\n"
"v_cvt_pk_bf16_f32 v96, v76, v77\n"
"v_mfma_f32_16x16x32_bf16 v[24:27], a[72:75], v[160:163], v[24:27]\n"
"v_cvt_pk_bf16_f32 v97, v78, v79\n"
"v_mfma_f32_16x16x32_bf16 v[24:27], a[100:103], v[164:167], v[24:27]\n"
"v_cvt_pk_bf16_f32 v98, v88, v89\n"
"v_mfma_f32_16x16x32_bf16 v[28:31], a[76:79], v[160:163], v[28:31]\n"
"v_cvt_pk_bf16_f32 v99, v90, v91\n"
"v_mfma_f32_16x16x32_bf16 v[28:31], a[104:107], v[164:167], v[28:31]\n"
"buffer_store_dwordx4 v[96:99], v214, s[36:39], s47 offen sc1\n"
"v_mfma_f32_16x16x32_bf16 v[40:43], a[96:99], v[160:163], v[40:43]\n"
"ds_read_b128 v[160:163], v195 offset:8192\n"
"v_mfma_f32_16x16x32_bf16 v[40:43], a[108:111], v[164:167], v[40:43]\n"
"s_waitcnt lgkmcnt(2)\n"
"ds_read_b128 v[164:167], v195 offset:9216\n"
"v_mfma_f32_16x16x32_bf16 v[184:187], v[32:35], v[168:171], v[184:187]\n"
"s_waitcnt lgkmcnt(2)\n"
"v_mfma_f32_16x16x32_bf16 v[184:187], v[64:67], v[172:175], v[184:187]\n"
"v_mfma_f32_16x16x32_bf16 v[188:191], v[36:39], v[168:171], v[188:191]\n"
"v_mfma_f32_16x16x32_bf16 v[188:191], v[68:71], v[172:175], v[188:191]\n"
"v_mfma_f32_16x16x32_bf16 v[8:11], v[48:51], v[168:171], v[8:11]\n"
"v_mfma_f32_16x16x32_bf16 v[8:11], v[80:83], v[172:175], v[8:11]\n"
"v_mfma_f32_16x16x32_bf16 v[12:15], v[52:55], v[168:171], v[12:15]\n"
"v_cvt_pk_bf16_f32 v110, v92, v93\n"
"v_mfma_f32_16x16x32_bf16 v[12:15], v[84:87], v[172:175], v[12:15]\n"
"v_cvt_pk_bf16_f32 v111, v94, v95\n"
"v_mfma_f32_16x16x32_bf16 v[24:27], a[112:115], v[168:171], v[24:27]\n"
"s_mov_b32 s47, 0x21000\n"
"v_mfma_f32_16x16x32_bf16 v[24:27], a[124:127], v[172:175], v[24:27]\n"
"buffer_store_dwordx4 v[108:111], v214, s[36:39], s47 offen sc1\n"
"v_mfma_f32_16x16x32_bf16 v[28:31], a[116:119], v[168:171], v[28:31]\n"
"v_mfma_f32_16x16x32_bf16 v[28:31], a[80:83], v[172:175], v[28:31]\n"
"v_mfma_f32_16x16x32_bf16 v[40:43], a[120:123], v[168:171], v[40:43]\n"
"ds_read_b128 v[168:171], v195 offset:10240\n"
"v_mfma_f32_16x16x32_bf16 v[40:43], a[84:87], v[172:175], v[40:43]\n"
"s_waitcnt lgkmcnt(2)\n"
"ds_read_b128 v[172:175], v195 offset:11264\n"
"v_mfma_f32_16x16x32_bf16 v[44:47], v[112:115], v[160:163], 0\n"
"s_waitcnt lgkmcnt(2)\n"
"v_mfma_f32_16x16x32_bf16 v[44:47], v[128:131], v[164:167], v[44:47]\n"
"v_mfma_f32_16x16x32_bf16 v[56:59], v[116:119], v[160:163], 0\n"
"v_mfma_f32_16x16x32_bf16 v[56:59], v[132:135], v[164:167], v[56:59]\n"
"v_mfma_f32_16x16x32_bf16 v[60:63], v[120:123], v[160:163], 0\n"
"v_mfma_f32_16x16x32_bf16 v[60:63], v[136:139], v[164:167], v[60:63]\n"
"s_mov_b32 s47, 0x10000\n"
"v_mfma_f32_16x16x32_bf16 v[72:75], v[124:127], v[160:163], 0\n"
"v_cvt_pk_bf16_f32 v100, v184, v185\n"
"v_mfma_f32_16x16x32_bf16 v[72:75], v[140:143], v[164:167], v[72:75]\n"
"v_mfma_f32_16x16x32_bf16 v[76:79], a[8:11], v[160:163], 0\n"
"v_cvt_pk_bf16_f32 v101, v186, v187\n"
"v_mfma_f32_16x16x32_bf16 v[76:79], a[28:31], v[164:167], v[76:79]\n"
"v_cvt_pk_bf16_f32 v102, v188, v189\n"
"v_mfma_f32_16x16x32_bf16 v[88:91], a[12:15], v[160:163], 0\n"
"v_cvt_pk_bf16_f32 v103, v190, v191\n"
"v_mfma_f32_16x16x32_bf16 v[88:91], a[40:43], v[164:167], v[88:91]\n"
"buffer_store_dwordx4 v[100:103], v212, s[36:39], s47 offen sc1\n"
"v_mfma_f32_16x16x32_bf16 v[92:95], a[24:27], v[160:163], 0\n"
"ds_read_b128 v[160:163], v195 offset:12288\n"
"v_mfma_f32_16x16x32_bf16 v[92:95], a[44:47], v[164:167], v[92:95]\n"
"s_waitcnt lgkmcnt(2)\n"
"ds_read_b128 v[164:167], v195 offset:13312\n"
"v_mfma_f32_16x16x32_bf16 v[44:47], v[144:147], v[168:171], v[44:47]\n"
"s_waitcnt lgkmcnt(2)\n"
"v_mfma_f32_16x16x32_bf16 v[44:47], v[176:179], v[172:175], v[44:47]\n"
"v_mfma_f32_16x16x32_bf16 v[56:59], v[148:151], v[168:171], v[56:59]\n"
"v_mfma_f32_16x16x32_bf16 v[56:59], v[180:183], v[172:175], v[56:59]\n"
"v_mfma_f32_16x16x32_bf16 v[60:63], v[152:155], v[168:171], v[60:63]\n"
"v_mfma_f32_16x16x32_bf16 v[60:63], a[0:3], v[172:175], v[60:63]\n"
"v_mfma_f32_16x16x32_bf16 v[72:75], v[156:159], v[168:171], v[72:75]\n"
"v_cvt_pk_bf16_f32 v104, v8, v9\n"
"v_mfma_f32_16x16x32_bf16 v[72:75], a[4:7], v[172:175], v[72:75]\n"
"v_mfma_f32_16x16x32_bf16 v[76:79], a[48:51], v[168:171], v[76:79]\n"
"v_cvt_pk_bf16_f32 v105, v10, v11\n"
"v_mfma_f32_16x16x32_bf16 v[76:79], a[60:63], v[172:175], v[76:79]\n"
"v_cvt_pk_bf16_f32 v106, v12, v13\n"
"v_mfma_f32_16x16x32_bf16 v[88:91], a[52:55], v[168:171], v[88:91]\n"
"v_mfma_f32_16x16x32_bf16 v[88:91], a[64:67], v[172:175], v[88:91]\n"
"v_cvt_pk_bf16_f32 v107, v14, v15\n"
"v_mfma_f32_16x16x32_bf16 v[92:95], a[56:59], v[168:171], v[92:95]\n"
"ds_read_b128 v[168:171], v195 offset:14336\n"
"v_mfma_f32_16x16x32_bf16 v[92:95], a[68:71], v[172:175], v[92:95]\n"
"s_waitcnt lgkmcnt(2)\n"
"ds_read_b128 v[172:175], v195 offset:15360\n"
"v_mfma_f32_16x16x32_bf16 v[44:47], a[16:19], v[160:163], v[44:47]\n"
"s_waitcnt lgkmcnt(2)\n"
"v_mfma_f32_16x16x32_bf16 v[44:47], v[0:3], v[164:167], v[44:47]\n"
"buffer_store_dwordx4 v[104:107], v212, s[36:39], s47 offen offset:1024 sc1\n"
"v_mfma_f32_16x16x32_bf16 v[56:59], a[20:23], v[160:163], v[56:59]\n"
"v_mfma_f32_16x16x32_bf16 v[56:59], v[4:7], v[164:167], v[56:59]\n"
"v_mfma_f32_16x16x32_bf16 v[60:63], a[32:35], v[160:163], v[60:63]\n"
"v_mfma_f32_16x16x32_bf16 v[60:63], v[16:19], v[164:167], v[60:63]\n"
"v_mfma_f32_16x16x32_bf16 v[72:75], a[36:39], v[160:163], v[72:75]\n"
"v_mfma_f32_16x16x32_bf16 v[72:75], v[20:23], v[164:167], v[72:75]\n"
"v_mfma_f32_16x16x32_bf16 v[76:79], a[72:75], v[160:163], v[76:79]\n"
"s_mov_b32 s47, 0x1c000\n"
"v_mfma_f32_16x16x32_bf16 v[76:79], a[100:103], v[164:167], v[76:79]\n"
"v_cvt_pk_bf16_f32 v96, v24, v25\n"
"v_mfma_f32_16x16x32_bf16 v[88:91], a[76:79], v[160:163], v[88:91]\n"
"v_mfma_f32_16x16x32_bf16 v[88:91], a[104:107], v[164:167], v[88:91]\n"
"v_cvt_pk_bf16_f32 v97, v26, v27\n"
"v_mfma_f32_16x16x32_bf16 v[92:95], a[96:99], v[160:163], v[92:95]\n"
"ds_read_b128 v[160:163], v194 offset:0\n"
"v_mfma_f32_16x16x32_bf16 v[92:95], a[108:111], v[164:167], v[92:95]\n"
"s_waitcnt lgkmcnt(2)\n"
"ds_read_b128 v[164:167], v194 offset:1024\n"
"v_mfma_f32_16x16x32_bf16 v[44:47], v[32:35], v[168:171], v[44:47]\n"
"s_waitcnt lgkmcnt(2)\n"
"v_mfma_f32_16x16x32_bf16 v[44:47], v[64:67], v[172:175], v[44:47]\n"
"v_cvt_pk_bf16_f32 v98, v28, v29\n"
"v_mfma_f32_16x16x32_bf16 v[56:59], v[36:39], v[168:171], v[56:59]\n"
"v_cvt_pk_bf16_f32 v99, v30, v31\n"
"v_mfma_f32_16x16x32_bf16 v[56:59], v[68:71], v[172:175], v[56:59]\n"
"buffer_store_dwordx4 v[96:99], v214, s[36:39], s47 offen sc1\n"
"v_mfma_f32_16x16x32_bf16 v[60:63], v[48:51], v[168:171], v[60:63]\n"
"v_mfma_f32_16x16x32_bf16 v[60:63], v[80:83], v[172:175], v[60:63]\n"
"v_mfma_f32_16x16x32_bf16 v[72:75], v[52:55], v[168:171], v[72:75]\n"
"v_mfma_f32_16x16x32_bf16 v[72:75], v[84:87], v[172:175], v[72:75]\n"
"v_mfma_f32_16x16x32_bf16 v[76:79], a[112:115], v[168:171], v[76:79]\n"
"v_mfma_f32_16x16x32_bf16 v[76:79], a[124:127], v[172:175], v[76:79]\n"
"v_mfma_f32_16x16x32_bf16 v[88:91], a[116:119], v[168:171], v[88:91]\n"
"v_cvt_pk_bf16_f32 v108, v40, v41\n"
"v_mfma_f32_16x16x32_bf16 v[88:91], a[80:83], v[172:175], v[88:91]\n"
"v_cvt_pk_bf16_f32 v109, v42, v43\n"
"v_mfma_f32_16x16x32_bf16 v[92:95], a[120:123], v[168:171], v[92:95]\n"
"ds_read_b128 v[168:171], v194 offset:2048\n"
"v_mfma_f32_16x16x32_bf16 v[92:95], a[84:87], v[172:175], v[92:95]\n"
"s_waitcnt lgkmcnt(2)\n"
"ds_read_b128 v[172:175], v194 offset:3072\n"
"v_mfma_f32_16x16x32_bf16 v[24:27], a[8:11], v[160:163], 0\n"
"s_waitcnt lgkmcnt(2)\n"
"v_mfma_f32_16x16x32_bf16 v[24:27], a[28:31], v[164:167], v[24:27]\n"
"v_mfma_f32_16x16x32_bf16 v[28:31], a[12:15], v[160:163], 0\n"
"v_mfma_f32_16x16x32_bf16 v[28:31], a[40:43], v[164:167], v[28:31]\n"
"v_mfma_f32_16x16x32_bf16 v[40:43], a[24:27], v[160:163], 0\n"
"ds_read_b128 v[160:163], v194 offset:4096\n"
"s_mov_b32 s47, 0x12000\n"
"v_mfma_f32_16x16x32_bf16 v[40:43], a[44:47], v[164:167], v[40:43]\n"
"s_waitcnt lgkmcnt(2)\n"
"ds_read_b128 v[164:167], v194 offset:5120\n"
"v_cvt_pk_bf16_f32 v100, v44, v45\n"
"v_cvt_pk_bf16_f32 v101, v46, v47\n"
"v_cvt_pk_bf16_f32 v102, v56, v57\n"
"v_mfma_f32_16x16x32_bf16 v[24:27], a[48:51], v[168:171], v[24:27]\n"
"s_waitcnt lgkmcnt(2)\n"
"v_cvt_pk_bf16_f32 v103, v58, v59\n"
"v_cvt_pk_bf16_f32 v104, v60, v61\n"
"v_mfma_f32_16x16x32_bf16 v[24:27], a[60:63], v[172:175], v[24:27]\n"
"buffer_store_dwordx4 v[100:103], v212, s[36:39], s47 offen sc1\n"
"v_mfma_f32_16x16x32_bf16 v[28:31], a[52:55], v[168:171], v[28:31]\n"
"v_cvt_pk_bf16_f32 v105, v62, v63\n"
"v_cvt_pk_bf16_f32 v106, v72, v73\n"
"v_mfma_f32_16x16x32_bf16 v[28:31], a[64:67], v[172:175], v[28:31]\n"
"v_cvt_pk_bf16_f32 v107, v74, v75\n"
"v_mfma_f32_16x16x32_bf16 v[40:43], a[56:59], v[168:171], v[40:43]\n"
"ds_read_b128 v[168:171], v194 offset:6144\n"
"v_mfma_f32_16x16x32_bf16 v[40:43], a[68:71], v[172:175], v[40:43]\n"
"s_waitcnt lgkmcnt(2)\n"
"ds_read_b128 v[172:175], v194 offset:7168\n"
"v_mfma_f32_16x16x32_bf16 v[24:27], a[72:75], v[160:163], v[24:27]\n"
"s_waitcnt lgkmcnt(2)\n"
"v_mfma_f32_16x16x32_bf16 v[24:27], a[100:103], v[164:167], v[24:27]\n"
"buffer_store_dwordx4 v[104:107], v212, s[36:39], s47 offen offset:1024 sc1\n"
"s_mov_b32 s47, 0x1d000\n"
"v_mfma_f32_16x16x32_bf16 v[28:31], a[76:79], v[160:163], v[28:31]\n"
"v_cvt_pk_bf16_f32 v96, v76, v77\n"
"v_cvt_pk_bf16_f32 v97, v78, v79\n"
"v_mfma_f32_16x16x32_bf16 v[28:31], a[104:107], v[164:167], v[28:31]\n"
"v_cvt_pk_bf16_f32 v98, v88, v89\n"
"v_cvt_pk_bf16_f32 v99, v90, v91\n"
"v_cvt_pk_bf16_f32 v110, v92, v93\n"
"v_cvt_pk_bf16_f32 v111, v94, v95\n"
"v_mfma_f32_16x16x32_bf16 v[40:43], a[96:99], v[160:163], v[40:43]\n"
"ds_read_b128 v[160:163], v194 offset:8192\n"
"v_mfma_f32_16x16x32_bf16 v[40:43], a[108:111], v[164:167], v[40:43]\n"
"s_waitcnt lgkmcnt(2)\n"
"ds_read_b128 v[164:167], v194 offset:9216\n"
"v_mfma_f32_16x16x32_bf16 v[24:27], a[112:115], v[168:171], v[24:27]\n"
"s_waitcnt lgkmcnt(2)\n"
"v_mfma_f32_16x16x32_bf16 v[24:27], a[124:127], v[172:175], v[24:27]\n"
"buffer_store_dwordx4 v[96:99], v214, s[36:39], s47 offen sc1\n"
"s_mov_b32 s47, 0x22000\n"
"v_mfma_f32_16x16x32_bf16 v[28:31], a[116:119], v[168:171], v[28:31]\n"
"v_mfma_f32_16x16x32_bf16 v[28:31], a[80:83], v[172:175], v[28:31]\n"
"buffer_store_dwordx4 v[108:111], v214, s[36:39], s47 offen sc1\n"
"v_mfma_f32_16x16x32_bf16 v[40:43], a[120:123], v[168:171], v[40:43]\n"
"ds_read_b128 v[168:171], v194 offset:10240\n"
"v_mfma_f32_16x16x32_bf16 v[40:43], a[84:87], v[172:175], v[40:43]\n"
"s_waitcnt lgkmcnt(2)\n"
"ds_read_b128 v[172:175], v194 offset:11264\n"
"v_mfma_f32_16x16x32_bf16 v[76:79], a[8:11], v[160:163], 0\n"
"s_waitcnt lgkmcnt(2)\n"
"v_mfma_f32_16x16x32_bf16 v[76:79], a[28:31], v[164:167], v[76:79]\n"
"v_mfma_f32_16x16x32_bf16 v[88:91], a[12:15], v[160:163], 0\n"
"v_mfma_f32_16x16x32_bf16 v[88:91], a[40:43], v[164:167], v[88:91]\n"
"v_mfma_f32_16x16x32_bf16 v[92:95], a[24:27], v[160:163], 0\n"
"ds_read_b128 v[160:163], v194 offset:12288\n"
"v_mfma_f32_16x16x32_bf16 v[92:95], a[44:47], v[164:167], v[92:95]\n"
"s_waitcnt lgkmcnt(2)\n"
"ds_read_b128 v[164:167], v194 offset:13312\n"
"v_mfma_f32_16x16x32_bf16 v[76:79], a[48:51], v[168:171], v[76:79]\n"
"s_waitcnt lgkmcnt(2)\n"
"v_mfma_f32_16x16x32_bf16 v[76:79], a[60:63], v[172:175], v[76:79]\n"
"s_mov_b32 s47, 0x14000\n"
"v_mfma_f32_16x16x32_bf16 v[88:91], a[52:55], v[168:171], v[88:91]\n"
"v_cvt_pk_bf16_f32 v100, v24, v25\n"
"v_mfma_f32_16x16x32_bf16 v[88:91], a[64:67], v[172:175], v[88:91]\n"
"v_cvt_pk_bf16_f32 v101, v26, v27\n"
"v_mfma_f32_16x16x32_bf16 v[92:95], a[56:59], v[168:171], v[92:95]\n"
"ds_read_b128 v[168:171], v194 offset:14336\n"
"v_mfma_f32_16x16x32_bf16 v[92:95], a[68:71], v[172:175], v[92:95]\n"
"s_waitcnt lgkmcnt(2)\n"
"ds_read_b128 v[172:175], v194 offset:15360\n"
"v_mfma_f32_16x16x32_bf16 v[76:79], a[72:75], v[160:163], v[76:79]\n"
"s_waitcnt lgkmcnt(2)\n"
"v_cvt_pk_bf16_f32 v102, v28, v29\n"
"v_mfma_f32_16x16x32_bf16 v[76:79], a[100:103], v[164:167], v[76:79]\n"
"v_cvt_pk_bf16_f32 v103, v30, v31\n"
"v_mfma_f32_16x16x32_bf16 v[88:91], a[76:79], v[160:163], v[88:91]\n"
"v_cvt_pk_bf16_f32 v108, v40, v41\n"
"v_mfma_f32_16x16x32_bf16 v[88:91], a[104:107], v[164:167], v[88:91]\n"
"buffer_store_dwordx4 v[100:103], v214, s[36:39], s47 offen sc1\n"
"v_mfma_f32_16x16x32_bf16 v[92:95], a[96:99], v[160:163], v[92:95]\n"
"ds_read_b128 v[160:163], v194 offset:16384\n"
"v_mfma_f32_16x16x32_bf16 v[92:95], a[108:111], v[164:167], v[92:95]\n"
"s_waitcnt lgkmcnt(2)\n"
"ds_read_b128 v[164:167], v194 offset:17408\n"
"v_mfma_f32_16x16x32_bf16 v[76:79], a[112:115], v[168:171], v[76:79]\n"
"s_waitcnt lgkmcnt(2)\n"
"v_mfma_f32_16x16x32_bf16 v[76:79], a[124:127], v[172:175], v[76:79]\n"
"v_mfma_f32_16x16x32_bf16 v[88:91], a[116:119], v[168:171], v[88:91]\n"
"v_mfma_f32_16x16x32_bf16 v[88:91], a[80:83], v[172:175], v[88:91]\n"
"v_cvt_pk_bf16_f32 v109, v42, v43\n"
"v_mfma_f32_16x16x32_bf16 v[92:95], a[120:123], v[168:171], v[92:95]\n"
"ds_read_b128 v[168:171], v194 offset:18432\n"
"v_mfma_f32_16x16x32_bf16 v[92:95], a[84:87], v[172:175], v[92:95]\n"
"s_waitcnt lgkmcnt(2)\n"
"ds_read_b128 v[172:175], v194 offset:19456\n"
"v_mfma_f32_16x16x32_bf16 v[24:27], a[8:11], v[160:163], 0\n"
"s_waitcnt lgkmcnt(2)\n"
"v_mfma_f32_16x16x32_bf16 v[24:27], a[28:31], v[164:167], v[24:27]\n"
"v_mfma_f32_16x16x32_bf16 v[28:31], a[12:15], v[160:163], 0\n"
"v_mfma_f32_16x16x32_bf16 v[28:31], a[40:43], v[164:167], v[28:31]\n"
"v_mfma_f32_16x16x32_bf16 v[40:43], a[24:27], v[160:163], 0\n"
"ds_read_b128 v[160:163], v194 offset:20480\n"
"v_mfma_f32_16x16x32_bf16 v[40:43], a[44:47], v[164:167], v[40:43]\n"
"s_waitcnt lgkmcnt(2)\n"
"ds_read_b128 v[164:167], v194 offset:21504\n"
"v_mfma_f32_16x16x32_bf16 v[24:27], a[48:51], v[168:171], v[24:27]\n"
"s_waitcnt lgkmcnt(2)\n"
"s_mov_b32 s47, 0x15000\n"
"v_mfma_f32_16x16x32_bf16 v[24:27], a[60:63], v[172:175], v[24:27]\n"
"v_cvt_pk_bf16_f32 v104, v76, v77\n"
"v_cvt_pk_bf16_f32 v105, v78, v79\n"
"v_mfma_f32_16x16x32_bf16 v[28:31], a[52:55], v[168:171], v[28:31]\n"
"v_cvt_pk_bf16_f32 v106, v88, v89\n"
"v_mfma_f32_16x16x32_bf16 v[28:31], a[64:67], v[172:175], v[28:31]\n"
"v_cvt_pk_bf16_f32 v107, v90, v91\n"
"v_mfma_f32_16x16x32_bf16 v[40:43], a[56:59], v[168:171], v[40:43]\n"
"ds_read_b128 v[168:171], v194 offset:22528\n"
"v_mfma_f32_16x16x32_bf16 v[40:43], a[68:71], v[172:175], v[40:43]\n"
"s_waitcnt lgkmcnt(2)\n"
"ds_read_b128 v[172:175], v194 offset:23552\n"
"v_cvt_pk_bf16_f32 v110, v92, v93\n"
"v_mfma_f32_16x16x32_bf16 v[24:27], a[72:75], v[160:163], v[24:27]\n"
"s_waitcnt lgkmcnt(2)\n"
"v_cvt_pk_bf16_f32 v111, v94, v95\n"
"v_mfma_f32_16x16x32_bf16 v[24:27], a[100:103], v[164:167], v[24:27]\n"
"buffer_store_dwordx4 v[104:107], v214, s[36:39], s47 offen sc1\n"
"v_mfma_f32_16x16x32_bf16 v[28:31], a[76:79], v[160:163], v[28:31]\n"
"v_mfma_f32_16x16x32_bf16 v[28:31], a[104:107], v[164:167], v[28:31]\n"
"v_mfma_f32_16x16x32_bf16 v[40:43], a[96:99], v[160:163], v[40:43]\n"
"ds_read_b128 v[160:163], v194 offset:24576\n"
"v_mfma_f32_16x16x32_bf16 v[40:43], a[108:111], v[164:167], v[40:43]\n"
"s_waitcnt lgkmcnt(2)\n"
"ds_read_b128 v[164:167], v194 offset:25600\n"
"v_mfma_f32_16x16x32_bf16 v[24:27], a[112:115], v[168:171], v[24:27]\n"
"s_waitcnt lgkmcnt(2)\n"
"s_mov_b32 s47, 0x1e000\n"
"v_mfma_f32_16x16x32_bf16 v[24:27], a[124:127], v[172:175], v[24:27]\n"
"buffer_store_dwordx4 v[108:111], v214, s[36:39], s47 offen sc1\n"
"v_mfma_f32_16x16x32_bf16 v[28:31], a[116:119], v[168:171], v[28:31]\n"
"v_mfma_f32_16x16x32_bf16 v[28:31], a[80:83], v[172:175], v[28:31]\n"
"v_mfma_f32_16x16x32_bf16 v[40:43], a[120:123], v[168:171], v[40:43]\n"
"ds_read_b128 v[168:171], v194 offset:26624\n"
"v_mfma_f32_16x16x32_bf16 v[40:43], a[84:87], v[172:175], v[40:43]\n"
"s_waitcnt lgkmcnt(2)\n"
"ds_read_b128 v[172:175], v194 offset:27648\n"
"v_mfma_f32_16x16x32_bf16 v[76:79], a[8:11], v[160:163], 0\n"
"s_waitcnt lgkmcnt(2)\n"
"v_mfma_f32_16x16x32_bf16 v[76:79], a[28:31], v[164:167], v[76:79]\n"
"v_mfma_f32_16x16x32_bf16 v[88:91], a[12:15], v[160:163], 0\n"
"v_mfma_f32_16x16x32_bf16 v[88:91], a[40:43], v[164:167], v[88:91]\n"
"v_mfma_f32_16x16x32_bf16 v[92:95], a[24:27], v[160:163], 0\n"
"ds_read_b128 v[160:163], v194 offset:28672\n"
"v_mfma_f32_16x16x32_bf16 v[92:95], a[44:47], v[164:167], v[92:95]\n"
"s_waitcnt lgkmcnt(2)\n"
"ds_read_b128 v[164:167], v194 offset:29696\n"
"v_mfma_f32_16x16x32_bf16 v[76:79], a[48:51], v[168:171], v[76:79]\n"
"s_waitcnt lgkmcnt(2)\n"
"v_mfma_f32_16x16x32_bf16 v[76:79], a[60:63], v[172:175], v[76:79]\n"
"s_mov_b32 s47, 0x16000\n"
"v_mfma_f32_16x16x32_bf16 v[88:91], a[52:55], v[168:171], v[88:91]\n"
"v_cvt_pk_bf16_f32 v96, v24, v25\n"
"v_mfma_f32_16x16x32_bf16 v[88:91], a[64:67], v[172:175], v[88:91]\n"
"v_cvt_pk_bf16_f32 v97, v26, v27\n"
"v_mfma_f32_16x16x32_bf16 v[92:95], a[56:59], v[168:171], v[92:95]\n"
"ds_read_b128 v[168:171], v194 offset:30720\n"
"v_mfma_f32_16x16x32_bf16 v[92:95], a[68:71], v[172:175], v[92:95]\n"
"s_waitcnt lgkmcnt(2)\n"
"ds_read_b128 v[172:175], v194 offset:31744\n"
"v_mfma_f32_16x16x32_bf16 v[76:79], a[72:75], v[160:163], v[76:79]\n"
"s_waitcnt lgkmcnt(2)\n"
"v_cvt_pk_bf16_f32 v98, v28, v29\n"
"v_mfma_f32_16x16x32_bf16 v[76:79], a[100:103], v[164:167], v[76:79]\n"
"v_cvt_pk_bf16_f32 v99, v30, v31\n"
"v_mfma_f32_16x16x32_bf16 v[88:91], a[76:79], v[160:163], v[88:91]\n"
"v_cvt_pk_bf16_f32 v108, v40, v41\n"
"v_mfma_f32_16x16x32_bf16 v[88:91], a[104:107], v[164:167], v[88:91]\n"
"buffer_store_dwordx4 v[96:99], v214, s[36:39], s47 offen sc1\n"
"v_mfma_f32_16x16x32_bf16 v[92:95], a[96:99], v[160:163], v[92:95]\n"
"ds_read_b128 v[160:163], v194 offset:32768\n"
"v_mfma_f32_16x16x32_bf16 v[92:95], a[108:111], v[164:167], v[92:95]\n"
"s_waitcnt lgkmcnt(2)\n"
"ds_read_b128 v[164:167], v194 offset:33792\n"
"v_mfma_f32_16x16x32_bf16 v[76:79], a[112:115], v[168:171], v[76:79]\n"
"s_waitcnt lgkmcnt(2)\n"
"v_mfma_f32_16x16x32_bf16 v[76:79], a[124:127], v[172:175], v[76:79]\n"
"v_mfma_f32_16x16x32_bf16 v[88:91], a[116:119], v[168:171], v[88:91]\n"
"v_mfma_f32_16x16x32_bf16 v[88:91], a[80:83], v[172:175], v[88:91]\n"
"v_cvt_pk_bf16_f32 v109, v42, v43\n"
"v_mfma_f32_16x16x32_bf16 v[92:95], a[120:123], v[168:171], v[92:95]\n"
"ds_read_b128 v[168:171], v194 offset:34816\n"
"v_mfma_f32_16x16x32_bf16 v[92:95], a[84:87], v[172:175], v[92:95]\n"
"s_waitcnt lgkmcnt(2)\n"
"ds_read_b128 v[172:175], v194 offset:35840\n"
"v_mfma_f32_16x16x32_bf16 v[24:27], a[8:11], v[160:163], 0\n"
"s_waitcnt lgkmcnt(2)\n"
"v_mfma_f32_16x16x32_bf16 v[24:27], a[28:31], v[164:167], v[24:27]\n"
"v_mfma_f32_16x16x32_bf16 v[28:31], a[12:15], v[160:163], 0\n"
".Lpad11_%=:\n"
"s_cbranch_vccnz .Lpad12_%=\n"
"v_mfma_f32_16x16x32_bf16 v[28:31], a[40:43], v[164:167], v[28:31]\n"
"v_mfma_f32_16x16x32_bf16 v[40:43], a[24:27], v[160:163], 0\n"
"ds_read_b128 v[160:163], v194 offset:36864\n"
"v_mfma_f32_16x16x32_bf16 v[40:43], a[44:47], v[164:167], v[40:43]\n"
"s_waitcnt lgkmcnt(2)\n"
"ds_read_b128 v[164:167], v194 offset:37888\n"
"v_mfma_f32_16x16x32_bf16 v[24:27], a[48:51], v[168:171], v[24:27]\n"
"s_waitcnt lgkmcnt(2)\n"
"s_mov_b32 s47, 0x17000\n"
"v_mfma_f32_16x16x32_bf16 v[24:27], a[60:63], v[172:175], v[24:27]\n"
"v_cvt_pk_bf16_f32 v100, v76, v77\n"
"v_cvt_pk_bf16_f32 v101, v78, v79\n"
"v_mfma_f32_16x16x32_bf16 v[28:31], a[52:55], v[168:171], v[28:31]\n"
"v_cvt_pk_bf16_f32 v102, v88, v89\n"
"v_mfma_f32_16x16x32_bf16 v[28:31], a[64:67], v[172:175], v[28:31]\n"
"v_cvt_pk_bf16_f32 v103, v90, v91\n"
"v_mfma_f32_16x16x32_bf16 v[40:43], a[56:59], v[168:171], v[40:43]\n"
"ds_read_b128 v[168:171], v194 offset:38912\n"
"v_mfma_f32_16x16x32_bf16 v[40:43], a[68:71], v[172:175], v[40:43]\n"
"s_waitcnt lgkmcnt(2)\n"
"ds_read_b128 v[172:175], v194 offset:39936\n"
"v_cvt_pk_bf16_f32 v110, v92, v93\n"
"v_mfma_f32_16x16x32_bf16 v[24:27], a[72:75], v[160:163], v[24:27]\n"
"s_waitcnt lgkmcnt(2)\n"
"v_cvt_pk_bf16_f32 v111, v94, v95\n"
"v_mfma_f32_16x16x32_bf16 v[24:27], a[100:103], v[164:167], v[24:27]\n"
"buffer_store_dwordx4 v[100:103], v214, s[36:39], s47 offen sc1\n"
"v_mfma_f32_16x16x32_bf16 v[28:31], a[76:79], v[160:163], v[28:31]\n"
"v_mfma_f32_16x16x32_bf16 v[28:31], a[104:107], v[164:167], v[28:31]\n"
"v_mfma_f32_16x16x32_bf16 v[40:43], a[96:99], v[160:163], v[40:43]\n"
"ds_read_b128 v[160:163], v194 offset:40960\n"
"v_mfma_f32_16x16x32_bf16 v[40:43], a[108:111], v[164:167], v[40:43]\n"
"s_waitcnt lgkmcnt(2)\n"
"ds_read_b128 v[164:167], v194 offset:41984\n"
"v_mfma_f32_16x16x32_bf16 v[24:27], a[112:115], v[168:171], v[24:27]\n"
"s_waitcnt lgkmcnt(2)\n"
"s_mov_b32 s47, 0x1f000\n"
"v_mfma_f32_16x16x32_bf16 v[24:27], a[124:127], v[172:175], v[24:27]\n"
"buffer_store_dwordx4 v[108:111], v214, s[36:39], s47 offen sc1\n"
"v_mfma_f32_16x16x32_bf16 v[28:31], a[116:119], v[168:171], v[28:31]\n"
"v_mfma_f32_16x16x32_bf16 v[28:31], a[80:83], v[172:175], v[28:31]\n"
"v_mfma_f32_16x16x32_bf16 v[40:43], a[120:123], v[168:171], v[40:43]\n"
"ds_read_b128 v[168:171], v194 offset:43008\n"
"v_mfma_f32_16x16x32_bf16 v[40:43], a[84:87], v[172:175], v[40:43]\n"
"s_waitcnt lgkmcnt(2)\n"
"ds_read_b128 v[172:175], v194 offset:44032\n"
"v_mfma_f32_16x16x32_bf16 v[76:79], a[8:11], v[160:163], 0\n"
"s_waitcnt lgkmcnt(2)\n"
"v_mfma_f32_16x16x32_bf16 v[76:79], a[28:31], v[164:167], v[76:79]\n"
"v_mfma_f32_16x16x32_bf16 v[88:91], a[12:15], v[160:163], 0\n"
"v_mfma_f32_16x16x32_bf16 v[88:91], a[40:43], v[164:167], v[88:91]\n"
"v_mfma_f32_16x16x32_bf16 v[92:95], a[24:27], v[160:163], 0\n"
"ds_read_b128 v[160:163], v194 offset:45056\n"
"v_mfma_f32_16x16x32_bf16 v[92:95], a[44:47], v[164:167], v[92:95]\n"
"s_waitcnt lgkmcnt(2)\n"
"ds_read_b128 v[164:167], v194 offset:46080\n"
"v_mfma_f32_16x16x32_bf16 v[76:79], a[48:51], v[168:171], v[76:79]\n"
"s_waitcnt lgkmcnt(2)\n"
"v_mfma_f32_16x16x32_bf16 v[76:79], a[60:63], v[172:175], v[76:79]\n"
"s_mov_b32 s47, 0x18000\n"
"v_mfma_f32_16x16x32_bf16 v[88:91], a[52:55], v[168:171], v[88:91]\n"
"v_cvt_pk_bf16_f32 v104, v24, v25\n"
"v_mfma_f32_16x16x32_bf16 v[88:91], a[64:67], v[172:175], v[88:91]\n"
"v_cvt_pk_bf16_f32 v105, v26, v27\n"
"v_mfma_f32_16x16x32_bf16 v[92:95], a[56:59], v[168:171], v[92:95]\n"
"ds_read_b128 v[168:171], v194 offset:47104\n"
"v_mfma_f32_16x16x32_bf16 v[92:95], a[68:71], v[172:175], v[92:95]\n"
"s_waitcnt lgkmcnt(2)\n"
"ds_read_b128 v[172:175], v194 offset:48128\n"
"v_mfma_f32_16x16x32_bf16 v[76:79], a[72:75], v[160:163], v[76:79]\n"
"s_waitcnt lgkmcnt(2)\n"
"v_mfma_f32_16x16x32_bf16 v[76:79], a[100:103], v[164:167], v[76:79]\n"
"v_cvt_pk_bf16_f32 v106, v28, v29\n"
"v_mfma_f32_16x16x32_bf16 v[88:91], a[76:79], v[160:163], v[88:91]\n"
"v_cvt_pk_bf16_f32 v107, v30, v31\n"
"v_mfma_f32_16x16x32_bf16 v[88:91], a[104:107], v[164:167], v[88:91]\n"
"buffer_store_dwordx4 v[104:107], v214, s[36:39], s47 offen sc1\n"
"v_mfma_f32_16x16x32_bf16 v[92:95], a[96:99], v[160:163], v[92:95]\n"
"v_mfma_f32_16x16x32_bf16 v[92:95], a[108:111], v[164:167], v[92:95]\n"
"s_waitcnt lgkmcnt(1)\n"
"v_mfma_f32_16x16x32_bf16 v[76:79], a[112:115], v[168:171], v[76:79]\n"
"s_waitcnt lgkmcnt(0)\n"
"v_mfma_f32_16x16x32_bf16 v[76:79], a[124:127], v[172:175], v[76:79]\n"
"v_mfma_f32_16x16x32_bf16 v[88:91], a[116:119], v[168:171], v[88:91]\n"
"v_mfma_f32_16x16x32_bf16 v[88:91], a[80:83], v[172:175], v[88:91]\n"
"v_cvt_pk_bf16_f32 v108, v40, v41\n"
"v_cvt_pk_bf16_f32 v109, v42, v43\n"
"v_mfma_f32_16x16x32_bf16 v[92:95], a[120:123], v[168:171], v[92:95]\n"
"v_mfma_f32_16x16x32_bf16 v[92:95], a[84:87], v[172:175], v[92:95]\n"
"s_mov_b32 s47, 0x19000\n"
"s_nop 0\n"
"v_cvt_pk_bf16_f32 v96, v76, v77\n"
"v_cvt_pk_bf16_f32 v97, v78, v79\n"
"v_cvt_pk_bf16_f32 v98, v88, v89\n"
"v_cvt_pk_bf16_f32 v99, v90, v91\n"
"buffer_store_dwordx4 v[96:99], v214, s[36:39], s47 offen sc1\n"
"s_nop 0\n"
"v_cvt_pk_bf16_f32 v110, v92, v93\n"
"v_cvt_pk_bf16_f32 v111, v94, v95\n"
"s_mov_b32 s47, 0x20000\n"
"buffer_store_dwordx4 v[108:111], v214, s[36:39], s47 offen sc1\n"
".Lmha_end%=:\n"
    :
    : "s"(px), "s"(pk), "s"(pv), "s"(pq), "s"(po), "s"(pp), "s"(w), "s"(r), "s"(ps), "s"((unsigned)(size_t)lds), "s"(((r >> 2) + 2u * (r & 3u)) % 7u)
    : "memory", "vcc", "scc", "v0", "v1", "v2", "v3", "v4", "v5", "v6", "v7", "v8", "v9", "v10", "v11", "v12", "v13", "v14", "v15", "v16", "v17", "v18", "v19", "v20", "v21", "v22", "v23", "v24", "v25", "v26", "v27", "v28", "v29", "v30", "v31", "v32", "v33", "v34", "v35", "v36", "v37", "v38", "v39", "v40", "v41", "v42", "v43", "v44", "v45", "v46", "v47", "v48", "v49", "v50", "v51", "v52", "v53", "v54", "v55", "v56", "v57", "v58", "v59", "v60", "v61", "v62", "v63", "v64", "v65", "v66", "v67", "v68", "v69", "v70", "v71", "v72", "v73", "v74", "v75", "v76", "v77", "v78", "v79", "v80", "v81", "v82", "v83", "v84", "v85", "v86", "v87", "v88", "v89", "v90", "v91", "v92", "v93", "v94", "v95", "v96", "v97", "v98", "v99", "v100", "v101", "v102", "v103", "v104", "v105", "v106", "v107", "v108", "v109", "v110", "v111", "v112", "v113", "v114", "v115", "v116", "v117", "v118", "v119", "v120", "v121", "v122", "v123", "v124", "v125", "v126", "v127", "v128", "v129", "v130", "v131", "v132", "v133", "v134", "v135", "v136", "v137", "v138", "v139", "v140", "v141", "v142", "v143", "v144", "v145", "v146", "v147", "v148", "v149", "v150", "v151", "v152", "v153", "v154", "v155", "v156", "v157", "v158", "v159", "v160", "v161", "v162", "v163", "v164", "v165", "v166", "v167", "v168", "v169", "v170", "v171", "v172", "v173", "v174", "v175", "v176", "v177", "v178", "v179", "v180", "v181", "v182", "v183", "v184", "v185", "v186", "v187", "v188", "v189", "v190", "v191", "v192", "v193", "v194", "v195", "v196", "v197", "v198", "v199", "v200", "v201", "v202", "v203", "v204", "v205", "v206", "v207", "v208", "v209", "v210", "v211", "v212", "v213", "v214", "v215", "v216", "v217", "v218", "v219", "v220", "v221", "v222", "v223", "v224", "v225", "v226", "v227", "v228", "v229", "v230", "v231", "v232", "v233", "v234", "v235", "v236", "v237", "v238", "v239", "v240", "v241", "v242", "v243", "v244", "v245", "v246", "v247", "v248", "v249", "v250", "v251", "v252", "v253", "v254", "v255", "a0", "a1", "a2", "a3", "a4", "a5", "a6", "a7", "a8", "a9", "a10", "a11", "a12", "a13", "a14", "a15", "a16", "a17", "a18", "a19", "a20", "a21", "a22", "a23", "a24", "a25", "a26", "a27", "a28", "a29", "a30", "a31", "a32", "a33", "a34", "a35", "a36", "a37", "a38", "a39", "a40", "a41", "a42", "a43", "a44", "a45", "a46", "a47", "a48", "a49", "a50", "a51", "a52", "a53", "a54", "a55", "a56", "a57", "a58", "a59", "a60", "a61", "a62", "a63", "a64", "a65", "a66", "a67", "a68", "a69", "a70", "a71", "a72", "a73", "a74", "a75", "a76", "a77", "a78", "a79", "a80", "a81", "a82", "a83", "a84", "a85", "a86", "a87", "a88", "a89", "a90", "a91", "a92", "a93", "a94", "a95", "a96", "a97", "a98", "a99", "a100", "a101", "a102", "a103", "a104", "a105", "a106", "a107", "a108", "a109", "a110", "a111", "a112", "a113", "a114", "a115", "a116", "a117", "a118", "a119", "a120", "a121", "a122", "a123", "a124", "a125", "a126", "a127", "a128", "a129", "a130", "a131", "a132", "a133", "a134", "a135", "a136", "a137", "a138", "a139", "a140", "a141", "a142", "a143", "a144", "a145", "a146", "a147", "a148", "a149", "a150", "a151", "a152", "a153", "a154", "a155", "a156", "a157", "a158", "a159", "a160", "a161", "a162", "a163", "a164", "a165", "a166", "a167", "a168", "a169", "a170", "a171", "a172", "a173", "a174", "a175", "a176", "a177", "a178", "a179", "a180", "a181", "a182", "a183", "a184", "a185", "a186", "a187", "a188", "a189", "a190", "a191", "a192", "a193", "a194", "a195", "a196", "a197", "a198", "a199", "a200", "a201", "a202", "a203", "a204", "a205", "a206", "a207", "a208", "a209", "a210", "a211", "a212", "a213", "a214", "a215", "a216", "a217", "a218", "a219", "a220", "a221", "a222", "a223", "a224", "a225", "a226", "a227", "a228", "a229", "a230", "a231", "a232", "a233", "a234", "a235", "a236", "a237", "a238", "a239", "a240", "a241", "a242", "a243", "a244", "a245", "a246", "a247", "a248", "a249", "a250", "a251", "a252", "a253", "a254", "a255", "s16", "s17", "s18", "s19", "s20", "s21", "s22", "s23", "s24", "s25", "s26", "s27", "s28", "s29", "s30", "s31", "s32", "s33", "s34", "s35", "s36", "s37", "s38", "s39", "s40", "s41", "s42", "s43", "s44", "s45", "s46", "s47", "s48", "s49", "s50", "s51", "s52", "s53", "s54", "s55", "s56", "s57", "s58", "s59", "s60", "s61", "s62", "s63", "s64", "s65", "s66", "s67", "s68", "s69", "s70", "s71", "s72", "s73", "s74", "s75", "s76", "s77", "s78", "s79", "s80", "s81", "s82", "s83", "s84", "s85", "s86", "s87", "s88", "s89", "s90", "s91", "s92", "s93", "s94", "s95");
}

__device__ __forceinline__ unsigned pk2(float lo, float hi) { f32x2 v = {lo, hi}; return __builtin_bit_cast(unsigned, __builtin_convertvector(v, bf16x2)); }

__global__ __launch_bounds__(640) void mha_reduce(const char* __restrict__ ws, unsigned short* __restrict__ out) {
  const unsigned lane = threadIdx.x & 63u;
  const unsigned id = blockIdx.x * 10u + __builtin_amdgcn_readfirstlane(threadIdx.x >> 6), b = id / 140u, j = id % 140u;
  const u32x4* p = (const u32x4*)(ws + (size_t)b * 16u * 143360u + j * 1024u) + lane;
  u32x4 t[16];
#pragma unroll
  for (int h = 0; h < 16; ++h) t[h] = p[h * (143360 / 16)];
  float s[8] = {};
#pragma unroll
  for (int h = 0; h < 16; ++h)
#pragma unroll
    for (int e = 0; e < 4; ++e) { s[2 * e] += __uint_as_float(t[h][e] << 16); s[2 * e + 1] += __uint_as_float(t[h][e] & 0xffff0000u); }
  u32x4 r = {pk2(s[0], s[1]), pk2(s[2], s[3]), pk2(s[4], s[5]), pk2(s[6], s[7])};
  unsigned short* ob = out + (size_t)b * 160u * 448u;
  if (j < 80u) {
    const unsigned q = 16u * (j >> 3) + (lane & 15u), n = 32u * (j & 7u) + 8u * (lane >> 4);
    *(u32x4*)(ob + q * 448u + n) = r;
  } else {
    const unsigned w = j & 3u, c = lane & 15u, g = lane >> 4, win = 256u + 32u * (w & 1u) + 96u * (w >> 1);
    if (j < 120u) {
      *(u32x4*)(ob + (16u * ((j - 80u) >> 2) + c) * 448u + win + 32u * (w & 1u) + 8u * g) = r;
    } else {
      const unsigned q = 32u * ((j - 120u) >> 2) + c, n = win + 32u * (1u - (w & 1u)) + 16u * (w & 1u) + 4u * g;
      *(u32x2*)(ob + q * 448u + n) = u32x2{r[0], r[1]};
      *(u32x2*)(ob + (q + 16u) * 448u + n) = u32x2{r[2], r[3]};
    }
  }
}

extern "C" void kernel_launch(void* const* d_in, const int* in_sizes, int n_in, void* d_out, int out_size, void* d_ws, size_t ws_size,
                              hipStream_t stream) {
  mha_fused<<<256, 256, 0, stream>>>((const char*)d_in[0], (const char*)d_in[1], (const char*)d_in[2], (const char*)d_in[3], (const char*)d_in[4], (char*)d_ws);
  mha_reduce<<<224, 640, 0, stream>>>((const char*)d_ws, (unsigned short*)d_out);
}
```
